```python
import torch

def _softmax(x, axis=-1):
    return torch.softmax(x, dim=axis)
def _gelu(x):
    return torch.nn.functional.gelu(x, approximate="tanh")
def _silu(x):
    return torch.nn.functional.silu(x)
def _sigmoid(x):
    return torch.sigmoid(x)
def _layernorm(x, axis=-1, eps=1e-5):
    m = x.mean(dim=axis, keepdim=True)
    v = x.var(dim=axis, keepdim=True, unbiased=False)
    return (x - m) / torch.sqrt(v + eps)
def _scatter_add(tgt, idx, src, axis=0):
    return tgt.clone().index_add_(axis, idx, src)
def _segment_sum(src, seg, n):
    out = torch.zeros((n,) + src.shape[1:], dtype=src.dtype)
    return out.index_add_(0, seg, src)
def _pad(x, pw):
    flat = tuple(v for pair in reversed(pw) for v in pair)
    return torch.nn.functional.pad(x, flat)

kernel_name = 'standalone__gqa_softmax__near_ridge'

def setup_inputs():

    _w = (lambda a: (lambda t: t.to(torch.bfloat16) if t.is_floating_point() else t)(torch.as_tensor(a)))
    B, Hq, Hkv, Sq, Sk, D = 32, 16, 8, 256, 512, 256
    return {
        "q": _w((lambda *s: torch.randn(*s, dtype=torch.bfloat16) * 0.8)(B, Hq, Sq, D)),
        "k": _w((lambda *s: torch.randn(*s, dtype=torch.bfloat16) * 0.8)(B, Hkv, Sk, D)),
        "v": _w((lambda *s: torch.randn(*s, dtype=torch.bfloat16) * 0.05)(B, Hkv, Sk, D)),
    }

def reference(q, k, v):
    B, Hq, Sq, D = q.shape
    Hkv = k.shape[1]
    rep = Hq // Hkv
    k = (lambda x, n, axis: torch.repeat_interleave(x, n, dim=axis))(k, rep, axis=1)
    v = (lambda x, n, axis: torch.repeat_interleave(x, n, dim=axis))(v, rep, axis=1)
    scale = D ** -0.5
    scores = torch.einsum("bhqd,bhkd->bhqk", q, k) * scale
    p = _softmax(scores, axis=-1)
    return torch.einsum("bhqk,bhkd->bhqd", p, v)
```

```cpp
#include <hip/hip_runtime.h>
#include <hip/hip_bf16.h>
constexpr int B=32,HQ=16,HKV=8,SQ=256,SK=512,D=256;
__device__ __forceinline__ float bf2f(unsigned short x){ return __uint_as_float(((unsigned)x)<<16); }
__global__ __launch_bounds__(256) void naive_attn(const unsigned short* __restrict__ q,const unsigned short* __restrict__ k,const unsigned short* __restrict__ v,__hip_bfloat16* __restrict__ o){
  __shared__ float qs[8][D];
  __shared__ float s[8][SK];
  __shared__ float red[8];
  int blk=blockIdx.x; int qt=blk%(SQ/8); int bh=blk/(SQ/8); int h=bh%HQ,b=bh/HQ; int kvh=h/(HQ/HKV);
  const unsigned short* qp=q+((size_t)bh*SQ+qt*8)*D;
  const unsigned short* kp=k+((size_t)(b*HKV+kvh))*SK*D;
  const unsigned short* vp=v+((size_t)(b*HKV+kvh))*SK*D;
  int tid=threadIdx.x;
  for(int i=tid;i<8*D;i+=256) qs[i/D][i%D]=bf2f(qp[i]);
  __syncthreads();
  for(int key=tid;key<SK;key+=256){
    float acc[8]={0,0,0,0,0,0,0,0};
    const uint4* kr=(const uint4*)(kp+(size_t)key*D);
    for(int c=0;c<D/8;c++){ uint4 w=kr[c]; unsigned ww[4]={w.x,w.y,w.z,w.w};
      #pragma unroll
      for(int j=0;j<4;j++){ float a=__uint_as_float(ww[j]<<16), bb=__uint_as_float(ww[j]&0xffff0000u);
        #pragma unroll
        for(int r=0;r<8;r++){ acc[r]+=a*qs[r][c*8+2*j]+bb*qs[r][c*8+2*j+1]; } } }
    #pragma unroll
    for(int r=0;r<8;r++) s[r][key]=acc[r]*0.0625f;
  }
  __syncthreads();
  int w=tid>>6,l=tid&63;
  for(int r=w;r<8;r+=4){
    float m=-1e30f; for(int i=l;i<SK;i+=64) m=fmaxf(m,s[r][i]);
    for(int o2=32;o2>0;o2>>=1) m=fmaxf(m,__shfl_xor(m,o2));
    float sum=0; for(int i=l;i<SK;i+=64){ float e=expf(s[r][i]-m); s[r][i]=e; sum+=e; }
    for(int o2=32;o2>0;o2>>=1) sum+=__shfl_xor(sum,o2);
    if(l==0) red[r]=1.f/sum;
  }
  __syncthreads();
  float acc[8]={0,0,0,0,0,0,0,0};
  for(int key=0;key<SK;key++){ float vv=bf2f(vp[(size_t)key*D+tid]);
    #pragma unroll
    for(int r=0;r<8;r++) acc[r]+=s[r][key]*vv; }
  #pragma unroll
  for(int r=0;r<8;r++) o[((size_t)bh*SQ+qt*8+r)*D+tid]=__float2bfloat16(acc[r]*red[r]);
}
extern "C" void kernel_launch(void* const* d_in, const int* in_sizes, int n_in, void* d_out, int out_size, void* d_ws, size_t ws_size, hipStream_t stream) {
  naive_attn<<<B*HQ*SQ/8,256,0,stream>>>((const unsigned short*)d_in[0],(const unsigned short*)d_in[1],(const unsigned short*)d_in[2],(__hip_bfloat16*)d_out);
}
```

```cpp
#include <hip/hip_runtime.h>
#include <hip/hip_bf16.h>
#include <stdint.h>

__global__ __launch_bounds__(256, 1) void gqa_attn_kernel(const void* q, const void* k, const void* v, void* o, void* ws) {
  __shared__ __attribute__((aligned(1024))) char lds[163840];
  unsigned tid = threadIdx.x;
  const uint64_t ka = (uint64_t)__builtin_amdgcn_kernarg_segment_ptr();
  asm volatile(
    "s_load_dwordx8 s[4:11], %1, 0x0\n"
    "v_lshrrev_b32 v32, 6, v127\n"
    "v_and_b32 v38, 63, v127\n"
    "v_readfirstlane_b32 s41, v32\n"
    "s_getpc_b64 s[88:89]\n"
    "L_pc:\n"
    "v_lshlrev_b32 v33, 7, v38\n"
    "s_lshr_b32 s43, %2, 3\n"
    "s_and_b32 s89, s89, 0xffff\n"
    "s_mov_b32 s90, L_end-L_pc\n"
    "s_mov_b32 s91, 0x20000\n"
    "s_and_b32 s43, s43, 1\n"
    "s_mul_i32 s43, s43, 3\n"
    "s_cmp_eq_u32 s41, 0\n"
    "s_cselect_b32 s43, 0, s43\n"
    "s_add_u32 s43, s43, s41\n"
    "s_lshl_b32 s43, s43, 13\n"
    "buffer_load_dword v112, v33, s[88:91], s43 offen offset:0\n"
    "s_and_b32 s43, %2, 7\n"
    "s_lshr_b32 s44, %2, 3\n"
    "s_and_b32 s45, s44, 1\n"
    "s_lshr_b32 s44, s44, 1\n"
    "s_lshl_b32 s43, s43, 4\n"
    "s_add_u32 s43, s43, s44\n"
    "s_lshl_b32 s46, s41, 12\n"
    "s_lshl_b32 s47, s43, 19\n"
    "s_add_u32 s47, s47, s46\n"
    "s_sub_u32 s48, 524288, s46\n"
    "v_bfe_u32 v32, v38, 2, 3\n"
    "v_lshrrev_b32 v33, 5, v38\n"
    "v_and_b32 v34, 3, v38\n"
    "v_lshlrev_b32 v100, 9, v32\n"
    "v_lshl_add_u32 v100, v33, 6, v100\n"
    "v_lshl_add_u32 v100, v34, 4, v100\n"
    "s_and_b32 s78, s41, 1\n"
    "s_lshl_b32 s78, s78, 5\n"
    "v_xor_b32 v99, s78, v100\n"
    "s_add_u32 s38, %3, s46\n"
    "s_mov_b32 s87, 0\n"
    "s_lshl_b32 s85, %2, 2\n"
    "s_or_b32 s85, s85, s41\n"
    "s_bfe_u32 s78, s85, 0x10001\n"
    "s_lshl_b32 s78, s78, 13\n"
    "s_xor_b32 s87, s87, s78\n"
    "s_waitcnt lgkmcnt(0)\n"
    "s_add_u32 s16, s6, s47\n"
    "s_addc_u32 s17, s7, 0\n"
    "s_and_b32 s17, s17, 0xffff\n"
    "s_mov_b32 s18, s48\n"
    "s_mov_b32 s19, 0x20000\n"
    "s_add_u32 m0, s38, 0\n"
    "s_mov_b32 s76, 0\n"
    "buffer_load_dwordx4 v99, s[16:19], s76 offen offset:0 nt lds\n"
    "s_add_u32 m0, s38, 896\n"
    "s_nop 0\n"
    "buffer_load_dwordx4 v99, s[16:19], s76 offen offset:128 nt lds\n"
    "s_add_u32 m0, s38, 1792\n"
    "s_nop 0\n"
    "buffer_load_dwordx4 v99, s[16:19], s76 offen offset:256 nt lds\n"
    "s_add_u32 m0, s38, 2688\n"
    "s_nop 0\n"
    "buffer_load_dwordx4 v99, s[16:19], s76 offen offset:384 nt lds\n"
    "s_add_u32 m0, s38, 16384\n"
    "s_mov_b32 s76, 16384\n"
    "buffer_load_dwordx4 v99, s[16:19], s76 offen offset:0 nt lds\n"
    "s_add_u32 m0, s38, 17280\n"
    "s_nop 0\n"
    "buffer_load_dwordx4 v99, s[16:19], s76 offen offset:128 nt lds\n"
    "s_add_u32 m0, s38, 18176\n"
    "s_nop 0\n"
    "buffer_load_dwordx4 v99, s[16:19], s76 offen offset:256 nt lds\n"
    "s_add_u32 m0, s38, 19072\n"
    "s_nop 0\n"
    "buffer_load_dwordx4 v99, s[16:19], s76 offen offset:384 nt lds\n"
    "s_add_u32 s20, s8, s47\n"
    "s_addc_u32 s21, s9, 0\n"
    "s_sub_u32 s20, s20, 32768\n"
    "s_subb_u32 s21, s21, 0\n"
    "s_and_b32 s21, s21, 0xffff\n"
    "s_add_u32 s22, s48, 32768\n"
    "s_mov_b32 s23, 0x20000\n"
    "s_lshl_b32 s47, s43, 2\n"
    "s_add_u32 s47, s47, s45\n"
    "s_lshl_b32 s47, s47, 17\n"
    "s_lshl_b32 s48, s41, 15\n"
    "s_add_u32 s47, s47, s48\n"
    "s_add_u32 s24, s4, s47\n"
    "s_addc_u32 s25, s5, 0\n"
    "s_and_b32 s25, s25, 0xffff\n"
    "s_mov_b32 s26, 32768\n"
    "s_mov_b32 s27, 0x20000\n"
    "v_and_b32 v35, 31, v38\n"
    "v_lshlrev_b32 v36, 2, v33\n"
    "v_xor_b32 v35, v35, v36\n"
    "v_lshlrev_b32 v110, 4, v35\n"
    "v_lshl_add_u32 v110, v33, 9, v110\n"
    "v_xor_b32 v111, 128, v110\n"
    "s_lshl_b32 s48, s41, 14\n"
    "s_add_u32 s70, %3, s48\n"
    "s_add_u32 s70, s70, 98304\n"
    "s_lshl_b32 s48, s41, 13\n"
    "s_add_u32 s80, %3, s48\n"
    "s_add_u32 s80, s80, 49152\n"
    "s_xor_b32 s78, s87, 0\n"
    "s_add_u32 m0, s70, 8192\n"
    "s_nop 0\n"
    "buffer_load_dwordx4 v110, s[24:27], s78 offen offset:0 nt lds\n"
    "buffer_load_dwordx4 v111, s[24:27], s78 offen offset:1024 nt lds\n"
    "buffer_load_dwordx4 v110, s[24:27], s78 offen offset:2048 nt lds\n"
    "buffer_load_dwordx4 v111, s[24:27], s78 offen offset:3072 nt lds\n"
    "s_xor_b32 s78, s87, 4096\n"
    "s_add_u32 m0, s70, 12288\n"
    "s_nop 0\n"
    "buffer_load_dwordx4 v110, s[24:27], s78 offen offset:0 nt lds\n"
    "buffer_load_dwordx4 v111, s[24:27], s78 offen offset:1024 nt lds\n"
    "buffer_load_dwordx4 v110, s[24:27], s78 offen offset:2048 nt lds\n"
    "buffer_load_dwordx4 v111, s[24:27], s78 offen offset:3072 nt lds\n"
    "s_add_u32 s28, s10, s47\n"
    "s_addc_u32 s29, s11, 0\n"
    "s_and_b32 s29, s29, 0xffff\n"
    "s_mov_b32 s30, 294912\n"
    "s_mov_b32 s31, 0x20000\n"
    "s_add_u32 s47, s47, 262144\n"
    "s_add_u32 s64, s4, s47\n"
    "s_addc_u32 s65, s5, 0\n"
    "s_and_b32 s65, s65, 0xffff\n"
    "s_mov_b32 s66, 32768\n"
    "s_mov_b32 s67, 0x20000\n"
    "s_mov_b32 s57, 16384\n"
    "s_add_u32 s51, s38, 49152\n"
    "s_mov_b32 s39, 0x3db8aa3b\n"
    "s_mov_b32 s40, 0x41000000\n"
    "s_mov_b32 s68, 53248\n"
    "s_mov_b32 s69, 57344\n"
    "s_xor_b32 s92, s87, 0\n"
    "s_xor_b32 s93, s87, 4096\n"
    "s_xor_b32 s94, s87, 8192\n"
    "s_xor_b32 s95, s87, 12288\n"
    "s_xor_b32 s96, s87, 16384\n"
    "s_xor_b32 s97, s87, 20480\n"
    "s_xor_b32 s98, s87, 24576\n"
    "s_xor_b32 s99, s87, 28672\n"
    "v_lshrrev_b32 v35, 2, v32\n"
    "v_lshlrev_b32 v36, 5, v35\n"
    "v_xor_b32 v100, v100, v36\n"
    "s_mov_b32 s48, %3\n"
    "v_bfe_u32 v34, v38, 3, 1\n"
    "v_lshlrev_b32 v96, 12, v34\n"
    "v_and_b32 v35, 7, v38\n"
    "v_lshl_add_u32 v96, v35, 6, v96\n"
    "v_lshrrev_b32 v35, 4, v38\n"
    "v_lshlrev_b32 v34, 1, v34\n"
    "v_xor_b32 v34, v34, v35\n"
    "v_lshl_add_u32 v96, v34, 4, v96\n"
    "v_add_u32 v96, s48, v96\n"
    "v_and_b32 v32, 15, v38\n"
    "v_lshlrev_b32 v123, 9, v32\n"
    "v_lshl_add_u32 v123, v35, 4, v123\n"
    "v_and_b32 v32, 3, v38\n"
    "v_lshl_add_u32 v123, v32, 6, v123\n"
    "s_add_u32 s48, s48, 49152\n"
    "v_bfe_u32 v34, v38, 2, 2\n"
    "v_and_b32 v36, 3, v38\n"
    "v_bfe_u32 v35, v38, 4, 1\n"
    "v_lshl_add_u32 v34, v35, 2, v34\n"
    "v_lshlrev_b32 v97, 6, v34\n"
    "v_lshl_add_u32 v97, v33, 12, v97\n"
    "v_lshl_add_u32 v97, v36, 3, v97\n"
    "v_lshl_add_u32 v97, v35, 5, v97\n"
    "v_add_u32 v97, s48, v97\n"
    "v_xor_b32 v98, 32, v97\n"
    "v_and_b32 v34, 1, v38\n"
    "v_xor_b32 v34, v33, v34\n"
    "v_lshlrev_b32 v113, 4, v34\n"
    "v_lshrrev_b32 v34, 3, v38\n"
    "v_lshl_add_u32 v113, v34, 5, v113\n"
    "v_bfe_u32 v34, v38, 1, 1\n"
    "v_lshlrev_b32 v34, 7, v34\n"
    "v_xor_b32 v113, v113, v34\n"
    "v_bfe_u32 v34, v38, 1, 2\n"
    "v_lshl_add_u32 v113, v34, 9, v113\n"
    "v_add_u32 v113, s70, v113\n"
    "s_mov_b32 s81, 262144\n"
    "s_mov_b32 s4, s24\n"
    "s_mov_b32 s5, s25\n"
    "s_mov_b32 s6, 294912\n"
    "s_mov_b32 s7, 0x20000\n"
    "s_sub_u32 s8, s16, s46\n"
    "s_subb_u32 s9, s17, 0\n"
    "s_mov_b32 s10, 524288\n"
    "s_mov_b32 s11, 0x20000\n"
    "v_add_u32 v118, s70, v123\n"
    "v_xor_b32 v119, 64, v118\n"
    "v_xor_b32 v120, 128, v118\n"
    "v_xor_b32 v121, 192, v118\n"
    "v_add_u32 v114, s80, v123\n"
    "v_xor_b32 v115, 64, v114\n"
    "v_xor_b32 v116, 128, v114\n"
    "v_xor_b32 v117, 192, v114\n"
    "; P0\n"
    "s_waitcnt vmcnt(8)\n"
    "s_barrier\n"
    "ds_read_b128 v[140:143], v96 offset:0\n"
    "ds_read_b128 v[156:159], v96 offset:512\n"
    "ds_read_b128 v[172:175], v96 offset:8192\n"
    "ds_read_b128 v[188:191], v96 offset:8704\n"
    "s_xor_b32 s78, s87, 8192\n"
    "s_add_u32 m0, s70, 0\n"
    "s_nop 0\n"
    "buffer_load_dwordx4 v110, s[24:27], s78 offen offset:0 nt lds\n"
    "ds_read_b128 v[204:207], v96 offset:1024\n"
    "ds_read_b128 v[220:223], v96 offset:1536\n"
    "ds_read_b128 v[236:239], v96 offset:9216\n"
    "ds_read_b128 v[252:255], v96 offset:9728\n"
    "s_add_u32 m0, s70, 0\n"
    "s_nop 0\n"
    "buffer_load_dwordx4 v111, s[24:27], s78 offen offset:1024 nt lds\n"
    "ds_read_b128 a[140:143], v96 offset:2048\n"
    "ds_read_b128 a[156:159], v96 offset:2560\n"
    "ds_read_b128 a[172:175], v96 offset:10240\n"
    "ds_read_b128 a[188:191], v96 offset:10752\n"
    "s_add_u32 m0, s70, 0\n"
    "s_nop 0\n"
    "buffer_load_dwordx4 v110, s[24:27], s78 offen offset:2048 nt lds\n"
    "ds_read_b128 a[204:207], v96 offset:3072\n"
    "ds_read_b128 v[76:79], v96 offset:3584\n"
    "ds_read_b128 a[236:239], v96 offset:11264\n"
    "ds_read_b128 v[92:95], v96 offset:11776\n"
    "s_add_u32 m0, s70, 0\n"
    "s_nop 0\n"
    "buffer_load_dwordx4 v111, s[24:27], s78 offen offset:3072 nt lds\n"
    "ds_read_b128 v[136:139], v96 offset:16384\n"
    "ds_read_b128 v[152:155], v96 offset:16896\n"
    "ds_read_b128 v[168:171], v96 offset:24576\n"
    "ds_read_b128 v[184:187], v96 offset:25088\n"
    "s_xor_b32 s78, s87, 12288\n"
    "s_add_u32 m0, s70, 4096\n"
    "s_nop 0\n"
    "buffer_load_dwordx4 v110, s[24:27], s78 offen offset:0 nt lds\n"
    "ds_read_b128 v[200:203], v96 offset:17408\n"
    "ds_read_b128 v[216:219], v96 offset:17920\n"
    "ds_read_b128 v[232:235], v96 offset:25600\n"
    "ds_read_b128 v[248:251], v96 offset:26112\n"
    "s_add_u32 m0, s70, 4096\n"
    "s_nop 0\n"
    "buffer_load_dwordx4 v111, s[24:27], s78 offen offset:1024 nt lds\n"
    "ds_read_b128 a[136:139], v96 offset:18432\n"
    "ds_read_b128 a[152:155], v96 offset:18944\n"
    "ds_read_b128 a[168:171], v96 offset:26624\n"
    "ds_read_b128 a[184:187], v96 offset:27136\n"
    "s_add_u32 m0, s70, 4096\n"
    "s_nop 0\n"
    "buffer_load_dwordx4 v110, s[24:27], s78 offen offset:2048 nt lds\n"
    "ds_read_b128 a[200:203], v96 offset:19456\n"
    "ds_read_b128 v[72:75], v96 offset:19968\n"
    "ds_read_b128 a[232:235], v96 offset:27648\n"
    "ds_read_b128 v[88:91], v96 offset:28160\n"
    "s_add_u32 m0, s70, 4096\n"
    "s_nop 0\n"
    "buffer_load_dwordx4 v111, s[24:27], s78 offen offset:3072 nt lds\n"
    "s_xor_b32 s78, s87, 16384\n"
    "s_add_u32 m0, s80, 0\n"
    "s_nop 0\n"
    "buffer_load_dwordx4 v110, s[24:27], s78 offen offset:0 nt lds\n"
    "s_add_u32 m0, s80, 0\n"
    "s_nop 0\n"
    "buffer_load_dwordx4 v111, s[24:27], s78 offen offset:1024 nt lds\n"
    "s_add_u32 m0, s80, 0\n"
    "s_nop 0\n"
    "buffer_load_dwordx4 v110, s[24:27], s78 offen offset:2048 nt lds\n"
    "s_add_u32 m0, s80, 0\n"
    "s_nop 0\n"
    "buffer_load_dwordx4 v111, s[24:27], s78 offen offset:3072 nt lds\n"
    "s_waitcnt vmcnt(12)\n"
    "ds_read_b128 a[0:3], v118 offset:8192\n"
    "ds_read_b128 a[4:7], v119 offset:8192\n"
    "ds_read_b128 a[8:11], v120 offset:8192\n"
    "ds_read_b128 a[12:15], v121 offset:8192\n"
    "ds_read_b128 a[16:19], v118 offset:8448\n"
    "ds_read_b128 a[20:23], v119 offset:8448\n"
    "ds_read_b128 a[24:27], v120 offset:8448\n"
    "ds_read_b128 a[28:31], v121 offset:8448\n"
    "s_waitcnt lgkmcnt(7)\n"
    "v_mfma_f32_16x16x32_bf16 v[0:3], v[140:143], a[0:3], 0\n"
    "s_waitcnt lgkmcnt(6)\n"
    "v_mfma_f32_16x16x32_bf16 v[0:3], v[156:159], a[4:7], v[0:3]\n"
    "v_mfma_f32_16x16x32_bf16 v[4:7], v[172:175], a[0:3], 0\n"
    "s_xor_b32 s78, s87, 20480\n"
    "s_add_u32 m0, s80, 4096\n"
    "v_mfma_f32_16x16x32_bf16 v[4:7], v[188:191], a[4:7], v[4:7]\n"
    "buffer_load_dwordx4 v110, s[24:27], s78 offen offset:0 nt lds\n"
    "s_waitcnt lgkmcnt(5)\n"
    "v_mfma_f32_16x16x32_bf16 v[0:3], v[204:207], a[8:11], v[0:3]\n"
    "s_waitcnt lgkmcnt(4)\n"
    "v_mfma_f32_16x16x32_bf16 v[0:3], v[220:223], a[12:15], v[0:3]\n"
    "v_mfma_f32_16x16x32_bf16 v[4:7], v[236:239], a[8:11], v[4:7]\n"
    "v_mfma_f32_16x16x32_bf16 v[4:7], v[252:255], a[12:15], v[4:7]\n"
    "s_waitcnt lgkmcnt(3)\n"
    "v_mfma_f32_16x16x32_bf16 v[0:3], a[140:143], a[16:19], v[0:3]\n"
    "s_waitcnt lgkmcnt(2)\n"
    "v_mfma_f32_16x16x32_bf16 v[0:3], a[156:159], a[20:23], v[0:3]\n"
    "v_mfma_f32_16x16x32_bf16 v[4:7], a[172:175], a[16:19], v[4:7]\n"
    "s_add_u32 m0, s80, 4096\n"
    "v_mfma_f32_16x16x32_bf16 v[4:7], a[188:191], a[20:23], v[4:7]\n"
    "buffer_load_dwordx4 v111, s[24:27], s78 offen offset:1024 nt lds\n"
    "s_waitcnt lgkmcnt(1)\n"
    "v_mfma_f32_16x16x32_bf16 v[0:3], a[204:207], a[24:27], v[0:3]\n"
    "s_waitcnt lgkmcnt(0)\n"
    "v_mfma_f32_16x16x32_bf16 v[0:3], v[76:79], a[28:31], v[0:3]\n"
    "v_mfma_f32_16x16x32_bf16 v[4:7], a[236:239], a[24:27], v[4:7]\n"
    "v_mfma_f32_16x16x32_bf16 v[4:7], v[92:95], a[28:31], v[4:7]\n"
    "v_mfma_f32_16x16x32_bf16 v[32:35], v[136:139], a[0:3], 0\n"
    "v_mfma_f32_16x16x32_bf16 v[32:35], v[152:155], a[4:7], v[32:35]\n"
    "v_mfma_f32_16x16x32_bf16 v[36:39], v[168:171], a[0:3], 0\n"
    "s_add_u32 m0, s80, 4096\n"
    "v_mfma_f32_16x16x32_bf16 v[36:39], v[184:187], a[4:7], v[36:39]\n"
    "buffer_load_dwordx4 v110, s[24:27], s78 offen offset:2048 nt lds\n"
    "v_max3_f32 v101, v0, v1, v2\n"
    "v_mfma_f32_16x16x32_bf16 v[32:35], v[200:203], a[8:11], v[32:35]\n"
    "v_max3_f32 v101, v101, v3, v4\n"
    "v_max3_f32 v101, v101, v5, v6\n"
    "v_mfma_f32_16x16x32_bf16 v[32:35], v[216:219], a[12:15], v[32:35]\n"
    "v_max_f32 v101, v101, v7\n"
    "v_mov_b32 v125, v101\n"
    "v_mfma_f32_16x16x32_bf16 v[36:39], v[232:235], a[8:11], v[36:39]\n"
    "s_nop 0\n"
    "v_permlane32_swap_b32 v101, v125\n"
    "v_max_f32 v101, v101, v125\n"
    "v_mfma_f32_16x16x32_bf16 v[36:39], v[248:251], a[12:15], v[36:39]\n"
    "v_mov_b32 v125, v101\n"
    "v_mov_b32 v105, 0\n"
    "v_mfma_f32_16x16x32_bf16 v[32:35], a[136:139], a[16:19], v[32:35]\n"
    "v_permlane16_swap_b32 v101, v125\n"
    "v_max_f32 v101, v101, v125\n"
    "v_mfma_f32_16x16x32_bf16 v[32:35], a[152:155], a[20:23], v[32:35]\n"
    "v_mul_f32 v101, s39, v101\n"
    "v_add_f32 v101, s40, v101\n"
    "v_mfma_f32_16x16x32_bf16 v[36:39], a[168:171], a[16:19], v[36:39]\n"
    "s_add_u32 m0, s80, 4096\n"
    "v_fma_f32 v0, v0, s39, -v101\n"
    "v_mfma_f32_16x16x32_bf16 v[36:39], a[184:187], a[20:23], v[36:39]\n"
    "buffer_load_dwordx4 v111, s[24:27], s78 offen offset:3072 nt lds\n"
    "v_fma_f32 v1, v1, s39, -v101\n"
    "v_mfma_f32_16x16x32_bf16 v[32:35], a[200:203], a[24:27], v[32:35]\n"
    "v_fma_f32 v2, v2, s39, -v101\n"
    "v_fma_f32 v3, v3, s39, -v101\n"
    "v_mfma_f32_16x16x32_bf16 v[32:35], v[72:75], a[28:31], v[32:35]\n"
    "v_fma_f32 v4, v4, s39, -v101\n"
    "v_fma_f32 v5, v5, s39, -v101\n"
    "v_mfma_f32_16x16x32_bf16 v[36:39], a[232:235], a[24:27], v[36:39]\n"
    "v_fma_f32 v6, v6, s39, -v101\n"
    "v_fma_f32 v7, v7, s39, -v101\n"
    "v_mfma_f32_16x16x32_bf16 v[36:39], v[88:91], a[28:31], v[36:39]\n"
    "v_exp_f32 v0, v0\n"
    "v_exp_f32 v1, v1\n"
    "v_exp_f32 v2, v2\n"
    "v_exp_f32 v3, v3\n"
    "v_add_f32 v105, v105, v0\n"
    "v_add_f32 v105, v105, v1\n"
    "v_cvt_pk_bf16_f32 v0, v0, v1\n"
    "v_exp_f32 v4, v4\n"
    "v_exp_f32 v5, v5\n"
    "v_add_f32 v105, v105, v2\n"
    "v_add_f32 v105, v105, v3\n"
    "v_cvt_pk_bf16_f32 v1, v2, v3\n"
    "v_exp_f32 v6, v6\n"
    "v_exp_f32 v7, v7\n"
    "v_add_f32 v105, v105, v4\n"
    "v_add_f32 v105, v105, v5\n"
    "v_cvt_pk_bf16_f32 v2, v4, v5\n"
    "v_add_f32 v105, v105, v6\n"
    "v_add_f32 v105, v105, v7\n"
    "v_cvt_pk_bf16_f32 v3, v6, v7\n"
    "v_fma_f32 v32, v32, s39, -v101\n"
    "v_fma_f32 v33, v33, s39, -v101\n"
    "v_fma_f32 v34, v34, s39, -v101\n"
    "v_fma_f32 v35, v35, s39, -v101\n"
    "v_exp_f32 v32, v32\n"
    "v_exp_f32 v33, v33\n"
    "v_exp_f32 v34, v34\n"
    "v_exp_f32 v35, v35\n"
    "v_add_f32 v109, v32, v33\n"
    "v_cvt_pk_bf16_f32 v32, v32, v33\n"
    "v_add_f32 v109, v109, v34\n"
    "v_add_f32 v109, v109, v35\n"
    "v_cvt_pk_bf16_f32 v33, v34, v35\n"
    "v_fma_f32 v36, v36, s39, -v101\n"
    "v_fma_f32 v37, v37, s39, -v101\n"
    "v_fma_f32 v38, v38, s39, -v101\n"
    "v_fma_f32 v39, v39, s39, -v101\n"
    "v_exp_f32 v36, v36\n"
    "v_exp_f32 v37, v37\n"
    "v_exp_f32 v38, v38\n"
    "v_cvt_pk_bf16_f32 v34, v36, v37\n"
    "v_add_f32 v36, v109, v36\n"
    "v_exp_f32 v39, v39\n"
    "v_add_f32 v36, v36, v37\n"
    "v_add_f32 v36, v36, v38\n"
    "v_cvt_pk_bf16_f32 v35, v38, v39\n"
    "v_add_f32 v36, v36, v39\n"
    "s_xor_b32 s78, s87, 24576\n"
    "s_add_u32 m0, s70, 8192\n"
    "s_nop 0\n"
    "buffer_load_dwordx4 v110, s[24:27], s78 offen offset:0 nt lds\n"
    "s_add_u32 m0, s70, 8192\n"
    "s_nop 0\n"
    "buffer_load_dwordx4 v111, s[24:27], s78 offen offset:1024 nt lds\n"
    "s_add_u32 m0, s70, 8192\n"
    "s_nop 0\n"
    "buffer_load_dwordx4 v110, s[24:27], s78 offen offset:2048 nt lds\n"
    "s_add_u32 m0, s70, 8192\n"
    "s_nop 0\n"
    "buffer_load_dwordx4 v111, s[24:27], s78 offen offset:3072 nt lds\n"
    "s_waitcnt vmcnt(12)\n"
    "ds_read_b128 a[32:35], v118 offset:0\n"
    "ds_read_b128 a[36:39], v119 offset:0\n"
    "ds_read_b128 a[40:43], v120 offset:0\n"
    "ds_read_b128 a[44:47], v121 offset:0\n"
    "ds_read_b128 a[48:51], v118 offset:256\n"
    "ds_read_b128 a[52:55], v119 offset:256\n"
    "ds_read_b128 a[56:59], v120 offset:256\n"
    "ds_read_b128 a[60:63], v121 offset:256\n"
    "s_waitcnt lgkmcnt(7)\n"
    "v_mfma_f32_16x16x32_bf16 v[8:11], v[140:143], a[32:35], 0\n"
    "s_waitcnt lgkmcnt(6)\n"
    "v_mfma_f32_16x16x32_bf16 v[8:11], v[156:159], a[36:39], v[8:11]\n"
    "v_mfma_f32_16x16x32_bf16 v[12:15], v[172:175], a[32:35], 0\n"
    "s_xor_b32 s78, s87, 28672\n"
    "s_add_u32 m0, s70, 12288\n"
    "v_mfma_f32_16x16x32_bf16 v[12:15], v[188:191], a[36:39], v[12:15]\n"
    "buffer_load_dwordx4 v110, s[24:27], s78 offen offset:0 nt lds\n"
    "s_waitcnt lgkmcnt(5)\n"
    "v_mfma_f32_16x16x32_bf16 v[8:11], v[204:207], a[40:43], v[8:11]\n"
    "s_waitcnt lgkmcnt(4)\n"
    "v_mfma_f32_16x16x32_bf16 v[8:11], v[220:223], a[44:47], v[8:11]\n"
    "v_mfma_f32_16x16x32_bf16 v[12:15], v[236:239], a[40:43], v[12:15]\n"
    "v_mfma_f32_16x16x32_bf16 v[12:15], v[252:255], a[44:47], v[12:15]\n"
    "s_waitcnt lgkmcnt(3)\n"
    "v_mfma_f32_16x16x32_bf16 v[8:11], a[140:143], a[48:51], v[8:11]\n"
    "s_waitcnt lgkmcnt(2)\n"
    "v_mfma_f32_16x16x32_bf16 v[8:11], a[156:159], a[52:55], v[8:11]\n"
    "v_mfma_f32_16x16x32_bf16 v[12:15], a[172:175], a[48:51], v[12:15]\n"
    "s_add_u32 m0, s70, 12288\n"
    "v_mfma_f32_16x16x32_bf16 v[12:15], a[188:191], a[52:55], v[12:15]\n"
    "buffer_load_dwordx4 v111, s[24:27], s78 offen offset:1024 nt lds\n"
    "s_waitcnt lgkmcnt(1)\n"
    "v_mfma_f32_16x16x32_bf16 v[8:11], a[204:207], a[56:59], v[8:11]\n"
    "s_waitcnt lgkmcnt(0)\n"
    "v_mfma_f32_16x16x32_bf16 v[8:11], v[76:79], a[60:63], v[8:11]\n"
    "v_mfma_f32_16x16x32_bf16 v[12:15], a[236:239], a[56:59], v[12:15]\n"
    "v_mfma_f32_16x16x32_bf16 v[12:15], v[92:95], a[60:63], v[12:15]\n"
    "v_mfma_f32_16x16x32_bf16 v[40:43], v[136:139], a[32:35], 0\n"
    "v_mfma_f32_16x16x32_bf16 v[40:43], v[152:155], a[36:39], v[40:43]\n"
    "v_mfma_f32_16x16x32_bf16 v[44:47], v[168:171], a[32:35], 0\n"
    "s_add_u32 m0, s70, 12288\n"
    "v_mfma_f32_16x16x32_bf16 v[44:47], v[184:187], a[36:39], v[44:47]\n"
    "buffer_load_dwordx4 v110, s[24:27], s78 offen offset:2048 nt lds\n"
    "v_max3_f32 v102, v8, v9, v10\n"
    "v_mfma_f32_16x16x32_bf16 v[40:43], v[200:203], a[40:43], v[40:43]\n"
    "v_max3_f32 v102, v102, v11, v12\n"
    "v_max3_f32 v102, v102, v13, v14\n"
    "v_mfma_f32_16x16x32_bf16 v[40:43], v[216:219], a[44:47], v[40:43]\n"
    "v_max_f32 v102, v102, v15\n"
    "v_mov_b32 v125, v102\n"
    "v_mfma_f32_16x16x32_bf16 v[44:47], v[232:235], a[40:43], v[44:47]\n"
    "s_nop 0\n"
    "v_permlane32_swap_b32 v102, v125\n"
    "v_max_f32 v102, v102, v125\n"
    "v_mfma_f32_16x16x32_bf16 v[44:47], v[248:251], a[44:47], v[44:47]\n"
    "v_mov_b32 v125, v102\n"
    "v_mov_b32 v106, 0\n"
    "v_mfma_f32_16x16x32_bf16 v[40:43], a[136:139], a[48:51], v[40:43]\n"
    "v_permlane16_swap_b32 v102, v125\n"
    "v_max_f32 v102, v102, v125\n"
    "v_mfma_f32_16x16x32_bf16 v[40:43], a[152:155], a[52:55], v[40:43]\n"
    "v_mul_f32 v102, s39, v102\n"
    "v_add_f32 v102, s40, v102\n"
    "v_mfma_f32_16x16x32_bf16 v[44:47], a[168:171], a[48:51], v[44:47]\n"
    "s_add_u32 m0, s70, 12288\n"
    "v_fma_f32 v8, v8, s39, -v102\n"
    "v_mfma_f32_16x16x32_bf16 v[44:47], a[184:187], a[52:55], v[44:47]\n"
    "buffer_load_dwordx4 v111, s[24:27], s78 offen offset:3072 nt lds\n"
    "v_fma_f32 v9, v9, s39, -v102\n"
    "v_mfma_f32_16x16x32_bf16 v[40:43], a[200:203], a[56:59], v[40:43]\n"
    "v_fma_f32 v10, v10, s39, -v102\n"
    "v_fma_f32 v11, v11, s39, -v102\n"
    "v_mfma_f32_16x16x32_bf16 v[40:43], v[72:75], a[60:63], v[40:43]\n"
    "v_fma_f32 v12, v12, s39, -v102\n"
    "v_fma_f32 v13, v13, s39, -v102\n"
    "v_mfma_f32_16x16x32_bf16 v[44:47], a[232:235], a[56:59], v[44:47]\n"
    "v_fma_f32 v14, v14, s39, -v102\n"
    "v_fma_f32 v15, v15, s39, -v102\n"
    "v_mfma_f32_16x16x32_bf16 v[44:47], v[88:91], a[60:63], v[44:47]\n"
    "v_exp_f32 v8, v8\n"
    "v_exp_f32 v9, v9\n"
    "v_exp_f32 v10, v10\n"
    "v_exp_f32 v11, v11\n"
    "v_add_f32 v106, v106, v8\n"
    "v_add_f32 v106, v106, v9\n"
    "v_cvt_pk_bf16_f32 v8, v8, v9\n"
    "v_exp_f32 v12, v12\n"
    "v_exp_f32 v13, v13\n"
    "v_add_f32 v106, v106, v10\n"
    "v_add_f32 v106, v106, v11\n"
    "v_cvt_pk_bf16_f32 v9, v10, v11\n"
    "v_exp_f32 v14, v14\n"
    "v_exp_f32 v15, v15\n"
    "v_add_f32 v106, v106, v12\n"
    "v_add_f32 v106, v106, v13\n"
    "v_cvt_pk_bf16_f32 v10, v12, v13\n"
    "v_add_f32 v106, v106, v14\n"
    "v_add_f32 v106, v106, v15\n"
    "v_cvt_pk_bf16_f32 v11, v14, v15\n"
    "v_fma_f32 v40, v40, s39, -v102\n"
    "v_fma_f32 v41, v41, s39, -v102\n"
    "v_fma_f32 v42, v42, s39, -v102\n"
    "v_fma_f32 v43, v43, s39, -v102\n"
    "v_exp_f32 v40, v40\n"
    "v_exp_f32 v41, v41\n"
    "v_exp_f32 v42, v42\n"
    "v_exp_f32 v43, v43\n"
    "v_add_f32 v109, v40, v41\n"
    "v_cvt_pk_bf16_f32 v40, v40, v41\n"
    "v_add_f32 v109, v109, v42\n"
    "v_add_f32 v109, v109, v43\n"
    "v_cvt_pk_bf16_f32 v41, v42, v43\n"
    "v_fma_f32 v44, v44, s39, -v102\n"
    "v_fma_f32 v45, v45, s39, -v102\n"
    "v_fma_f32 v46, v46, s39, -v102\n"
    "v_fma_f32 v47, v47, s39, -v102\n"
    "v_exp_f32 v44, v44\n"
    "v_exp_f32 v45, v45\n"
    "v_exp_f32 v46, v46\n"
    "v_cvt_pk_bf16_f32 v42, v44, v45\n"
    "v_add_f32 v44, v109, v44\n"
    "v_exp_f32 v47, v47\n"
    "v_add_f32 v44, v44, v45\n"
    "v_add_f32 v44, v44, v46\n"
    "v_cvt_pk_bf16_f32 v43, v46, v47\n"
    "v_add_f32 v44, v44, v47\n"
    "s_add_u32 m0, s38, 81920\n"
    "s_mov_b32 s77, 32768\n"
    "buffer_load_dwordx4 v100, s[20:23], s77 offen offset:0 nt lds\n"
    "s_add_u32 m0, s38, 82816\n"
    "s_nop 0\n"
    "buffer_load_dwordx4 v100, s[20:23], s77 offen offset:128 nt lds\n"
    "s_add_u32 m0, s38, 83712\n"
    "s_nop 0\n"
    "buffer_load_dwordx4 v100, s[20:23], s77 offen offset:256 nt lds\n"
    "s_add_u32 m0, s38, 84608\n"
    "s_nop 0\n"
    "buffer_load_dwordx4 v100, s[20:23], s77 offen offset:384 nt lds\n"
    "s_waitcnt vmcnt(12)\n"
    "ds_read_b128 a[64:67], v114 offset:0\n"
    "ds_read_b128 a[68:71], v115 offset:0\n"
    "ds_read_b128 a[72:75], v116 offset:0\n"
    "ds_read_b128 a[76:79], v117 offset:0\n"
    "ds_read_b128 a[80:83], v114 offset:256\n"
    "ds_read_b128 a[84:87], v115 offset:256\n"
    "ds_read_b128 a[88:91], v116 offset:256\n"
    "ds_read_b128 a[92:95], v117 offset:256\n"
    "s_waitcnt lgkmcnt(7)\n"
    "v_mfma_f32_16x16x32_bf16 v[16:19], v[140:143], a[64:67], 0\n"
    "s_add_u32 m0, s38, 32768\n"
    "s_mov_b32 s76, 32768\n"
    "s_waitcnt lgkmcnt(6)\n"
    "v_mfma_f32_16x16x32_bf16 v[16:19], v[156:159], a[68:71], v[16:19]\n"
    "buffer_load_dwordx4 v99, s[16:19], s76 offen offset:0 nt lds\n"
    "v_mfma_f32_16x16x32_bf16 v[20:23], v[172:175], a[64:67], 0\n"
    "v_mfma_f32_16x16x32_bf16 v[20:23], v[188:191], a[68:71], v[20:23]\n"
    "s_add_u32 m0, s38, 33664\n"
    "s_waitcnt lgkmcnt(5)\n"
    "v_mfma_f32_16x16x32_bf16 v[16:19], v[204:207], a[72:75], v[16:19]\n"
    "buffer_load_dwordx4 v99, s[16:19], s76 offen offset:128 nt lds\n"
    "s_waitcnt lgkmcnt(4)\n"
    "v_mfma_f32_16x16x32_bf16 v[16:19], v[220:223], a[76:79], v[16:19]\n"
    "v_mfma_f32_16x16x32_bf16 v[20:23], v[236:239], a[72:75], v[20:23]\n"
    "s_add_u32 m0, s38, 34560\n"
    "v_mfma_f32_16x16x32_bf16 v[20:23], v[252:255], a[76:79], v[20:23]\n"
    "buffer_load_dwordx4 v99, s[16:19], s76 offen offset:256 nt lds\n"
    "s_waitcnt lgkmcnt(3)\n"
    "v_mfma_f32_16x16x32_bf16 v[16:19], a[140:143], a[80:83], v[16:19]\n"
    "s_waitcnt lgkmcnt(2)\n"
    "v_mfma_f32_16x16x32_bf16 v[16:19], a[156:159], a[84:87], v[16:19]\n"
    "s_add_u32 m0, s38, 35456\n"
    "v_mfma_f32_16x16x32_bf16 v[20:23], a[172:175], a[80:83], v[20:23]\n"
    "buffer_load_dwordx4 v99, s[16:19], s76 offen offset:384 nt lds\n"
    "v_mfma_f32_16x16x32_bf16 v[20:23], a[188:191], a[84:87], v[20:23]\n"
    "s_waitcnt lgkmcnt(1)\n"
    "v_mfma_f32_16x16x32_bf16 v[16:19], a[204:207], a[88:91], v[16:19]\n"
    "s_waitcnt lgkmcnt(0)\n"
    "v_mfma_f32_16x16x32_bf16 v[16:19], v[76:79], a[92:95], v[16:19]\n"
    "v_mfma_f32_16x16x32_bf16 v[20:23], a[236:239], a[88:91], v[20:23]\n"
    "s_waitcnt lgkmcnt(0)\n"
    "s_barrier\n"
    "v_mfma_f32_16x16x32_bf16 v[20:23], v[92:95], a[92:95], v[20:23]\n"
    "s_add_u32 m0, s38, 0\n"
    "s_mov_b32 s76, 49152\n"
    "v_mfma_f32_16x16x32_bf16 v[48:51], v[136:139], a[64:67], 0\n"
    "buffer_load_dwordx4 v99, s[16:19], s76 offen offset:0 nt lds\n"
    "v_mfma_f32_16x16x32_bf16 v[48:51], v[152:155], a[68:71], v[48:51]\n"
    "v_mfma_f32_16x16x32_bf16 v[52:55], v[168:171], a[64:67], 0\n"
    "v_mfma_f32_16x16x32_bf16 v[52:55], v[184:187], a[68:71], v[52:55]\n"
    "s_add_u32 m0, s38, 896\n"
    "v_max3_f32 v103, v16, v17, v18\n"
    "v_mfma_f32_16x16x32_bf16 v[48:51], v[200:203], a[72:75], v[48:51]\n"
    "buffer_load_dwordx4 v99, s[16:19], s76 offen offset:128 nt lds\n"
    "v_max3_f32 v103, v103, v19, v20\n"
    "v_mfma_f32_16x16x32_bf16 v[48:51], v[216:219], a[76:79], v[48:51]\n"
    "v_max3_f32 v103, v103, v21, v22\n"
    "v_max_f32 v103, v103, v23\n"
    "v_mfma_f32_16x16x32_bf16 v[52:55], v[232:235], a[72:75], v[52:55]\n"
    "v_mov_b32 v125, v103\n"
    "v_mov_b32 v107, 0\n"
    "v_mfma_f32_16x16x32_bf16 v[52:55], v[248:251], a[76:79], v[52:55]\n"
    "s_add_u32 m0, s38, 1792\n"
    "v_permlane32_swap_b32 v103, v125\n"
    "v_mfma_f32_16x16x32_bf16 v[48:51], a[136:139], a[80:83], v[48:51]\n"
    "buffer_load_dwordx4 v99, s[16:19], s76 offen offset:256 nt lds\n"
    "v_max_f32 v103, v103, v125\n"
    "v_mfma_f32_16x16x32_bf16 v[48:51], a[152:155], a[84:87], v[48:51]\n"
    "v_mov_b32 v125, v103\n"
    "s_nop 1\n"
    "v_permlane16_swap_b32 v103, v125\n"
    "v_mfma_f32_16x16x32_bf16 v[52:55], a[168:171], a[80:83], v[52:55]\n"
    "v_max_f32 v103, v103, v125\n"
    "v_mul_f32 v103, s39, v103\n"
    "v_mfma_f32_16x16x32_bf16 v[52:55], a[184:187], a[84:87], v[52:55]\n"
    "s_add_u32 m0, s38, 2688\n"
    "v_add_f32 v103, s40, v103\n"
    "v_mfma_f32_16x16x32_bf16 v[48:51], a[200:203], a[88:91], v[48:51]\n"
    "buffer_load_dwordx4 v99, s[16:19], s76 offen offset:384 nt lds\n"
    "v_fma_f32 v16, v16, s39, -v103\n"
    "v_mfma_f32_16x16x32_bf16 v[48:51], v[72:75], a[92:95], v[48:51]\n"
    "v_fma_f32 v17, v17, s39, -v103\n"
    "v_fma_f32 v18, v18, s39, -v103\n"
    "v_mfma_f32_16x16x32_bf16 v[52:55], a[232:235], a[88:91], v[52:55]\n"
    "v_fma_f32 v19, v19, s39, -v103\n"
    "v_fma_f32 v20, v20, s39, -v103\n"
    "v_mfma_f32_16x16x32_bf16 v[52:55], v[88:91], a[92:95], v[52:55]\n"
    "v_fma_f32 v21, v21, s39, -v103\n"
    "v_fma_f32 v22, v22, s39, -v103\n"
    "v_fma_f32 v23, v23, s39, -v103\n"
    "v_exp_f32 v16, v16\n"
    "v_exp_f32 v17, v17\n"
    "v_exp_f32 v18, v18\n"
    "v_exp_f32 v19, v19\n"
    "v_add_f32 v107, v107, v16\n"
    "v_add_f32 v107, v107, v17\n"
    "v_cvt_pk_bf16_f32 v16, v16, v17\n"
    "v_exp_f32 v20, v20\n"
    "v_exp_f32 v21, v21\n"
    "v_add_f32 v107, v107, v18\n"
    "v_add_f32 v107, v107, v19\n"
    "v_cvt_pk_bf16_f32 v17, v18, v19\n"
    "v_exp_f32 v22, v22\n"
    "v_exp_f32 v23, v23\n"
    "v_add_f32 v107, v107, v20\n"
    "v_add_f32 v107, v107, v21\n"
    "v_cvt_pk_bf16_f32 v18, v20, v21\n"
    "v_add_f32 v107, v107, v22\n"
    "v_add_f32 v107, v107, v23\n"
    "v_cvt_pk_bf16_f32 v19, v22, v23\n"
    "v_fma_f32 v48, v48, s39, -v103\n"
    "v_fma_f32 v49, v49, s39, -v103\n"
    "v_fma_f32 v50, v50, s39, -v103\n"
    "v_fma_f32 v51, v51, s39, -v103\n"
    "v_exp_f32 v48, v48\n"
    "v_exp_f32 v49, v49\n"
    "v_exp_f32 v50, v50\n"
    "v_exp_f32 v51, v51\n"
    "v_add_f32 v109, v48, v49\n"
    "v_cvt_pk_bf16_f32 v48, v48, v49\n"
    "v_add_f32 v109, v109, v50\n"
    "v_add_f32 v109, v109, v51\n"
    "v_cvt_pk_bf16_f32 v49, v50, v51\n"
    "v_fma_f32 v52, v52, s39, -v103\n"
    "v_fma_f32 v53, v53, s39, -v103\n"
    "v_fma_f32 v54, v54, s39, -v103\n"
    "v_fma_f32 v55, v55, s39, -v103\n"
    "s_waitcnt vmcnt(12)\n"
    "ds_read_b128 a[96:99], v118 offset:8192\n"
    "ds_read_b128 a[100:103], v119 offset:8192\n"
    "ds_read_b128 a[104:107], v120 offset:8192\n"
    "ds_read_b128 a[108:111], v121 offset:8192\n"
    "ds_read_b128 a[112:115], v118 offset:8448\n"
    "ds_read_b128 a[116:119], v119 offset:8448\n"
    "ds_read_b128 a[120:123], v120 offset:8448\n"
    "ds_read_b128 a[124:127], v121 offset:8448\n"
    "s_waitcnt lgkmcnt(7)\n"
    "v_mfma_f32_16x16x32_bf16 v[24:27], v[140:143], a[96:99], 0\n"
    "s_mov_b32 s37, 6\n"
    "s_add_u32 s34, s38, 32768\n"
    "s_waitcnt lgkmcnt(6)\n"
    "v_mfma_f32_16x16x32_bf16 v[24:27], v[156:159], a[100:103], v[24:27]\n"
    "s_add_u32 m0, s38, 49152\n"
    "s_mov_b32 s77, 49152\n"
    "v_mfma_f32_16x16x32_bf16 v[28:31], v[172:175], a[96:99], 0\n"
    "buffer_load_dwordx4 v100, s[20:23], s77 offen offset:0 nt lds\n"
    "s_mov_b32 s35, -32768\n"
    "v_mfma_f32_16x16x32_bf16 v[28:31], v[188:191], a[100:103], v[28:31]\n"
    "s_mov_b32 s36, 81920\n"
    "s_waitcnt lgkmcnt(5)\n"
    "v_mfma_f32_16x16x32_bf16 v[24:27], v[204:207], a[104:107], v[24:27]\n"
    "s_waitcnt lgkmcnt(4)\n"
    "v_mfma_f32_16x16x32_bf16 v[24:27], v[220:223], a[108:111], v[24:27]\n"
    "v_mfma_f32_16x16x32_bf16 v[28:31], v[236:239], a[104:107], v[28:31]\n"
    "v_mfma_f32_16x16x32_bf16 v[28:31], v[252:255], a[108:111], v[28:31]\n"
    "s_add_u32 m0, s38, 50048\n"
    "s_waitcnt lgkmcnt(3)\n"
    "v_mfma_f32_16x16x32_bf16 v[24:27], a[140:143], a[112:115], v[24:27]\n"
    "buffer_load_dwordx4 v100, s[20:23], s77 offen offset:128 nt lds\n"
    "s_waitcnt lgkmcnt(2)\n"
    "v_mfma_f32_16x16x32_bf16 v[24:27], a[156:159], a[116:119], v[24:27]\n"
    "v_mfma_f32_16x16x32_bf16 v[28:31], a[172:175], a[112:115], v[28:31]\n"
    "v_mfma_f32_16x16x32_bf16 v[28:31], a[188:191], a[116:119], v[28:31]\n"
    "s_waitcnt lgkmcnt(1)\n"
    "v_mfma_f32_16x16x32_bf16 v[24:27], a[204:207], a[120:123], v[24:27]\n"
    "s_waitcnt lgkmcnt(0)\n"
    "v_mfma_f32_16x16x32_bf16 v[24:27], v[76:79], a[124:127], v[24:27]\n"
    "s_add_u32 m0, s38, 50944\n"
    "v_mfma_f32_16x16x32_bf16 v[28:31], a[236:239], a[120:123], v[28:31]\n"
    "buffer_load_dwordx4 v100, s[20:23], s77 offen offset:256 nt lds\n"
    "v_mfma_f32_16x16x32_bf16 v[28:31], v[92:95], a[124:127], v[28:31]\n"
    "v_mfma_f32_16x16x32_bf16 v[56:59], v[136:139], a[96:99], 0\n"
    "v_mfma_f32_16x16x32_bf16 v[56:59], v[152:155], a[100:103], v[56:59]\n"
    "v_mfma_f32_16x16x32_bf16 v[60:63], v[168:171], a[96:99], 0\n"
    "v_mfma_f32_16x16x32_bf16 v[60:63], v[184:187], a[100:103], v[60:63]\n"
    "s_add_u32 m0, s38, 51840\n"
    "v_max3_f32 v104, v24, v25, v26\n"
    "s_nop 1\n"
    "v_max3_f32 v104, v104, v27, v28\n"
    "v_max3_f32 v104, v104, v29, v30\n"
    "v_max_f32 v104, v104, v31\n"
    "v_mov_b32 v125, v104\n"
    "v_mfma_f32_16x16x32_bf16 v[56:59], v[200:203], a[104:107], v[56:59]\n"
    "s_waitcnt vmcnt(11) lgkmcnt(0)\n"
    "s_barrier\n"
    "buffer_load_dwordx4 v100, s[20:23], s77 offen offset:384 nt lds\n"
    "ds_read_b64_tr_b16 a[208:209], v97 offset:32768\n"
    "v_mfma_f32_16x16x32_bf16 v[56:59], v[216:219], a[108:111], v[56:59]\n"
    "ds_read_b64_tr_b16 a[210:211], v97 offset:40960\n"
    "ds_read_b64_tr_b16 a[212:213], v98 offset:32768\n"
    "ds_read_b64_tr_b16 a[214:215], v98 offset:40960\n"
    "ds_read_b64_tr_b16 a[216:217], v97 offset:33280\n"
    "ds_read_b64_tr_b16 a[218:219], v97 offset:41472\n"
    "v_mfma_f32_16x16x32_bf16 v[60:63], v[232:235], a[104:107], v[60:63]\n"
    "ds_read_b64_tr_b16 a[220:221], v98 offset:33280\n"
    "ds_read_b64_tr_b16 a[222:223], v98 offset:41472\n"
    "ds_read_b64_tr_b16 a[240:241], v97 offset:33792\n"
    "ds_read_b64_tr_b16 a[242:243], v97 offset:41984\n"
    "v_mfma_f32_16x16x32_bf16 v[60:63], v[248:251], a[108:111], v[60:63]\n"
    "ds_read_b64_tr_b16 a[244:245], v98 offset:33792\n"
    "ds_read_b64_tr_b16 a[246:247], v98 offset:41984\n"
    "ds_read_b64_tr_b16 a[248:249], v97 offset:34304\n"
    "ds_read_b64_tr_b16 a[250:251], v97 offset:42496\n"
    "ds_read_b64_tr_b16 a[252:253], v98 offset:34304\n"
    "v_mfma_f32_16x16x32_bf16 v[56:59], a[136:139], a[112:115], v[56:59]\n"
    "ds_read_b64_tr_b16 a[254:255], v98 offset:42496\n"
    "v_permlane32_swap_b32 v104, v125\n"
    "v_max_f32 v104, v104, v125\n"
    "v_mov_b32 v125, v104\n"
    "v_mfma_f32_16x16x32_bf16 v[56:59], a[152:155], a[116:119], v[56:59]\n"
    "s_nop 0\n"
    "v_permlane16_swap_b32 v104, v125\n"
    "v_max_f32 v104, v104, v125\n"
    "v_mul_f32 v104, s39, v104\n"
    "v_add_f32 v104, s40, v104\n"
    "v_fma_f32 v24, v24, s39, -v104\n"
    "v_mfma_f32_16x16x32_bf16 v[60:63], a[168:171], a[112:115], v[60:63]\n"
    "v_fma_f32 v25, v25, s39, -v104\n"
    "v_fma_f32 v26, v26, s39, -v104\n"
    "v_fma_f32 v27, v27, s39, -v104\n"
    "v_fma_f32 v28, v28, s39, -v104\n"
    "v_mfma_f32_16x16x32_bf16 v[60:63], a[184:187], a[116:119], v[60:63]\n"
    "v_fma_f32 v29, v29, s39, -v104\n"
    "v_fma_f32 v30, v30, s39, -v104\n"
    "v_fma_f32 v31, v31, s39, -v104\n"
    "v_mov_b32 v108, 0\n"
    "v_mfma_f32_16x16x32_bf16 v[56:59], a[200:203], a[120:123], v[56:59]\n"
    "v_exp_f32 v24, v24\n"
    "v_exp_f32 v25, v25\n"
    "v_add_f32 v108, v108, v24\n"
    "v_mfma_f32_16x16x32_bf16 v[56:59], v[72:75], a[124:127], v[56:59]\n"
    "v_exp_f32 v26, v26\n"
    "v_exp_f32 v27, v27\n"
    "v_add_f32 v108, v108, v25\n"
    "v_mfma_f32_16x16x32_bf16 v[60:63], a[232:235], a[120:123], v[60:63]\n"
    "v_cvt_pk_bf16_f32 v24, v24, v25\n"
    "v_exp_f32 v28, v28\n"
    "v_add_f32 v108, v108, v26\n"
    "v_mfma_f32_16x16x32_bf16 v[60:63], v[88:91], a[124:127], v[60:63]\n"
    "v_exp_f32 v29, v29\n"
    "v_add_f32 v108, v108, v27\n"
    "v_cvt_pk_bf16_f32 v25, v26, v27\n"
    "s_waitcnt lgkmcnt(12)\n"
    "v_mfma_f32_16x16x32_bf16 v[128:131], a[208:211], v[0:3], 0\n"
    "v_exp_f32 v30, v30\n"
    "v_exp_f32 v31, v31\n"
    "v_mfma_f32_16x16x32_bf16 v[132:135], a[208:211], v[8:11], 0\n"
    "v_add_f32 v108, v108, v28\n"
    "v_add_f32 v108, v108, v29\n"
    "v_cvt_pk_bf16_f32 v26, v28, v29\n"
    "v_add_f32 v108, v108, v30\n"
    "v_add_f32 v108, v108, v31\n"
    "v_mfma_f32_16x16x32_bf16 v[136:139], a[208:211], v[16:19], 0\n"
    "v_cvt_pk_bf16_f32 v27, v30, v31\n"
    "s_nop 1\n"
    "v_mfma_f32_16x16x32_bf16 v[140:143], a[208:211], v[24:27], 0\n"
    "ds_read_b64_tr_b16 a[208:209], v97 offset:34816\n"
    "ds_read_b64_tr_b16 a[210:211], v97 offset:43008\n"
    "v_mfma_f32_16x16x32_bf16 v[156:159], a[212:215], v[24:27], 0\n"
    "v_mfma_f32_16x16x32_bf16 v[152:155], a[212:215], v[16:19], 0\n"
    "s_add_u32 m0, s38, 65536\n"
    "s_mov_b32 s77, 65536\n"
    "v_mfma_f32_16x16x32_bf16 v[148:151], a[212:215], v[8:11], 0\n"
    "buffer_load_dwordx4 v100, s[20:23], s77 offen offset:0 nt lds\n"
    "v_mfma_f32_16x16x32_bf16 v[144:147], a[212:215], v[0:3], 0\n"
    "ds_read_b64_tr_b16 a[212:213], v98 offset:34816\n"
    "s_waitcnt lgkmcnt(11)\n"
    "v_mfma_f32_16x16x32_bf16 v[160:163], a[216:219], v[0:3], 0\n"
    "ds_read_b64_tr_b16 a[214:215], v98 offset:43008\n"
    "v_mfma_f32_16x16x32_bf16 v[164:167], a[216:219], v[8:11], 0\n"
    "v_mfma_f32_16x16x32_bf16 v[168:171], a[216:219], v[16:19], 0\n"
    "v_mfma_f32_16x16x32_bf16 v[172:175], a[216:219], v[24:27], 0\n"
    "s_add_u32 m0, s38, 66432\n"
    "ds_read_b64_tr_b16 a[216:217], v97 offset:35328\n"
    "v_mfma_f32_16x16x32_bf16 v[188:191], a[220:223], v[24:27], 0\n"
    "buffer_load_dwordx4 v100, s[20:23], s77 offen offset:128 nt lds\n"
    "ds_read_b64_tr_b16 a[218:219], v97 offset:43520\n"
    "v_mfma_f32_16x16x32_bf16 v[184:187], a[220:223], v[16:19], 0\n"
    "v_mfma_f32_16x16x32_bf16 v[180:183], a[220:223], v[8:11], 0\n"
    "v_mfma_f32_16x16x32_bf16 v[176:179], a[220:223], v[0:3], 0\n"
    "ds_read_b64_tr_b16 a[220:221], v98 offset:35328\n"
    "s_waitcnt lgkmcnt(11)\n"
    "v_mfma_f32_16x16x32_bf16 v[192:195], a[240:243], v[0:3], 0\n"
    "ds_read_b64_tr_b16 a[222:223], v98 offset:43520\n"
    "v_mfma_f32_16x16x32_bf16 v[196:199], a[240:243], v[8:11], 0\n"
    "s_add_u32 m0, s38, 67328\n"
    "v_mfma_f32_16x16x32_bf16 v[200:203], a[240:243], v[16:19], 0\n"
    "buffer_load_dwordx4 v100, s[20:23], s77 offen offset:256 nt lds\n"
    "v_mfma_f32_16x16x32_bf16 v[204:207], a[240:243], v[24:27], 0\n"
    "ds_read_b64_tr_b16 a[240:241], v97 offset:35840\n"
    "ds_read_b64_tr_b16 a[242:243], v97 offset:44032\n"
    "v_mfma_f32_16x16x32_bf16 v[220:223], a[244:247], v[24:27], 0\n"
    "v_mfma_f32_16x16x32_bf16 v[216:219], a[244:247], v[16:19], 0\n"
    "v_mfma_f32_16x16x32_bf16 v[212:215], a[244:247], v[8:11], 0\n"
    "v_mfma_f32_16x16x32_bf16 v[208:211], a[244:247], v[0:3], 0\n"
    "s_add_u32 m0, s38, 68224\n"
    "s_waitcnt lgkmcnt(10)\n"
    "v_mfma_f32_16x16x32_bf16 v[224:227], a[248:251], v[0:3], 0\n"
    "buffer_load_dwordx4 v100, s[20:23], s77 offen offset:384 nt lds\n"
    "ds_read_b64_tr_b16 a[244:245], v98 offset:35840\n"
    "v_mfma_f32_16x16x32_bf16 v[228:231], a[248:251], v[8:11], 0\n"
    "ds_read_b64_tr_b16 a[246:247], v98 offset:44032\n"
    "v_mfma_f32_16x16x32_bf16 v[232:235], a[248:251], v[16:19], 0\n"
    "v_mfma_f32_16x16x32_bf16 v[236:239], a[248:251], v[24:27], 0\n"
    "ds_read_b64_tr_b16 a[248:249], v97 offset:36352\n"
    "ds_read_b64_tr_b16 a[250:251], v97 offset:44544\n"
    "v_mfma_f32_16x16x32_bf16 v[252:255], a[252:255], v[24:27], 0\n"
    "v_mfma_f32_16x16x32_bf16 v[248:251], a[252:255], v[16:19], 0\n"
    "s_add_u32 m0, s38, 16384\n"
    "s_mov_b32 s76, 65536\n"
    "v_mfma_f32_16x16x32_bf16 v[244:247], a[252:255], v[8:11], 0\n"
    "buffer_load_dwordx4 v99, s[16:19], s76 offen offset:0 nt lds\n"
    "v_mfma_f32_16x16x32_bf16 v[240:243], a[252:255], v[0:3], 0\n"
    "ds_read_b64_tr_b16 a[252:253], v98 offset:36352\n"
    "s_waitcnt lgkmcnt(11)\n"
    "v_mfma_f32_16x16x32_bf16 a[128:131], a[208:211], v[0:3], 0\n"
    "ds_read_b64_tr_b16 a[254:255], v98 offset:44544\n"
    "v_mfma_f32_16x16x32_bf16 a[132:135], a[208:211], v[8:11], 0\n"
    "v_mfma_f32_16x16x32_bf16 a[136:139], a[208:211], v[16:19], 0\n"
    "v_mfma_f32_16x16x32_bf16 a[140:143], a[208:211], v[24:27], 0\n"
    "s_add_u32 m0, s38, 17280\n"
    "v_mfma_f32_16x16x32_bf16 a[156:159], a[212:215], v[24:27], 0\n"
    "buffer_load_dwordx4 v99, s[16:19], s76 offen offset:128 nt lds\n"
    "v_mfma_f32_16x16x32_bf16 a[152:155], a[212:215], v[16:19], 0\n"
    "v_mfma_f32_16x16x32_bf16 a[148:151], a[212:215], v[8:11], 0\n"
    "v_mfma_f32_16x16x32_bf16 a[144:147], a[212:215], v[0:3], 0\n"
    "s_waitcnt lgkmcnt(8)\n"
    "v_mfma_f32_16x16x32_bf16 a[160:163], a[216:219], v[0:3], 0\n"
    "v_mfma_f32_16x16x32_bf16 a[164:167], a[216:219], v[8:11], 0\n"
    "s_add_u32 m0, s38, 18176\n"
    "v_mfma_f32_16x16x32_bf16 a[168:171], a[216:219], v[16:19], 0\n"
    "buffer_load_dwordx4 v99, s[16:19], s76 offen offset:256 nt lds\n"
    "v_mfma_f32_16x16x32_bf16 a[172:175], a[216:219], v[24:27], 0\n"
    "v_mfma_f32_16x16x32_bf16 a[188:191], a[220:223], v[24:27], 0\n"
    "v_mfma_f32_16x16x32_bf16 a[184:187], a[220:223], v[16:19], 0\n"
    "v_mfma_f32_16x16x32_bf16 a[180:183], a[220:223], v[8:11], 0\n"
    "v_mfma_f32_16x16x32_bf16 a[176:179], a[220:223], v[0:3], 0\n"
    "s_add_u32 m0, s38, 19072\n"
    "s_waitcnt lgkmcnt(4)\n"
    "v_mfma_f32_16x16x32_bf16 a[192:195], a[240:243], v[0:3], 0\n"
    "buffer_load_dwordx4 v99, s[16:19], s76 offen offset:384 nt lds\n"
    "s_waitcnt vmcnt(16) lgkmcnt(0)\n"
    "s_barrier\n"
    "v_mfma_f32_16x16x32_bf16 a[196:199], a[240:243], v[8:11], 0\n"
    "ds_read_b128 a[208:211], v96 offset:32768\n"
    "ds_read_b128 a[212:215], v96 offset:33280\n"
    "v_mfma_f32_16x16x32_bf16 a[200:203], a[240:243], v[16:19], 0\n"
    "ds_read_b128 a[216:219], v96 offset:40960\n"
    "ds_read_b128 a[220:223], v96 offset:41472\n"
    "v_mfma_f32_16x16x32_bf16 a[204:207], a[240:243], v[24:27], 0\n"
    "v_add_u32 v96, 32768, v96\n"
    "v_add_u32 v97, 32768, v97\n"
    "v_mfma_f32_16x16x32_bf16 v[76:79], a[244:247], v[24:27], 0\n"
    "v_add_u32 v98, 32768, v98\n"
    "v_mfma_f32_16x16x32_bf16 v[72:75], a[244:247], v[16:19], 0\n"
    "s_and_b32 s43, s68, 16383\n"
    "s_add_u32 m0, s43, s70\n"
    "s_xor_b32 s85, s68, s87\n"
    "v_mfma_f32_16x16x32_bf16 v[68:71], a[244:247], v[8:11], 0\n"
    "buffer_load_dwordx4 v110, s[64:67], s85 offen offset:0 nt lds\n"
    "v_mfma_f32_16x16x32_bf16 v[64:67], a[244:247], v[0:3], 0\n"
    "v_mfma_f32_16x16x32_bf16 a[224:227], a[248:251], v[0:3], 0\n"
    "v_mfma_f32_16x16x32_bf16 a[228:231], a[248:251], v[8:11], 0\n"
    "v_mfma_f32_16x16x32_bf16 a[232:235], a[248:251], v[16:19], 0\n"
    "v_mfma_f32_16x16x32_bf16 a[236:239], a[248:251], v[24:27], 0\n"
    "s_add_u32 s68, s68, 2048\n"
    "s_and_b32 s68, s68, 0xffff\n"
    "v_mfma_f32_16x16x32_bf16 v[92:95], a[252:255], v[24:27], 0\n"
    "buffer_load_dwordx4 v111, s[64:67], s85 offen offset:1024 nt lds\n"
    "v_mfma_f32_16x16x32_bf16 v[88:91], a[252:255], v[16:19], 0\n"
    "v_mfma_f32_16x16x32_bf16 v[84:87], a[252:255], v[8:11], 0\n"
    "v_mfma_f32_16x16x32_bf16 v[80:83], a[252:255], v[0:3], 0\n"
    "s_sleep 4\n"
    ".p2align 6\n"
    "L_loop0:\n"
    "s_waitcnt lgkmcnt(2)\n"
    "v_mfma_f32_16x16x32_bf16 v[0:3], a[208:211], a[0:3], 0\n"
    "ds_read_b128 a[240:243], v96 offset:1024\n"
    "ds_read_b128 a[244:247], v96 offset:1536\n"
    "v_mfma_f32_16x16x32_bf16 v[0:3], a[212:215], a[4:7], v[0:3]\n"
    "ds_read_b128 a[248:251], v96 offset:9216\n"
    "ds_read_b128 a[252:255], v96 offset:9728\n"
    "v_mfma_f32_16x16x32_bf16 v[8:11], a[208:211], a[32:35], 0\n"
    "v_add_u32 v97, s35, v97\n"
    "v_add_u32 v98, s35, v98\n"
    "v_mfma_f32_16x16x32_bf16 v[8:11], a[212:215], a[36:39], v[8:11]\n"
    "v_exp_f32 v52, v52\n"
    "v_mfma_f32_16x16x32_bf16 v[16:19], a[208:211], a[64:67], 0\n"
    "v_exp_f32 v53, v53\n"
    "v_mfma_f32_16x16x32_bf16 v[16:19], a[212:215], a[68:71], v[16:19]\n"
    "v_exp_f32 v54, v54\n"
    "v_mfma_f32_16x16x32_bf16 v[24:27], a[208:211], a[96:99], 0\n"
    "ds_read_b128 a[208:211], v96 offset:2048\n"
    "v_cvt_pk_bf16_f32 v50, v52, v53\n"
    "v_mfma_f32_16x16x32_bf16 v[24:27], a[212:215], a[100:103], v[24:27]\n"
    "ds_read_b128 a[212:215], v96 offset:2560\n"
    "s_waitcnt lgkmcnt(6)\n"
    "v_mfma_f32_16x16x32_bf16 v[4:7], a[216:219], a[0:3], 0\n"
    "v_add_f32 v52, v109, v52\n"
    "v_add_f32 v52, v52, v53\n"
    "v_mfma_f32_16x16x32_bf16 v[4:7], a[220:223], a[4:7], v[4:7]\n"
    "s_and_b32 s43, s68, 16383\n"
    "s_add_u32 m0, s43, s70\n"
    "s_xor_b32 s85, s68, s87\n"
    "v_mfma_f32_16x16x32_bf16 v[12:15], a[216:219], a[32:35], 0\n"
    "buffer_load_dwordx4 v110, s[64:67], s85 offen offset:0 nt lds\n"
    "v_add_f32 v52, v52, v54\n"
    "v_mfma_f32_16x16x32_bf16 v[12:15], a[220:223], a[36:39], v[12:15]\n"
    "v_exp_f32 v55, v55\n"
    "v_mfma_f32_16x16x32_bf16 v[20:23], a[216:219], a[64:67], 0\n"
    "v_cvt_pk_bf16_f32 v51, v54, v55\n"
    "v_add_f32 v52, v52, v55\n"
    "v_mfma_f32_16x16x32_bf16 v[20:23], a[220:223], a[68:71], v[20:23]\n"
    "s_add_u32 s68, s68, 2048\n"
    "s_and_b32 s68, s68, 0xffff\n"
    "v_mfma_f32_16x16x32_bf16 v[28:31], a[216:219], a[96:99], 0\n"
    "buffer_load_dwordx4 v111, s[64:67], s85 offen offset:1024 nt lds\n"
    "ds_read_b128 a[216:219], v96 offset:10240\n"
    "v_mfma_f32_16x16x32_bf16 v[28:31], a[220:223], a[100:103], v[28:31]\n"
    "ds_read_b128 a[220:223], v96 offset:10752\n"
    "s_waitcnt lgkmcnt(6)\n"
    "v_mfma_f32_16x16x32_bf16 v[0:3], a[240:243], a[8:11], v[0:3]\n"
    "v_fma_f32 v56, v56, s39, -v104\n"
    "v_fma_f32 v57, v57, s39, -v104\n"
    "v_mfma_f32_16x16x32_bf16 v[0:3], a[244:247], a[12:15], v[0:3]\n"
    "v_fma_f32 v58, v58, s39, -v104\n"
    "v_fma_f32 v59, v59, s39, -v104\n"
    "v_mfma_f32_16x16x32_bf16 v[8:11], a[240:243], a[40:43], v[8:11]\n"
    "v_exp_f32 v56, v56\n"
    "v_mfma_f32_16x16x32_bf16 v[8:11], a[244:247], a[44:47], v[8:11]\n"
    "v_exp_f32 v57, v57\n"
    "v_mfma_f32_16x16x32_bf16 v[16:19], a[240:243], a[72:75], v[16:19]\n"
    "v_exp_f32 v58, v58\n"
    "v_mfma_f32_16x16x32_bf16 v[16:19], a[244:247], a[76:79], v[16:19]\n"
    "v_exp_f32 v59, v59\n"
    "v_mfma_f32_16x16x32_bf16 v[24:27], a[240:243], a[104:107], v[24:27]\n"
    "ds_read_b128 a[240:243], v96 offset:3072\n"
    "v_add_f32 v109, v56, v57\n"
    "v_mfma_f32_16x16x32_bf16 v[24:27], a[244:247], a[108:111], v[24:27]\n"
    "ds_read_b128 a[244:247], v96 offset:3584\n"
    "s_waitcnt lgkmcnt(6)\n"
    "v_mfma_f32_16x16x32_bf16 v[4:7], a[248:251], a[8:11], v[4:7]\n"
    "v_cvt_pk_bf16_f32 v56, v56, v57\n"
    "v_add_f32 v109, v109, v58\n"
    "v_mfma_f32_16x16x32_bf16 v[4:7], a[252:255], a[12:15], v[4:7]\n"
    "v_add_f32 v109, v109, v59\n"
    "v_cvt_pk_bf16_f32 v57, v58, v59\n"
    "v_mfma_f32_16x16x32_bf16 v[12:15], a[248:251], a[40:43], v[12:15]\n"
    "v_fma_f32 v60, v60, s39, -v104\n"
    "v_fma_f32 v61, v61, s39, -v104\n"
    "v_mfma_f32_16x16x32_bf16 v[12:15], a[252:255], a[44:47], v[12:15]\n"
    "v_fma_f32 v62, v62, s39, -v104\n"
    "v_fma_f32 v63, v63, s39, -v104\n"
    "v_mfma_f32_16x16x32_bf16 v[20:23], a[248:251], a[72:75], v[20:23]\n"
    "v_exp_f32 v60, v60\n"
    "v_mfma_f32_16x16x32_bf16 v[20:23], a[252:255], a[76:79], v[20:23]\n"
    "v_exp_f32 v61, v61\n"
    "v_mfma_f32_16x16x32_bf16 v[28:31], a[248:251], a[104:107], v[28:31]\n"
    "ds_read_b128 a[248:251], v96 offset:11264\n"
    "v_cvt_pk_bf16_f32 v58, v60, v61\n"
    "v_mfma_f32_16x16x32_bf16 v[28:31], a[252:255], a[108:111], v[28:31]\n"
    "ds_read_b128 a[252:255], v96 offset:11776\n"
    "s_waitcnt lgkmcnt(6)\n"
    "v_mfma_f32_16x16x32_bf16 v[0:3], a[208:211], a[16:19], v[0:3]\n"
    "v_exp_f32 v62, v62\n"
    "v_mfma_f32_16x16x32_bf16 v[0:3], a[212:215], a[20:23], v[0:3]\n"
    "v_add_f32 v60, v109, v60\n"
    "v_add_f32 v60, v60, v61\n"
    "v_mfma_f32_16x16x32_bf16 v[8:11], a[208:211], a[48:51], v[8:11]\n"
    "v_exp_f32 v63, v63\n"
    "v_mfma_f32_16x16x32_bf16 v[8:11], a[212:215], a[52:55], v[8:11]\n"
    "v_max3_f32 v109, v36, v44, v52\n"
    "v_add_f32 v60, v60, v62\n"
    "v_mfma_f32_16x16x32_bf16 v[16:19], a[208:211], a[80:83], v[16:19]\n"
    "v_cvt_pk_bf16_f32 v59, v62, v63\n"
    "v_add_f32 v60, v60, v63\n"
    "v_mfma_f32_16x16x32_bf16 v[16:19], a[212:215], a[84:87], v[16:19]\n"
    "v_max_f32 v109, v109, v60\n"
    "v_cmp_le_f32 vcc, 1.0, v109\n"
    "v_mfma_f32_16x16x32_bf16 v[24:27], a[208:211], a[112:115], v[24:27]\n"
    "v_mfma_f32_16x16x32_bf16 v[24:27], a[212:215], a[116:119], v[24:27]\n"
    "s_waitcnt lgkmcnt(4)\n"
    "v_mfma_f32_16x16x32_bf16 v[4:7], a[216:219], a[16:19], v[4:7]\n"
    "v_mfma_f32_16x16x32_bf16 v[4:7], a[220:223], a[20:23], v[4:7]\n"
    "v_mfma_f32_16x16x32_bf16 v[12:15], a[216:219], a[48:51], v[12:15]\n"
    "v_mfma_f32_16x16x32_bf16 v[12:15], a[220:223], a[52:55], v[12:15]\n"
    "v_mfma_f32_16x16x32_bf16 v[20:23], a[216:219], a[80:83], v[20:23]\n"
    "s_waitcnt vmcnt(12) lgkmcnt(0)\n"
    "s_barrier\n"
    "v_mfma_f32_16x16x32_bf16 v[20:23], a[220:223], a[84:87], v[20:23]\n"
    "ds_read_b64_tr_b16 a[208:209], v97 offset:0\n"
    "ds_read_b64_tr_b16 a[210:211], v97 offset:8192\n"
    "v_mfma_f32_16x16x32_bf16 v[28:31], a[216:219], a[112:115], v[28:31]\n"
    "s_add_u32 m0, s34, 0\n"
    "ds_read_b64_tr_b16 a[212:213], v98 offset:0\n"
    "v_mfma_f32_16x16x32_bf16 v[28:31], a[220:223], a[116:119], v[28:31]\n"
    "buffer_load_dwordx4 v99, s[16:19], s36 offen offset:0 nt lds\n"
    "v_mfma_f32_16x16x32_bf16 v[0:3], a[240:243], a[24:27], v[0:3]\n"
    "ds_read_b64_tr_b16 a[214:215], v98 offset:8192\n"
    "ds_read_b64_tr_b16 a[216:217], v97 offset:512\n"
    "v_mfma_f32_16x16x32_bf16 v[0:3], a[244:247], a[28:31], v[0:3]\n"
    "ds_read_b64_tr_b16 a[218:219], v97 offset:8704\n"
    "ds_read_b64_tr_b16 a[220:221], v98 offset:512\n"
    "v_mfma_f32_16x16x32_bf16 v[8:11], a[240:243], a[56:59], v[8:11]\n"
    "s_cbranch_vccnz L_ra2\n"
    "L_ra2_ret:\n"
    "v_add_f32 v105, v105, v36\n"
    "v_mfma_f32_16x16x32_bf16 v[8:11], a[244:247], a[60:63], v[8:11]\n"
    "v_add_f32 v106, v106, v44\n"
    "v_add_f32 v107, v107, v52\n"
    "v_mfma_f32_16x16x32_bf16 v[16:19], a[240:243], a[88:91], v[16:19]\n"
    "s_add_u32 m0, s34, 896\n"
    "v_add_f32 v108, v108, v60\n"
    "v_mfma_f32_16x16x32_bf16 v[16:19], a[244:247], a[92:95], v[16:19]\n"
    "buffer_load_dwordx4 v99, s[16:19], s36 offen offset:128 nt lds\n"
    "ds_read_b64_tr_b16 a[222:223], v98 offset:8704\n"
    "v_mfma_f32_16x16x32_bf16 v[24:27], a[240:243], a[120:123], v[24:27]\n"
    "ds_read_b64_tr_b16 a[240:241], v97 offset:1024\n"
    "ds_read_b64_tr_b16 a[242:243], v97 offset:9216\n"
    "v_mfma_f32_16x16x32_bf16 v[24:27], a[244:247], a[124:127], v[24:27]\n"
    "ds_read_b64_tr_b16 a[244:245], v98 offset:1024\n"
    "v_mfma_f32_16x16x32_bf16 v[4:7], a[248:251], a[24:27], v[4:7]\n"
    "ds_read_b64_tr_b16 a[246:247], v98 offset:9216\n"
    "v_add_u32 v96, s35, v96\n"
    "v_mfma_f32_16x16x32_bf16 v[4:7], a[252:255], a[28:31], v[4:7]\n"
    "v_fma_f32 v0, v0, s39, -v101\n"
    "v_fma_f32 v1, v1, s39, -v101\n"
    "v_mfma_f32_16x16x32_bf16 v[12:15], a[248:251], a[56:59], v[12:15]\n"
    "s_add_u32 m0, s34, 1792\n"
    "v_fma_f32 v2, v2, s39, -v101\n"
    "v_mfma_f32_16x16x32_bf16 v[12:15], a[252:255], a[60:63], v[12:15]\n"
    "buffer_load_dwordx4 v99, s[16:19], s36 offen offset:256 nt lds\n"
    "v_fma_f32 v3, v3, s39, -v101\n"
    "v_mfma_f32_16x16x32_bf16 v[20:23], a[248:251], a[88:91], v[20:23]\n"
    "v_exp_f32 v0, v0\n"
    "v_mfma_f32_16x16x32_bf16 v[20:23], a[252:255], a[92:95], v[20:23]\n"
    "v_exp_f32 v1, v1\n"
    "v_mfma_f32_16x16x32_bf16 v[28:31], a[248:251], a[120:123], v[28:31]\n"
    "ds_read_b64_tr_b16 a[248:249], v97 offset:1536\n"
    "ds_read_b64_tr_b16 a[250:251], v97 offset:9728\n"
    "v_mfma_f32_16x16x32_bf16 v[28:31], a[252:255], a[124:127], v[28:31]\n"
    "ds_read_b64_tr_b16 a[252:253], v98 offset:1536\n"
    "s_waitcnt lgkmcnt(11)\n"
    "v_mfma_f32_16x16x32_bf16 v[128:131], a[208:211], v[32:35], v[128:131]\n"
    "ds_read_b64_tr_b16 a[254:255], v98 offset:9728\n"
    "v_add_f32 v109, v0, v1\n"
    "v_mfma_f32_16x16x32_bf16 v[132:135], a[208:211], v[40:43], v[132:135]\n"
    "s_add_u32 m0, s34, 2688\n"
    "v_cvt_pk_bf16_f32 v0, v0, v1\n"
    "v_mfma_f32_16x16x32_bf16 v[136:139], a[208:211], v[48:51], v[136:139]\n"
    "buffer_load_dwordx4 v99, s[16:19], s36 offen offset:384 nt lds\n"
    "v_fma_f32 v4, v4, s39, -v101\n"
    "v_mfma_f32_16x16x32_bf16 v[140:143], a[208:211], v[56:59], v[140:143]\n"
    "ds_read_b64_tr_b16 a[208:209], v97 offset:2048\n"
    "ds_read_b64_tr_b16 a[210:211], v97 offset:10240\n"
    "v_mfma_f32_16x16x32_bf16 v[156:159], a[212:215], v[56:59], v[156:159]\n"
    "v_exp_f32 v2, v2\n"
    "v_mfma_f32_16x16x32_bf16 v[152:155], a[212:215], v[48:51], v[152:155]\n"
    "v_exp_f32 v3, v3\n"
    "v_mfma_f32_16x16x32_bf16 v[148:151], a[212:215], v[40:43], v[148:151]\n"
    "v_add_f32 v109, v109, v2\n"
    "v_add_f32 v109, v109, v3\n"
    "v_mfma_f32_16x16x32_bf16 v[144:147], a[212:215], v[32:35], v[144:147]\n"
    "ds_read_b64_tr_b16 a[212:213], v98 offset:2048\n"
    "s_waitcnt lgkmcnt(11)\n"
    "v_mfma_f32_16x16x32_bf16 v[160:163], a[216:219], v[32:35], v[160:163]\n"
    "ds_read_b64_tr_b16 a[214:215], v98 offset:10240\n"
    "v_cvt_pk_bf16_f32 v1, v2, v3\n"
    "v_mfma_f32_16x16x32_bf16 v[164:167], a[216:219], v[40:43], v[164:167]\n"
    "s_add_u32 m0, s34, 49152\n"
    "v_fma_f32 v5, v5, s39, -v101\n"
    "v_mfma_f32_16x16x32_bf16 v[168:171], a[216:219], v[48:51], v[168:171]\n"
    "buffer_load_dwordx4 v100, s[20:23], s36 offen offset:0 nt lds\n"
    "v_fma_f32 v6, v6, s39, -v101\n"
    "v_mfma_f32_16x16x32_bf16 v[172:175], a[216:219], v[56:59], v[172:175]\n"
    "ds_read_b64_tr_b16 a[216:217], v97 offset:2560\n"
    "ds_read_b64_tr_b16 a[218:219], v97 offset:10752\n"
    "v_mfma_f32_16x16x32_bf16 v[188:191], a[220:223], v[56:59], v[188:191]\n"
    "v_fma_f32 v7, v7, s39, -v101\n"
    "v_fma_f32 v8, v8, s39, -v102\n"
    "v_mfma_f32_16x16x32_bf16 v[184:187], a[220:223], v[48:51], v[184:187]\n"
    "v_exp_f32 v4, v4\n"
    "v_mfma_f32_16x16x32_bf16 v[180:183], a[220:223], v[40:43], v[180:183]\n"
    "v_exp_f32 v5, v5\n"
    "v_mfma_f32_16x16x32_bf16 v[176:179], a[220:223], v[32:35], v[176:179]\n"
    "ds_read_b64_tr_b16 a[220:221], v98 offset:2560\n"
    "s_waitcnt lgkmcnt(11)\n"
    "v_mfma_f32_16x16x32_bf16 v[192:195], a[240:243], v[32:35], v[192:195]\n"
    "ds_read_b64_tr_b16 a[222:223], v98 offset:10752\n"
    "v_cvt_pk_bf16_f32 v2, v4, v5\n"
    "v_mfma_f32_16x16x32_bf16 v[196:199], a[240:243], v[40:43], v[196:199]\n"
    "s_add_u32 m0, s34, 50048\n"
    "v_add_f32 v4, v109, v4\n"
    "v_mfma_f32_16x16x32_bf16 v[200:203], a[240:243], v[48:51], v[200:203]\n"
    "buffer_load_dwordx4 v100, s[20:23], s36 offen offset:128 nt lds\n"
    "v_add_f32 v4, v4, v5\n"
    "v_mfma_f32_16x16x32_bf16 v[204:207], a[240:243], v[56:59], v[204:207]\n"
    "ds_read_b64_tr_b16 a[240:241], v97 offset:3072\n"
    "ds_read_b64_tr_b16 a[242:243], v97 offset:11264\n"
    "v_mfma_f32_16x16x32_bf16 v[220:223], a[244:247], v[56:59], v[220:223]\n"
    "v_exp_f32 v6, v6\n"
    "v_mfma_f32_16x16x32_bf16 v[216:219], a[244:247], v[48:51], v[216:219]\n"
    "v_exp_f32 v7, v7\n"
    "v_mfma_f32_16x16x32_bf16 v[212:215], a[244:247], v[40:43], v[212:215]\n"
    "v_add_f32 v4, v4, v6\n"
    "v_cvt_pk_bf16_f32 v3, v6, v7\n"
    "v_mfma_f32_16x16x32_bf16 v[208:211], a[244:247], v[32:35], v[208:211]\n"
    "ds_read_b64_tr_b16 a[244:245], v98 offset:3072\n"
    "s_waitcnt lgkmcnt(11)\n"
    "v_mfma_f32_16x16x32_bf16 v[224:227], a[248:251], v[32:35], v[224:227]\n"
    "ds_read_b64_tr_b16 a[246:247], v98 offset:11264\n"
    "v_add_f32 v4, v4, v7\n"
    "v_mfma_f32_16x16x32_bf16 v[228:231], a[248:251], v[40:43], v[228:231]\n"
    "s_add_u32 m0, s34, 50944\n"
    "v_fma_f32 v9, v9, s39, -v102\n"
    "v_mfma_f32_16x16x32_bf16 v[232:235], a[248:251], v[48:51], v[232:235]\n"
    "buffer_load_dwordx4 v100, s[20:23], s36 offen offset:256 nt lds\n"
    "v_fma_f32 v10, v10, s39, -v102\n"
    "v_mfma_f32_16x16x32_bf16 v[236:239], a[248:251], v[56:59], v[236:239]\n"
    "ds_read_b64_tr_b16 a[248:249], v97 offset:3584\n"
    "ds_read_b64_tr_b16 a[250:251], v97 offset:11776\n"
    "v_mfma_f32_16x16x32_bf16 v[252:255], a[252:255], v[56:59], v[252:255]\n"
    "v_fma_f32 v11, v11, s39, -v102\n"
    "v_fma_f32 v12, v12, s39, -v102\n"
    "v_mfma_f32_16x16x32_bf16 v[248:251], a[252:255], v[48:51], v[248:251]\n"
    "v_exp_f32 v8, v8\n"
    "v_mfma_f32_16x16x32_bf16 v[244:247], a[252:255], v[40:43], v[244:247]\n"
    "v_exp_f32 v9, v9\n"
    "v_mfma_f32_16x16x32_bf16 v[240:243], a[252:255], v[32:35], v[240:243]\n"
    "ds_read_b64_tr_b16 a[252:253], v98 offset:3584\n"
    "s_waitcnt lgkmcnt(11)\n"
    "v_mfma_f32_16x16x32_bf16 a[128:131], a[208:211], v[32:35], a[128:131]\n"
    "ds_read_b64_tr_b16 a[254:255], v98 offset:11776\n"
    "v_add_f32 v109, v8, v9\n"
    "v_mfma_f32_16x16x32_bf16 a[132:135], a[208:211], v[40:43], a[132:135]\n"
    "s_add_u32 m0, s34, 51840\n"
    "v_cvt_pk_bf16_f32 v8, v8, v9\n"
    "v_mfma_f32_16x16x32_bf16 a[136:139], a[208:211], v[48:51], a[136:139]\n"
    "buffer_load_dwordx4 v100, s[20:23], s36 offen offset:384 nt lds\n"
    "v_fma_f32 v13, v13, s39, -v102\n"
    "v_mfma_f32_16x16x32_bf16 a[140:143], a[208:211], v[56:59], a[140:143]\n"
    "s_add_u32 s34, s34, s35\n"
    "s_add_u32 s56, s34, 16384\n"
    "v_mfma_f32_16x16x32_bf16 a[156:159], a[212:215], v[56:59], a[156:159]\n"
    "s_cmp_eq_u32 s56, s51\n"
    "s_cselect_b32 s35, -32768, s57\n"
    "v_mfma_f32_16x16x32_bf16 a[152:155], a[212:215], v[48:51], a[152:155]\n"
    "s_add_u32 s36, s36, 16384\n"
    "ds_read_b128 a[208:211], v96 offset:0\n"
    "v_mfma_f32_16x16x32_bf16 a[148:151], a[212:215], v[40:43], a[148:151]\n"
    "v_exp_f32 v10, v10\n"
    "v_mfma_f32_16x16x32_bf16 a[144:147], a[212:215], v[32:35], a[144:147]\n"
    "ds_read_b128 a[212:215], v96 offset:512\n"
    "s_waitcnt lgkmcnt(10)\n"
    "v_mfma_f32_16x16x32_bf16 a[160:163], a[216:219], v[32:35], a[160:163]\n"
    "v_exp_f32 v11, v11\n"
    "v_mfma_f32_16x16x32_bf16 a[164:167], a[216:219], v[40:43], a[164:167]\n"
    "v_add_f32 v109, v109, v10\n"
    "v_add_f32 v109, v109, v11\n"
    "v_mfma_f32_16x16x32_bf16 a[168:171], a[216:219], v[48:51], a[168:171]\n"
    "v_cvt_pk_bf16_f32 v9, v10, v11\n"
    "v_fma_f32 v14, v14, s39, -v102\n"
    "v_mfma_f32_16x16x32_bf16 a[172:175], a[216:219], v[56:59], a[172:175]\n"
    "ds_read_b128 a[216:219], v96 offset:8192\n"
    "v_fma_f32 v15, v15, s39, -v102\n"
    "v_mfma_f32_16x16x32_bf16 a[188:191], a[220:223], v[56:59], a[188:191]\n"
    "v_exp_f32 v12, v12\n"
    "v_mfma_f32_16x16x32_bf16 a[184:187], a[220:223], v[48:51], a[184:187]\n"
    "v_exp_f32 v13, v13\n"
    "v_mfma_f32_16x16x32_bf16 a[180:183], a[220:223], v[40:43], a[180:183]\n"
    "v_exp_f32 v14, v14\n"
    "v_mfma_f32_16x16x32_bf16 a[176:179], a[220:223], v[32:35], a[176:179]\n"
    "ds_read_b128 a[220:223], v96 offset:8704\n"
    "s_waitcnt lgkmcnt(8)\n"
    "v_mfma_f32_16x16x32_bf16 a[192:195], a[240:243], v[32:35], a[192:195]\n"
    "v_cvt_pk_bf16_f32 v10, v12, v13\n"
    "v_add_f32 v12, v109, v12\n"
    "v_mfma_f32_16x16x32_bf16 a[196:199], a[240:243], v[40:43], a[196:199]\n"
    "v_exp_f32 v15, v15\n"
    "v_mfma_f32_16x16x32_bf16 a[200:203], a[240:243], v[48:51], a[200:203]\n"
    "v_add_f32 v12, v12, v13\n"
    "v_add_f32 v12, v12, v14\n"
    "v_mfma_f32_16x16x32_bf16 a[204:207], a[240:243], v[56:59], a[204:207]\n"
    "v_cvt_pk_bf16_f32 v11, v14, v15\n"
    "v_add_f32 v12, v12, v15\n"
    "v_mfma_f32_16x16x32_bf16 v[76:79], a[244:247], v[56:59], v[76:79]\n"
    "v_fma_f32 v16, v16, s39, -v103\n"
    "v_fma_f32 v17, v17, s39, -v103\n"
    "v_mfma_f32_16x16x32_bf16 v[72:75], a[244:247], v[48:51], v[72:75]\n"
    "v_fma_f32 v18, v18, s39, -v103\n"
    "v_fma_f32 v19, v19, s39, -v103\n"
    "v_mfma_f32_16x16x32_bf16 v[68:71], a[244:247], v[40:43], v[68:71]\n"
    "v_exp_f32 v16, v16\n"
    "v_mfma_f32_16x16x32_bf16 v[64:67], a[244:247], v[32:35], v[64:67]\n"
    "v_fma_f32 v20, v20, s39, -v103\n"
    "s_waitcnt lgkmcnt(4)\n"
    "v_mfma_f32_16x16x32_bf16 a[224:227], a[248:251], v[32:35], a[224:227]\n"
    "v_exp_f32 v17, v17\n"
    "v_mfma_f32_16x16x32_bf16 a[228:231], a[248:251], v[40:43], a[228:231]\n"
    "v_exp_f32 v18, v18\n"
    "v_mfma_f32_16x16x32_bf16 a[232:235], a[248:251], v[48:51], a[232:235]\n"
    "v_exp_f32 v19, v19\n"
    "v_mfma_f32_16x16x32_bf16 a[236:239], a[248:251], v[56:59], a[236:239]\n"
    "v_add_f32 v109, v16, v17\n"
    "v_cvt_pk_bf16_f32 v16, v16, v17\n"
    "v_mfma_f32_16x16x32_bf16 v[92:95], a[252:255], v[56:59], v[92:95]\n"
    "v_add_f32 v109, v109, v18\n"
    "v_add_f32 v109, v109, v19\n"
    "v_mfma_f32_16x16x32_bf16 v[88:91], a[252:255], v[48:51], v[88:91]\n"
    "v_cvt_pk_bf16_f32 v17, v18, v19\n"
    "v_fma_f32 v21, v21, s39, -v103\n"
    "v_mfma_f32_16x16x32_bf16 v[84:87], a[252:255], v[40:43], v[84:87]\n"
    "v_fma_f32 v22, v22, s39, -v103\n"
    "v_fma_f32 v23, v23, s39, -v103\n"
    "v_mfma_f32_16x16x32_bf16 v[80:83], a[252:255], v[32:35], v[80:83]\n"
    "s_cmp_eq_u32 s37, 0\n"
    "s_cbranch_scc1 L_p15\n"
    "s_waitcnt lgkmcnt(2)\n"
    "v_mfma_f32_16x16x32_bf16 v[32:35], a[208:211], a[0:3], 0\n"
    "ds_read_b128 a[240:243], v96 offset:1024\n"
    "ds_read_b128 a[244:247], v96 offset:1536\n"
    "v_mfma_f32_16x16x32_bf16 v[32:35], a[212:215], a[4:7], v[32:35]\n"
    "ds_read_b128 a[248:251], v96 offset:9216\n"
    "ds_read_b128 a[252:255], v96 offset:9728\n"
    "v_mfma_f32_16x16x32_bf16 v[40:43], a[208:211], a[32:35], 0\n"
    "v_add_u32 v97, s35, v97\n"
    "v_add_u32 v98, s35, v98\n"
    "v_mfma_f32_16x16x32_bf16 v[40:43], a[212:215], a[36:39], v[40:43]\n"
    "v_exp_f32 v20, v20\n"
    "v_mfma_f32_16x16x32_bf16 v[48:51], a[208:211], a[64:67], 0\n"
    "v_exp_f32 v21, v21\n"
    "v_mfma_f32_16x16x32_bf16 v[48:51], a[212:215], a[68:71], v[48:51]\n"
    "v_exp_f32 v22, v22\n"
    "v_mfma_f32_16x16x32_bf16 v[56:59], a[208:211], a[96:99], 0\n"
    "ds_read_b128 a[208:211], v96 offset:2048\n"
    "v_cvt_pk_bf16_f32 v18, v20, v21\n"
    "v_mfma_f32_16x16x32_bf16 v[56:59], a[212:215], a[100:103], v[56:59]\n"
    "ds_read_b128 a[212:215], v96 offset:2560\n"
    "s_waitcnt lgkmcnt(6)\n"
    "v_mfma_f32_16x16x32_bf16 v[36:39], a[216:219], a[0:3], 0\n"
    "v_add_f32 v20, v109, v20\n"
    "v_add_f32 v20, v20, v21\n"
    "v_mfma_f32_16x16x32_bf16 v[36:39], a[220:223], a[4:7], v[36:39]\n"
    "s_and_b32 s43, s68, 16383\n"
    "s_add_u32 m0, s43, s70\n"
    "s_xor_b32 s85, s68, s87\n"
    "v_mfma_f32_16x16x32_bf16 v[44:47], a[216:219], a[32:35], 0\n"
    "buffer_load_dwordx4 v110, s[64:67], s85 offen offset:0 nt lds\n"
    "v_add_f32 v20, v20, v22\n"
    "v_mfma_f32_16x16x32_bf16 v[44:47], a[220:223], a[36:39], v[44:47]\n"
    "v_exp_f32 v23, v23\n"
    "v_mfma_f32_16x16x32_bf16 v[52:55], a[216:219], a[64:67], 0\n"
    "v_cvt_pk_bf16_f32 v19, v22, v23\n"
    "v_add_f32 v20, v20, v23\n"
    "v_mfma_f32_16x16x32_bf16 v[52:55], a[220:223], a[68:71], v[52:55]\n"
    "s_add_u32 s68, s68, 2048\n"
    "s_and_b32 s68, s68, 0xffff\n"
    "v_mfma_f32_16x16x32_bf16 v[60:63], a[216:219], a[96:99], 0\n"
    "buffer_load_dwordx4 v111, s[64:67], s85 offen offset:1024 nt lds\n"
    "ds_read_b128 a[216:219], v96 offset:10240\n"
    "v_mfma_f32_16x16x32_bf16 v[60:63], a[220:223], a[100:103], v[60:63]\n"
    "ds_read_b128 a[220:223], v96 offset:10752\n"
    "s_waitcnt lgkmcnt(6)\n"
    "v_mfma_f32_16x16x32_bf16 v[32:35], a[240:243], a[8:11], v[32:35]\n"
    "v_fma_f32 v24, v24, s39, -v104\n"
    "v_fma_f32 v25, v25, s39, -v104\n"
    "v_mfma_f32_16x16x32_bf16 v[32:35], a[244:247], a[12:15], v[32:35]\n"
    "v_fma_f32 v26, v26, s39, -v104\n"
    "v_fma_f32 v27, v27, s39, -v104\n"
    "v_mfma_f32_16x16x32_bf16 v[40:43], a[240:243], a[40:43], v[40:43]\n"
    "v_exp_f32 v24, v24\n"
    "v_mfma_f32_16x16x32_bf16 v[40:43], a[244:247], a[44:47], v[40:43]\n"
    "v_exp_f32 v25, v25\n"
    "v_mfma_f32_16x16x32_bf16 v[48:51], a[240:243], a[72:75], v[48:51]\n"
    "v_exp_f32 v26, v26\n"
    "v_mfma_f32_16x16x32_bf16 v[48:51], a[244:247], a[76:79], v[48:51]\n"
    "v_exp_f32 v27, v27\n"
    "v_mfma_f32_16x16x32_bf16 v[56:59], a[240:243], a[104:107], v[56:59]\n"
    "ds_read_b128 a[240:243], v96 offset:3072\n"
    "v_add_f32 v109, v24, v25\n"
    "v_mfma_f32_16x16x32_bf16 v[56:59], a[244:247], a[108:111], v[56:59]\n"
    "ds_read_b128 a[244:247], v96 offset:3584\n"
    "s_waitcnt lgkmcnt(6)\n"
    "v_mfma_f32_16x16x32_bf16 v[36:39], a[248:251], a[8:11], v[36:39]\n"
    "v_cvt_pk_bf16_f32 v24, v24, v25\n"
    "v_add_f32 v109, v109, v26\n"
    "v_mfma_f32_16x16x32_bf16 v[36:39], a[252:255], a[12:15], v[36:39]\n"
    "v_add_f32 v109, v109, v27\n"
    "v_cvt_pk_bf16_f32 v25, v26, v27\n"
    "v_mfma_f32_16x16x32_bf16 v[44:47], a[248:251], a[40:43], v[44:47]\n"
    "v_fma_f32 v28, v28, s39, -v104\n"
    "v_fma_f32 v29, v29, s39, -v104\n"
    "v_mfma_f32_16x16x32_bf16 v[44:47], a[252:255], a[44:47], v[44:47]\n"
    "v_fma_f32 v30, v30, s39, -v104\n"
    "v_fma_f32 v31, v31, s39, -v104\n"
    "v_mfma_f32_16x16x32_bf16 v[52:55], a[248:251], a[72:75], v[52:55]\n"
    "v_exp_f32 v28, v28\n"
    "v_mfma_f32_16x16x32_bf16 v[52:55], a[252:255], a[76:79], v[52:55]\n"
    "v_exp_f32 v29, v29\n"
    "v_mfma_f32_16x16x32_bf16 v[60:63], a[248:251], a[104:107], v[60:63]\n"
    "ds_read_b128 a[248:251], v96 offset:11264\n"
    "v_cvt_pk_bf16_f32 v26, v28, v29\n"
    "v_mfma_f32_16x16x32_bf16 v[60:63], a[252:255], a[108:111], v[60:63]\n"
    "ds_read_b128 a[252:255], v96 offset:11776\n"
    "s_waitcnt lgkmcnt(6)\n"
    "v_mfma_f32_16x16x32_bf16 v[32:35], a[208:211], a[16:19], v[32:35]\n"
    "v_exp_f32 v30, v30\n"
    "v_mfma_f32_16x16x32_bf16 v[32:35], a[212:215], a[20:23], v[32:35]\n"
    "v_add_f32 v28, v109, v28\n"
    "v_add_f32 v28, v28, v29\n"
    "v_mfma_f32_16x16x32_bf16 v[40:43], a[208:211], a[48:51], v[40:43]\n"
    "v_exp_f32 v31, v31\n"
    "v_mfma_f32_16x16x32_bf16 v[40:43], a[212:215], a[52:55], v[40:43]\n"
    "v_max3_f32 v109, v4, v12, v20\n"
    "v_add_f32 v28, v28, v30\n"
    "v_mfma_f32_16x16x32_bf16 v[48:51], a[208:211], a[80:83], v[48:51]\n"
    "v_cvt_pk_bf16_f32 v27, v30, v31\n"
    "v_add_f32 v28, v28, v31\n"
    "v_mfma_f32_16x16x32_bf16 v[48:51], a[212:215], a[84:87], v[48:51]\n"
    "v_max_f32 v109, v109, v28\n"
    "v_cmp_le_f32 vcc, 1.0, v109\n"
    "v_mfma_f32_16x16x32_bf16 v[56:59], a[208:211], a[112:115], v[56:59]\n"
    "v_mfma_f32_16x16x32_bf16 v[56:59], a[212:215], a[116:119], v[56:59]\n"
    "s_waitcnt lgkmcnt(4)\n"
    "v_mfma_f32_16x16x32_bf16 v[36:39], a[216:219], a[16:19], v[36:39]\n"
    "v_mfma_f32_16x16x32_bf16 v[36:39], a[220:223], a[20:23], v[36:39]\n"
    "v_mfma_f32_16x16x32_bf16 v[44:47], a[216:219], a[48:51], v[44:47]\n"
    "v_mfma_f32_16x16x32_bf16 v[44:47], a[220:223], a[52:55], v[44:47]\n"
    "v_mfma_f32_16x16x32_bf16 v[52:55], a[216:219], a[80:83], v[52:55]\n"
    "s_waitcnt vmcnt(12) lgkmcnt(0)\n"
    "s_barrier\n"
    "v_mfma_f32_16x16x32_bf16 v[52:55], a[220:223], a[84:87], v[52:55]\n"
    "ds_read_b64_tr_b16 a[208:209], v97 offset:0\n"
    "ds_read_b64_tr_b16 a[210:211], v97 offset:8192\n"
    "v_mfma_f32_16x16x32_bf16 v[60:63], a[216:219], a[112:115], v[60:63]\n"
    "s_add_u32 m0, s34, 0\n"
    "ds_read_b64_tr_b16 a[212:213], v98 offset:0\n"
    "v_mfma_f32_16x16x32_bf16 v[60:63], a[220:223], a[116:119], v[60:63]\n"
    "buffer_load_dwordx4 v99, s[16:19], s36 offen offset:0 nt lds\n"
    "v_mfma_f32_16x16x32_bf16 v[32:35], a[240:243], a[24:27], v[32:35]\n"
    "ds_read_b64_tr_b16 a[214:215], v98 offset:8192\n"
    "ds_read_b64_tr_b16 a[216:217], v97 offset:512\n"
    "v_mfma_f32_16x16x32_bf16 v[32:35], a[244:247], a[28:31], v[32:35]\n"
    "ds_read_b64_tr_b16 a[218:219], v97 offset:8704\n"
    "ds_read_b64_tr_b16 a[220:221], v98 offset:512\n"
    "v_mfma_f32_16x16x32_bf16 v[40:43], a[240:243], a[56:59], v[40:43]\n"
    "s_cbranch_vccnz L_ra3\n"
    "L_ra3_ret:\n"
    "v_add_f32 v105, v105, v4\n"
    "v_mfma_f32_16x16x32_bf16 v[40:43], a[244:247], a[60:63], v[40:43]\n"
    "v_add_f32 v106, v106, v12\n"
    "v_add_f32 v107, v107, v20\n"
    "v_mfma_f32_16x16x32_bf16 v[48:51], a[240:243], a[88:91], v[48:51]\n"
    "s_add_u32 m0, s34, 896\n"
    "v_add_f32 v108, v108, v28\n"
    "v_mfma_f32_16x16x32_bf16 v[48:51], a[244:247], a[92:95], v[48:51]\n"
    "buffer_load_dwordx4 v99, s[16:19], s36 offen offset:128 nt lds\n"
    "ds_read_b64_tr_b16 a[222:223], v98 offset:8704\n"
    "v_mfma_f32_16x16x32_bf16 v[56:59], a[240:243], a[120:123], v[56:59]\n"
    "ds_read_b64_tr_b16 a[240:241], v97 offset:1024\n"
    "ds_read_b64_tr_b16 a[242:243], v97 offset:9216\n"
    "v_mfma_f32_16x16x32_bf16 v[56:59], a[244:247], a[124:127], v[56:59]\n"
    "ds_read_b64_tr_b16 a[244:245], v98 offset:1024\n"
    "v_mfma_f32_16x16x32_bf16 v[36:39], a[248:251], a[24:27], v[36:39]\n"
    "ds_read_b64_tr_b16 a[246:247], v98 offset:9216\n"
    "v_add_u32 v96, s35, v96\n"
    "v_mfma_f32_16x16x32_bf16 v[36:39], a[252:255], a[28:31], v[36:39]\n"
    "v_fma_f32 v32, v32, s39, -v101\n"
    "v_fma_f32 v33, v33, s39, -v101\n"
    "v_mfma_f32_16x16x32_bf16 v[44:47], a[248:251], a[56:59], v[44:47]\n"
    "s_add_u32 m0, s34, 1792\n"
    "v_fma_f32 v34, v34, s39, -v101\n"
    "v_mfma_f32_16x16x32_bf16 v[44:47], a[252:255], a[60:63], v[44:47]\n"
    "buffer_load_dwordx4 v99, s[16:19], s36 offen offset:256 nt lds\n"
    "v_fma_f32 v35, v35, s39, -v101\n"
    "v_mfma_f32_16x16x32_bf16 v[52:55], a[248:251], a[88:91], v[52:55]\n"
    "v_exp_f32 v32, v32\n"
    "v_mfma_f32_16x16x32_bf16 v[52:55], a[252:255], a[92:95], v[52:55]\n"
    "v_exp_f32 v33, v33\n"
    "v_mfma_f32_16x16x32_bf16 v[60:63], a[248:251], a[120:123], v[60:63]\n"
    "ds_read_b64_tr_b16 a[248:249], v97 offset:1536\n"
    "ds_read_b64_tr_b16 a[250:251], v97 offset:9728\n"
    "v_mfma_f32_16x16x32_bf16 v[60:63], a[252:255], a[124:127], v[60:63]\n"
    "ds_read_b64_tr_b16 a[252:253], v98 offset:1536\n"
    "s_waitcnt lgkmcnt(11)\n"
    "v_mfma_f32_16x16x32_bf16 v[128:131], a[208:211], v[0:3], v[128:131]\n"
    "ds_read_b64_tr_b16 a[254:255], v98 offset:9728\n"
    "v_add_f32 v109, v32, v33\n"
    "v_mfma_f32_16x16x32_bf16 v[132:135], a[208:211], v[8:11], v[132:135]\n"
    "s_add_u32 m0, s34, 2688\n"
    "v_cvt_pk_bf16_f32 v32, v32, v33\n"
    "v_mfma_f32_16x16x32_bf16 v[136:139], a[208:211], v[16:19], v[136:139]\n"
    "buffer_load_dwordx4 v99, s[16:19], s36 offen offset:384 nt lds\n"
    "v_fma_f32 v36, v36, s39, -v101\n"
    "v_mfma_f32_16x16x32_bf16 v[140:143], a[208:211], v[24:27], v[140:143]\n"
    "ds_read_b64_tr_b16 a[208:209], v97 offset:2048\n"
    "ds_read_b64_tr_b16 a[210:211], v97 offset:10240\n"
    "v_mfma_f32_16x16x32_bf16 v[156:159], a[212:215], v[24:27], v[156:159]\n"
    "v_exp_f32 v34, v34\n"
    "v_mfma_f32_16x16x32_bf16 v[152:155], a[212:215], v[16:19], v[152:155]\n"
    "v_exp_f32 v35, v35\n"
    "v_mfma_f32_16x16x32_bf16 v[148:151], a[212:215], v[8:11], v[148:151]\n"
    "v_add_f32 v109, v109, v34\n"
    "v_add_f32 v109, v109, v35\n"
    "v_mfma_f32_16x16x32_bf16 v[144:147], a[212:215], v[0:3], v[144:147]\n"
    "ds_read_b64_tr_b16 a[212:213], v98 offset:2048\n"
    "s_waitcnt lgkmcnt(11)\n"
    "v_mfma_f32_16x16x32_bf16 v[160:163], a[216:219], v[0:3], v[160:163]\n"
    "ds_read_b64_tr_b16 a[214:215], v98 offset:10240\n"
    "v_cvt_pk_bf16_f32 v33, v34, v35\n"
    "v_mfma_f32_16x16x32_bf16 v[164:167], a[216:219], v[8:11], v[164:167]\n"
    "s_add_u32 m0, s34, 49152\n"
    "v_fma_f32 v37, v37, s39, -v101\n"
    "v_mfma_f32_16x16x32_bf16 v[168:171], a[216:219], v[16:19], v[168:171]\n"
    "buffer_load_dwordx4 v100, s[20:23], s36 offen offset:0 nt lds\n"
    "v_fma_f32 v38, v38, s39, -v101\n"
    "v_mfma_f32_16x16x32_bf16 v[172:175], a[216:219], v[24:27], v[172:175]\n"
    "ds_read_b64_tr_b16 a[216:217], v97 offset:2560\n"
    "ds_read_b64_tr_b16 a[218:219], v97 offset:10752\n"
    "v_mfma_f32_16x16x32_bf16 v[188:191], a[220:223], v[24:27], v[188:191]\n"
    "v_fma_f32 v39, v39, s39, -v101\n"
    "v_fma_f32 v40, v40, s39, -v102\n"
    "v_mfma_f32_16x16x32_bf16 v[184:187], a[220:223], v[16:19], v[184:187]\n"
    "v_exp_f32 v36, v36\n"
    "v_mfma_f32_16x16x32_bf16 v[180:183], a[220:223], v[8:11], v[180:183]\n"
    "v_exp_f32 v37, v37\n"
    "v_mfma_f32_16x16x32_bf16 v[176:179], a[220:223], v[0:3], v[176:179]\n"
    "ds_read_b64_tr_b16 a[220:221], v98 offset:2560\n"
    "s_waitcnt lgkmcnt(11)\n"
    "v_mfma_f32_16x16x32_bf16 v[192:195], a[240:243], v[0:3], v[192:195]\n"
    "ds_read_b64_tr_b16 a[222:223], v98 offset:10752\n"
    "v_cvt_pk_bf16_f32 v34, v36, v37\n"
    "v_mfma_f32_16x16x32_bf16 v[196:199], a[240:243], v[8:11], v[196:199]\n"
    "s_add_u32 m0, s34, 50048\n"
    "v_add_f32 v36, v109, v36\n"
    "v_mfma_f32_16x16x32_bf16 v[200:203], a[240:243], v[16:19], v[200:203]\n"
    "buffer_load_dwordx4 v100, s[20:23], s36 offen offset:128 nt lds\n"
    "v_add_f32 v36, v36, v37\n"
    "v_mfma_f32_16x16x32_bf16 v[204:207], a[240:243], v[24:27], v[204:207]\n"
    "ds_read_b64_tr_b16 a[240:241], v97 offset:3072\n"
    "ds_read_b64_tr_b16 a[242:243], v97 offset:11264\n"
    "v_mfma_f32_16x16x32_bf16 v[220:223], a[244:247], v[24:27], v[220:223]\n"
    "v_exp_f32 v38, v38\n"
    "v_mfma_f32_16x16x32_bf16 v[216:219], a[244:247], v[16:19], v[216:219]\n"
    "v_exp_f32 v39, v39\n"
    "v_mfma_f32_16x16x32_bf16 v[212:215], a[244:247], v[8:11], v[212:215]\n"
    "v_add_f32 v36, v36, v38\n"
    "v_cvt_pk_bf16_f32 v35, v38, v39\n"
    "v_mfma_f32_16x16x32_bf16 v[208:211], a[244:247], v[0:3], v[208:211]\n"
    "ds_read_b64_tr_b16 a[244:245], v98 offset:3072\n"
    "s_waitcnt lgkmcnt(11)\n"
    "v_mfma_f32_16x16x32_bf16 v[224:227], a[248:251], v[0:3], v[224:227]\n"
    "ds_read_b64_tr_b16 a[246:247], v98 offset:11264\n"
    "v_add_f32 v36, v36, v39\n"
    "v_mfma_f32_16x16x32_bf16 v[228:231], a[248:251], v[8:11], v[228:231]\n"
    "s_add_u32 m0, s34, 50944\n"
    "v_fma_f32 v41, v41, s39, -v102\n"
    "v_mfma_f32_16x16x32_bf16 v[232:235], a[248:251], v[16:19], v[232:235]\n"
    "buffer_load_dwordx4 v100, s[20:23], s36 offen offset:256 nt lds\n"
    "v_fma_f32 v42, v42, s39, -v102\n"
    "v_mfma_f32_16x16x32_bf16 v[236:239], a[248:251], v[24:27], v[236:239]\n"
    "ds_read_b64_tr_b16 a[248:249], v97 offset:3584\n"
    "ds_read_b64_tr_b16 a[250:251], v97 offset:11776\n"
    "v_mfma_f32_16x16x32_bf16 v[252:255], a[252:255], v[24:27], v[252:255]\n"
    "v_fma_f32 v43, v43, s39, -v102\n"
    "v_fma_f32 v44, v44, s39, -v102\n"
    "v_mfma_f32_16x16x32_bf16 v[248:251], a[252:255], v[16:19], v[248:251]\n"
    "v_exp_f32 v40, v40\n"
    "v_mfma_f32_16x16x32_bf16 v[244:247], a[252:255], v[8:11], v[244:247]\n"
    "v_exp_f32 v41, v41\n"
    "v_mfma_f32_16x16x32_bf16 v[240:243], a[252:255], v[0:3], v[240:243]\n"
    "ds_read_b64_tr_b16 a[252:253], v98 offset:3584\n"
    "s_waitcnt lgkmcnt(11)\n"
    "v_mfma_f32_16x16x32_bf16 a[128:131], a[208:211], v[0:3], a[128:131]\n"
    "ds_read_b64_tr_b16 a[254:255], v98 offset:11776\n"
    "v_add_f32 v109, v40, v41\n"
    "v_mfma_f32_16x16x32_bf16 a[132:135], a[208:211], v[8:11], a[132:135]\n"
    "s_add_u32 m0, s34, 51840\n"
    "v_cvt_pk_bf16_f32 v40, v40, v41\n"
    "v_mfma_f32_16x16x32_bf16 a[136:139], a[208:211], v[16:19], a[136:139]\n"
    "buffer_load_dwordx4 v100, s[20:23], s36 offen offset:384 nt lds\n"
    "v_fma_f32 v45, v45, s39, -v102\n"
    "v_mfma_f32_16x16x32_bf16 a[140:143], a[208:211], v[24:27], a[140:143]\n"
    "s_add_u32 s34, s34, s35\n"
    "s_add_u32 s56, s34, 16384\n"
    "v_mfma_f32_16x16x32_bf16 a[156:159], a[212:215], v[24:27], a[156:159]\n"
    "s_cmp_eq_u32 s56, s51\n"
    "s_cselect_b32 s35, -32768, s57\n"
    "v_mfma_f32_16x16x32_bf16 a[152:155], a[212:215], v[16:19], a[152:155]\n"
    "s_add_u32 s36, s36, 16384\n"
    "ds_read_b128 a[208:211], v96 offset:0\n"
    "v_mfma_f32_16x16x32_bf16 a[148:151], a[212:215], v[8:11], a[148:151]\n"
    "v_exp_f32 v42, v42\n"
    "v_mfma_f32_16x16x32_bf16 a[144:147], a[212:215], v[0:3], a[144:147]\n"
    "ds_read_b128 a[212:215], v96 offset:512\n"
    "s_waitcnt lgkmcnt(10)\n"
    "v_mfma_f32_16x16x32_bf16 a[160:163], a[216:219], v[0:3], a[160:163]\n"
    "v_exp_f32 v43, v43\n"
    "v_mfma_f32_16x16x32_bf16 a[164:167], a[216:219], v[8:11], a[164:167]\n"
    "v_add_f32 v109, v109, v42\n"
    "v_add_f32 v109, v109, v43\n"
    "v_mfma_f32_16x16x32_bf16 a[168:171], a[216:219], v[16:19], a[168:171]\n"
    "v_cvt_pk_bf16_f32 v41, v42, v43\n"
    "v_fma_f32 v46, v46, s39, -v102\n"
    "v_mfma_f32_16x16x32_bf16 a[172:175], a[216:219], v[24:27], a[172:175]\n"
    "ds_read_b128 a[216:219], v96 offset:8192\n"
    "v_fma_f32 v47, v47, s39, -v102\n"
    "v_mfma_f32_16x16x32_bf16 a[188:191], a[220:223], v[24:27], a[188:191]\n"
    "v_exp_f32 v44, v44\n"
    "v_mfma_f32_16x16x32_bf16 a[184:187], a[220:223], v[16:19], a[184:187]\n"
    "v_exp_f32 v45, v45\n"
    "v_mfma_f32_16x16x32_bf16 a[180:183], a[220:223], v[8:11], a[180:183]\n"
    "v_exp_f32 v46, v46\n"
    "v_mfma_f32_16x16x32_bf16 a[176:179], a[220:223], v[0:3], a[176:179]\n"
    "ds_read_b128 a[220:223], v96 offset:8704\n"
    "s_waitcnt lgkmcnt(8)\n"
    "v_mfma_f32_16x16x32_bf16 a[192:195], a[240:243], v[0:3], a[192:195]\n"
    "v_cvt_pk_bf16_f32 v42, v44, v45\n"
    "v_add_f32 v44, v109, v44\n"
    "v_mfma_f32_16x16x32_bf16 a[196:199], a[240:243], v[8:11], a[196:199]\n"
    "v_exp_f32 v47, v47\n"
    "v_mfma_f32_16x16x32_bf16 a[200:203], a[240:243], v[16:19], a[200:203]\n"
    "v_add_f32 v44, v44, v45\n"
    "v_add_f32 v44, v44, v46\n"
    "v_mfma_f32_16x16x32_bf16 a[204:207], a[240:243], v[24:27], a[204:207]\n"
    "v_cvt_pk_bf16_f32 v43, v46, v47\n"
    "v_add_f32 v44, v44, v47\n"
    "v_mfma_f32_16x16x32_bf16 v[76:79], a[244:247], v[24:27], v[76:79]\n"
    "v_fma_f32 v48, v48, s39, -v103\n"
    "v_fma_f32 v49, v49, s39, -v103\n"
    "v_mfma_f32_16x16x32_bf16 v[72:75], a[244:247], v[16:19], v[72:75]\n"
    "v_fma_f32 v50, v50, s39, -v103\n"
    "v_fma_f32 v51, v51, s39, -v103\n"
    "v_mfma_f32_16x16x32_bf16 v[68:71], a[244:247], v[8:11], v[68:71]\n"
    "v_exp_f32 v48, v48\n"
    "v_mfma_f32_16x16x32_bf16 v[64:67], a[244:247], v[0:3], v[64:67]\n"
    "v_fma_f32 v52, v52, s39, -v103\n"
    "s_waitcnt lgkmcnt(4)\n"
    "v_mfma_f32_16x16x32_bf16 a[224:227], a[248:251], v[0:3], a[224:227]\n"
    "v_exp_f32 v49, v49\n"
    "v_mfma_f32_16x16x32_bf16 a[228:231], a[248:251], v[8:11], a[228:231]\n"
    "v_exp_f32 v50, v50\n"
    "v_mfma_f32_16x16x32_bf16 a[232:235], a[248:251], v[16:19], a[232:235]\n"
    "v_exp_f32 v51, v51\n"
    "v_mfma_f32_16x16x32_bf16 a[236:239], a[248:251], v[24:27], a[236:239]\n"
    "v_add_f32 v109, v48, v49\n"
    "v_cvt_pk_bf16_f32 v48, v48, v49\n"
    "v_mfma_f32_16x16x32_bf16 v[92:95], a[252:255], v[24:27], v[92:95]\n"
    "v_add_f32 v109, v109, v50\n"
    "v_add_f32 v109, v109, v51\n"
    "v_mfma_f32_16x16x32_bf16 v[88:91], a[252:255], v[16:19], v[88:91]\n"
    "v_cvt_pk_bf16_f32 v49, v50, v51\n"
    "v_fma_f32 v53, v53, s39, -v103\n"
    "v_mfma_f32_16x16x32_bf16 v[84:87], a[252:255], v[8:11], v[84:87]\n"
    "v_fma_f32 v54, v54, s39, -v103\n"
    "v_fma_f32 v55, v55, s39, -v103\n"
    "v_mfma_f32_16x16x32_bf16 v[80:83], a[252:255], v[0:3], v[80:83]\n"
    "s_sub_u32 s37, s37, 1\n"
    "s_cbranch_scc0 L_loop0\n"
    "L_p15:\n"
    "s_waitcnt lgkmcnt(2)\n"
    "v_mfma_f32_16x16x32_bf16 v[32:35], a[208:211], a[0:3], 0\n"
    "ds_read_b128 a[240:243], v96 offset:1024\n"
    "ds_read_b128 a[244:247], v96 offset:1536\n"
    "v_mfma_f32_16x16x32_bf16 v[32:35], a[212:215], a[4:7], v[32:35]\n"
    "ds_read_b128 a[248:251], v96 offset:9216\n"
    "ds_read_b128 a[252:255], v96 offset:9728\n"
    "v_add_u32 v118, s70, v123\n"
    "v_mfma_f32_16x16x32_bf16 v[40:43], a[208:211], a[32:35], 0\n"
    "v_xor_b32 v119, 64, v118\n"
    "v_xor_b32 v120, 128, v118\n"
    "v_xor_b32 v121, 192, v118\n"
    "v_mfma_f32_16x16x32_bf16 v[40:43], a[212:215], a[36:39], v[40:43]\n"
    "s_waitcnt vmcnt(8)\n"
    "ds_read_b128 a[208:211], v96 offset:2048\n"
    "s_waitcnt lgkmcnt(5)\n"
    "v_mfma_f32_16x16x32_bf16 v[36:39], a[216:219], a[0:3], 0\n"
    "ds_read_b128 a[0:3], v118 offset:0\n"
    "ds_read_b128 a[212:215], v96 offset:2560\n"
    "v_fma_f32 v24, v24, s39, -v104\n"
    "v_mfma_f32_16x16x32_bf16 v[36:39], a[220:223], a[4:7], v[36:39]\n"
    "v_exp_f32 v20, v20\n"
    "v_fma_f32 v25, v25, s39, -v104\n"
    "v_mfma_f32_16x16x32_bf16 v[44:47], a[216:219], a[32:35], 0\n"
    "ds_read_b128 a[32:35], v118 offset:8192\n"
    "ds_read_b128 a[4:7], v119 offset:0\n"
    "ds_read_b128 a[216:219], v96 offset:10240\n"
    "v_mfma_f32_16x16x32_bf16 v[44:47], a[220:223], a[36:39], v[44:47]\n"
    "ds_read_b128 a[36:39], v119 offset:8192\n"
    "ds_read_b128 a[220:223], v96 offset:10752\n"
    "s_waitcnt lgkmcnt(10)\n"
    "v_mfma_f32_16x16x32_bf16 v[32:35], a[240:243], a[8:11], v[32:35]\n"
    "v_exp_f32 v21, v21\n"
    "v_fma_f32 v26, v26, s39, -v104\n"
    "v_mfma_f32_16x16x32_bf16 v[32:35], a[244:247], a[12:15], v[32:35]\n"
    "v_exp_f32 v22, v22\n"
    "v_mfma_f32_16x16x32_bf16 v[40:43], a[240:243], a[40:43], v[40:43]\n"
    "ds_read_b128 a[240:243], v96 offset:3072\n"
    "v_cvt_pk_bf16_f32 v18, v20, v21\n"
    "v_add_f32 v20, v109, v20\n"
    "v_mfma_f32_16x16x32_bf16 v[40:43], a[244:247], a[44:47], v[40:43]\n"
    "ds_read_b128 a[244:247], v96 offset:3584\n"
    "v_add_f32 v20, v20, v21\n"
    "s_waitcnt lgkmcnt(10)\n"
    "v_mfma_f32_16x16x32_bf16 v[36:39], a[248:251], a[8:11], v[36:39]\n"
    "ds_read_b128 a[8:11], v120 offset:0\n"
    "v_exp_f32 v23, v23\n"
    "v_mfma_f32_16x16x32_bf16 v[36:39], a[252:255], a[12:15], v[36:39]\n"
    "v_add_f32 v20, v20, v22\n"
    "v_cvt_pk_bf16_f32 v19, v22, v23\n"
    "v_add_f32 v20, v20, v23\n"
    "v_mfma_f32_16x16x32_bf16 v[44:47], a[248:251], a[40:43], v[44:47]\n"
    "ds_read_b128 a[40:43], v120 offset:8192\n"
    "ds_read_b128 a[12:15], v121 offset:0\n"
    "ds_read_b128 a[248:251], v96 offset:11264\n"
    "v_mfma_f32_16x16x32_bf16 v[44:47], a[252:255], a[44:47], v[44:47]\n"
    "ds_read_b128 a[44:47], v121 offset:8192\n"
    "ds_read_b128 a[252:255], v96 offset:11776\n"
    "s_waitcnt lgkmcnt(13)\n"
    "v_mfma_f32_16x16x32_bf16 v[32:35], a[208:211], a[16:19], v[32:35]\n"
    "v_fma_f32 v27, v27, s39, -v104\n"
    "v_exp_f32 v24, v24\n"
    "v_mfma_f32_16x16x32_bf16 v[32:35], a[212:215], a[20:23], v[32:35]\n"
    "v_exp_f32 v25, v25\n"
    "v_fma_f32 v28, v28, s39, -v104\n"
    "v_mfma_f32_16x16x32_bf16 v[40:43], a[208:211], a[48:51], v[40:43]\n"
    "ds_read_b128 a[208:211], v96 offset:0\n"
    "v_add_f32 v109, v24, v25\n"
    "v_mfma_f32_16x16x32_bf16 v[40:43], a[212:215], a[52:55], v[40:43]\n"
    "ds_read_b128 a[212:215], v96 offset:512\n"
    "v_cvt_pk_bf16_f32 v24, v24, v25\n"
    "s_waitcnt lgkmcnt(10)\n"
    "v_mfma_f32_16x16x32_bf16 v[36:39], a[216:219], a[16:19], v[36:39]\n"
    "ds_read_b128 a[16:19], v118 offset:256\n"
    "v_exp_f32 v26, v26\n"
    "v_mfma_f32_16x16x32_bf16 v[36:39], a[220:223], a[20:23], v[36:39]\n"
    "v_exp_f32 v27, v27\n"
    "v_add_f32 v109, v109, v26\n"
    "v_mfma_f32_16x16x32_bf16 v[44:47], a[216:219], a[48:51], v[44:47]\n"
    "ds_read_b128 a[48:51], v118 offset:8448\n"
    "ds_read_b128 a[20:23], v119 offset:256\n"
    "ds_read_b128 a[216:219], v96 offset:8192\n"
    "v_mfma_f32_16x16x32_bf16 v[44:47], a[220:223], a[52:55], v[44:47]\n"
    "ds_read_b128 a[52:55], v119 offset:8448\n"
    "ds_read_b128 a[220:223], v96 offset:8704\n"
    "s_waitcnt lgkmcnt(14)\n"
    "v_mfma_f32_16x16x32_bf16 v[32:35], a[240:243], a[24:27], v[32:35]\n"
    "v_add_f32 v109, v109, v27\n"
    "v_cvt_pk_bf16_f32 v25, v26, v27\n"
    "v_fma_f32 v29, v29, s39, -v104\n"
    "v_mfma_f32_16x16x32_bf16 v[32:35], a[244:247], a[28:31], v[32:35]\n"
    "v_fma_f32 v30, v30, s39, -v104\n"
    "v_fma_f32 v31, v31, s39, -v104\n"
    "v_mfma_f32_16x16x32_bf16 v[40:43], a[240:243], a[56:59], v[40:43]\n"
    "ds_read_b128 a[240:243], v96 offset:1024\n"
    "v_exp_f32 v28, v28\n"
    "v_mfma_f32_16x16x32_bf16 v[40:43], a[244:247], a[60:63], v[40:43]\n"
    "ds_read_b128 a[244:247], v96 offset:1536\n"
    "v_exp_f32 v29, v29\n"
    "s_waitcnt lgkmcnt(10)\n"
    "v_mfma_f32_16x16x32_bf16 v[36:39], a[248:251], a[24:27], v[36:39]\n"
    "ds_read_b128 a[24:27], v120 offset:256\n"
    "v_cvt_pk_bf16_f32 v26, v28, v29\n"
    "v_mfma_f32_16x16x32_bf16 v[36:39], a[252:255], a[28:31], v[36:39]\n"
    "v_exp_f32 v30, v30\n"
    "v_add_f32 v28, v109, v28\n"
    "v_mfma_f32_16x16x32_bf16 v[44:47], a[248:251], a[56:59], v[44:47]\n"
    "ds_read_b128 a[56:59], v120 offset:8448\n"
    "ds_read_b128 a[28:31], v121 offset:256\n"
    "ds_read_b128 a[248:251], v96 offset:9216\n"
    "v_mfma_f32_16x16x32_bf16 v[44:47], a[252:255], a[60:63], v[44:47]\n"
    "ds_read_b128 a[60:63], v121 offset:8448\n"
    "ds_read_b128 a[252:255], v96 offset:9728\n"
    "s_waitcnt lgkmcnt(14)\n"
    "v_mfma_f32_16x16x32_bf16 v[48:51], a[208:211], a[64:67], 0\n"
    "v_exp_f32 v31, v31\n"
    "v_add_f32 v28, v28, v29\n"
    "v_mfma_f32_16x16x32_bf16 v[48:51], a[212:215], a[68:71], v[48:51]\n"
    "v_max3_f32 v109, v4, v12, v20\n"
    "v_add_f32 v28, v28, v30\n"
    "v_cvt_pk_bf16_f32 v27, v30, v31\n"
    "v_mfma_f32_16x16x32_bf16 v[56:59], a[208:211], a[96:99], 0\n"
    "ds_read_b128 a[208:211], v96 offset:2048\n"
    "v_add_f32 v28, v28, v31\n"
    "v_max_f32 v109, v109, v28\n"
    "v_mfma_f32_16x16x32_bf16 v[56:59], a[212:215], a[100:103], v[56:59]\n"
    "ds_read_b128 a[212:215], v96 offset:2560\n"
    "v_cmp_le_f32 vcc, 1.0, v109\n"
    "s_waitcnt lgkmcnt(10)\n"
    "v_mfma_f32_16x16x32_bf16 v[52:55], a[216:219], a[64:67], 0\n"
    "s_waitcnt lgkmcnt(3)\n"
    "s_cbranch_vccnz L_ra4\n"
    "L_ra4_ret:\n"
    "v_add_f32 v105, v105, v4\n"
    "v_mfma_f32_16x16x32_bf16 v[52:55], a[220:223], a[68:71], v[52:55]\n"
    "s_xor_b32 s78, s87, 16384\n"
    "s_add_u32 m0, s70, 0\n"
    "v_mfma_f32_16x16x32_bf16 v[60:63], a[216:219], a[96:99], 0\n"
    "buffer_load_dwordx4 v110, s[64:67], s78 offen offset:0 nt lds\n"
    "ds_read_b128 a[216:219], v96 offset:10240\n"
    "v_add_f32 v106, v106, v12\n"
    "v_mfma_f32_16x16x32_bf16 v[60:63], a[220:223], a[100:103], v[60:63]\n"
    "ds_read_b128 a[220:223], v96 offset:10752\n"
    "v_add_f32 v107, v107, v20\n"
    "v_mfma_f32_16x16x32_bf16 v[48:51], a[240:243], a[72:75], v[48:51]\n"
    "v_add_f32 v108, v108, v28\n"
    "v_fma_f32 v32, v32, s39, -v101\n"
    "v_fma_f32 v33, v33, s39, -v101\n"
    "v_mfma_f32_16x16x32_bf16 v[48:51], a[244:247], a[76:79], v[48:51]\n"
    "v_fma_f32 v34, v34, s39, -v101\n"
    "v_fma_f32 v35, v35, s39, -v101\n"
    "v_fma_f32 v36, v36, s39, -v101\n"
    "v_mfma_f32_16x16x32_bf16 v[56:59], a[240:243], a[104:107], v[56:59]\n"
    "ds_read_b128 a[240:243], v96 offset:3072\n"
    "v_exp_f32 v32, v32\n"
    "v_mfma_f32_16x16x32_bf16 v[56:59], a[244:247], a[108:111], v[56:59]\n"
    "s_add_u32 m0, s70, 0\n"
    "ds_read_b128 a[244:247], v96 offset:3584\n"
    "s_waitcnt lgkmcnt(6)\n"
    "v_mfma_f32_16x16x32_bf16 v[52:55], a[248:251], a[72:75], v[52:55]\n"
    "buffer_load_dwordx4 v111, s[64:67], s78 offen offset:1024 nt lds\n"
    "v_exp_f32 v33, v33\n"
    "v_mfma_f32_16x16x32_bf16 v[52:55], a[252:255], a[76:79], v[52:55]\n"
    "v_exp_f32 v34, v34\n"
    "v_add_f32 v109, v32, v33\n"
    "v_mfma_f32_16x16x32_bf16 v[60:63], a[248:251], a[104:107], v[60:63]\n"
    "ds_read_b128 a[248:251], v96 offset:11264\n"
    "v_exp_f32 v35, v35\n"
    "v_mfma_f32_16x16x32_bf16 v[60:63], a[252:255], a[108:111], v[60:63]\n"
    "ds_read_b128 a[252:255], v96 offset:11776\n"
    "s_waitcnt lgkmcnt(6)\n"
    "v_mfma_f32_16x16x32_bf16 v[48:51], a[208:211], a[80:83], v[48:51]\n"
    "v_cvt_pk_bf16_f32 v32, v32, v33\n"
    "v_add_f32 v109, v109, v34\n"
    "v_add_f32 v109, v109, v35\n"
    "v_mfma_f32_16x16x32_bf16 v[48:51], a[212:215], a[84:87], v[48:51]\n"
    "s_add_u32 m0, s70, 0\n"
    "v_cvt_pk_bf16_f32 v33, v34, v35\n"
    "v_fma_f32 v37, v37, s39, -v101\n"
    "v_mfma_f32_16x16x32_bf16 v[56:59], a[208:211], a[112:115], v[56:59]\n"
    "buffer_load_dwordx4 v110, s[64:67], s78 offen offset:2048 nt lds\n"
    "v_fma_f32 v38, v38, s39, -v101\n"
    "v_fma_f32 v39, v39, s39, -v101\n"
    "v_mfma_f32_16x16x32_bf16 v[56:59], a[212:215], a[116:119], v[56:59]\n"
    "v_exp_f32 v36, v36\n"
    "s_waitcnt lgkmcnt(4)\n"
    "v_mfma_f32_16x16x32_bf16 v[52:55], a[216:219], a[80:83], v[52:55]\n"
    "s_waitcnt vmcnt(13) lgkmcnt(0)\n"
    "s_barrier\n"
    "ds_read_b64_tr_b16 a[208:209], v97 offset:16384\n"
    "v_mfma_f32_16x16x32_bf16 v[52:55], a[220:223], a[84:87], v[52:55]\n"
    "ds_read_b64_tr_b16 a[210:211], v97 offset:24576\n"
    "ds_read_b64_tr_b16 a[212:213], v98 offset:16384\n"
    "ds_read_b64_tr_b16 a[214:215], v98 offset:24576\n"
    "v_mfma_f32_16x16x32_bf16 v[60:63], a[216:219], a[112:115], v[60:63]\n"
    "ds_read_b64_tr_b16 a[216:217], v97 offset:16896\n"
    "ds_read_b64_tr_b16 a[218:219], v97 offset:25088\n"
    "v_fma_f32 v40, v40, s39, -v102\n"
    "v_mfma_f32_16x16x32_bf16 v[60:63], a[220:223], a[116:119], v[60:63]\n"
    "s_add_u32 m0, s70, 0\n"
    "ds_read_b64_tr_b16 a[220:221], v98 offset:16896\n"
    "v_mfma_f32_16x16x32_bf16 v[48:51], a[240:243], a[88:91], v[48:51]\n"
    "buffer_load_dwordx4 v111, s[64:67], s78 offen offset:3072 nt lds\n"
    "ds_read_b64_tr_b16 a[222:223], v98 offset:25088\n"
    "v_mfma_f32_16x16x32_bf16 v[48:51], a[244:247], a[92:95], v[48:51]\n"
    "v_exp_f32 v37, v37\n"
    "v_fma_f32 v41, v41, s39, -v102\n"
    "v_mfma_f32_16x16x32_bf16 v[56:59], a[240:243], a[120:123], v[56:59]\n"
    "ds_read_b64_tr_b16 a[240:241], v97 offset:17408\n"
    "ds_read_b64_tr_b16 a[242:243], v97 offset:25600\n"
    "v_cvt_pk_bf16_f32 v34, v36, v37\n"
    "v_mfma_f32_16x16x32_bf16 v[56:59], a[244:247], a[124:127], v[56:59]\n"
    "ds_read_b64_tr_b16 a[244:245], v98 offset:17408\n"
    "ds_read_b64_tr_b16 a[246:247], v98 offset:25600\n"
    "v_mfma_f32_16x16x32_bf16 v[52:55], a[248:251], a[88:91], v[52:55]\n"
    "v_exp_f32 v38, v38\n"
    "v_add_f32 v36, v109, v36\n"
    "v_mfma_f32_16x16x32_bf16 v[52:55], a[252:255], a[92:95], v[52:55]\n"
    "s_xor_b32 s78, s87, 20480\n"
    "s_add_u32 m0, s70, 4096\n"
    "v_add_f32 v36, v36, v37\n"
    "v_mfma_f32_16x16x32_bf16 v[60:63], a[248:251], a[120:123], v[60:63]\n"
    "buffer_load_dwordx4 v110, s[64:67], s78 offen offset:0 nt lds\n"
    "ds_read_b64_tr_b16 a[248:249], v97 offset:17920\n"
    "ds_read_b64_tr_b16 a[250:251], v97 offset:26112\n"
    "v_mfma_f32_16x16x32_bf16 v[60:63], a[252:255], a[124:127], v[60:63]\n"
    "ds_read_b64_tr_b16 a[252:253], v98 offset:17920\n"
    "ds_read_b64_tr_b16 a[254:255], v98 offset:26112\n"
    "s_waitcnt lgkmcnt(12)\n"
    "v_mfma_f32_16x16x32_bf16 v[128:131], a[208:211], v[0:3], v[128:131]\n"
    "v_exp_f32 v39, v39\n"
    "v_add_f32 v36, v36, v38\n"
    "v_mfma_f32_16x16x32_bf16 v[132:135], a[208:211], v[8:11], v[132:135]\n"
    "v_cvt_pk_bf16_f32 v35, v38, v39\n"
    "v_add_f32 v36, v36, v39\n"
    "v_mfma_f32_16x16x32_bf16 v[136:139], a[208:211], v[16:19], v[136:139]\n"
    "v_fma_f32 v42, v42, s39, -v102\n"
    "v_fma_f32 v43, v43, s39, -v102\n"
    "v_fma_f32 v44, v44, s39, -v102\n"
    "v_mfma_f32_16x16x32_bf16 v[140:143], a[208:211], v[24:27], v[140:143]\n"
    "s_add_u32 m0, s70, 4096\n"
    "ds_read_b64_tr_b16 a[208:209], v97 offset:18432\n"
    "ds_read_b64_tr_b16 a[210:211], v97 offset:26624\n"
    "v_mfma_f32_16x16x32_bf16 v[156:159], a[212:215], v[24:27], v[156:159]\n"
    "buffer_load_dwordx4 v111, s[64:67], s78 offen offset:1024 nt lds\n"
    "v_exp_f32 v40, v40\n"
    "v_mfma_f32_16x16x32_bf16 v[152:155], a[212:215], v[16:19], v[152:155]\n"
    "v_exp_f32 v41, v41\n"
    "v_fma_f32 v45, v45, s39, -v102\n"
    "v_mfma_f32_16x16x32_bf16 v[148:151], a[212:215], v[8:11], v[148:151]\n"
    "v_exp_f32 v42, v42\n"
    "v_add_f32 v109, v40, v41\n"
    "v_mfma_f32_16x16x32_bf16 v[144:147], a[212:215], v[0:3], v[144:147]\n"
    "ds_read_b64_tr_b16 a[212:213], v98 offset:18432\n"
    "ds_read_b64_tr_b16 a[214:215], v98 offset:26624\n"
    "s_waitcnt lgkmcnt(12)\n"
    "v_mfma_f32_16x16x32_bf16 v[160:163], a[216:219], v[0:3], v[160:163]\n"
    "v_exp_f32 v43, v43\n"
    "v_cvt_pk_bf16_f32 v40, v40, v41\n"
    "v_mfma_f32_16x16x32_bf16 v[164:167], a[216:219], v[8:11], v[164:167]\n"
    "s_add_u32 m0, s70, 4096\n"
    "v_add_f32 v109, v109, v42\n"
    "v_add_f32 v109, v109, v43\n"
    "v_mfma_f32_16x16x32_bf16 v[168:171], a[216:219], v[16:19], v[168:171]\n"
    "buffer_load_dwordx4 v110, s[64:67], s78 offen offset:2048 nt lds\n"
    "v_cvt_pk_bf16_f32 v41, v42, v43\n"
    "v_fma_f32 v46, v46, s39, -v102\n"
    "v_mfma_f32_16x16x32_bf16 v[172:175], a[216:219], v[24:27], v[172:175]\n"
    "ds_read_b64_tr_b16 a[216:217], v97 offset:18944\n"
    "ds_read_b64_tr_b16 a[218:219], v97 offset:27136\n"
    "v_mfma_f32_16x16x32_bf16 v[188:191], a[220:223], v[24:27], v[188:191]\n"
    "v_fma_f32 v47, v47, s39, -v102\n"
    "v_exp_f32 v44, v44\n"
    "v_mfma_f32_16x16x32_bf16 v[184:187], a[220:223], v[16:19], v[184:187]\n"
    "v_exp_f32 v45, v45\n"
    "v_fma_f32 v48, v48, s39, -v103\n"
    "v_mfma_f32_16x16x32_bf16 v[180:183], a[220:223], v[8:11], v[180:183]\n"
    "v_exp_f32 v46, v46\n"
    "v_cvt_pk_bf16_f32 v42, v44, v45\n"
    "v_mfma_f32_16x16x32_bf16 v[176:179], a[220:223], v[0:3], v[176:179]\n"
    "s_add_u32 m0, s70, 4096\n"
    "ds_read_b64_tr_b16 a[220:221], v98 offset:18944\n"
    "s_waitcnt lgkmcnt(11)\n"
    "v_mfma_f32_16x16x32_bf16 v[192:195], a[240:243], v[0:3], v[192:195]\n"
    "buffer_load_dwordx4 v111, s[64:67], s78 offen offset:3072 nt lds\n"
    "ds_read_b64_tr_b16 a[222:223], v98 offset:27136\n"
    "v_add_f32 v44, v109, v44\n"
    "v_mfma_f32_16x16x32_bf16 v[196:199], a[240:243], v[8:11], v[196:199]\n"
    "v_exp_f32 v47, v47\n"
    "v_add_f32 v44, v44, v45\n"
    "v_mfma_f32_16x16x32_bf16 v[200:203], a[240:243], v[16:19], v[200:203]\n"
    "v_add_f32 v44, v44, v46\n"
    "v_cvt_pk_bf16_f32 v43, v46, v47\n"
    "v_add_f32 v44, v44, v47\n"
    "v_mfma_f32_16x16x32_bf16 v[204:207], a[240:243], v[24:27], v[204:207]\n"
    "ds_read_b64_tr_b16 a[240:241], v97 offset:19456\n"
    "ds_read_b64_tr_b16 a[242:243], v97 offset:27648\n"
    "v_fma_f32 v49, v49, s39, -v103\n"
    "v_mfma_f32_16x16x32_bf16 v[220:223], a[244:247], v[24:27], v[220:223]\n"
    "s_add_u32 m0, s38, 0\n"
    "s_mov_b32 s76, 294912\n"
    "v_mfma_f32_16x16x32_bf16 v[216:219], a[244:247], v[16:19], v[216:219]\n"
    "buffer_load_dwordx4 v99, s[16:19], s76 offen offset:0 nt lds\n"
    "s_xor_b32 s78, s87, 24576\n"
    "s_add_u32 m0, s70, 8192\n"
    "v_fma_f32 v50, v50, s39, -v103\n"
    "v_mfma_f32_16x16x32_bf16 v[212:215], a[244:247], v[8:11], v[212:215]\n"
    "buffer_load_dwordx4 v110, s[64:67], s78 offen offset:0 nt lds\n"
    "v_fma_f32 v51, v51, s39, -v103\n"
    "v_mfma_f32_16x16x32_bf16 v[208:211], a[244:247], v[0:3], v[208:211]\n"
    "ds_read_b64_tr_b16 a[244:245], v98 offset:19456\n"
    "ds_read_b64_tr_b16 a[246:247], v98 offset:27648\n"
    "s_waitcnt lgkmcnt(12)\n"
    "v_mfma_f32_16x16x32_bf16 v[224:227], a[248:251], v[0:3], v[224:227]\n"
    "v_exp_f32 v48, v48\n"
    "v_fma_f32 v52, v52, s39, -v103\n"
    "v_mfma_f32_16x16x32_bf16 v[228:231], a[248:251], v[8:11], v[228:231]\n"
    "v_exp_f32 v49, v49\n"
    "v_fma_f32 v53, v53, s39, -v103\n"
    "v_mfma_f32_16x16x32_bf16 v[232:235], a[248:251], v[16:19], v[232:235]\n"
    "v_exp_f32 v50, v50\n"
    "v_add_f32 v109, v48, v49\n"
    "v_mfma_f32_16x16x32_bf16 v[236:239], a[248:251], v[24:27], v[236:239]\n"
    "s_add_u32 m0, s70, 8192\n"
    "ds_read_b64_tr_b16 a[248:249], v97 offset:19968\n"
    "ds_read_b64_tr_b16 a[250:251], v97 offset:28160\n"
    "v_mfma_f32_16x16x32_bf16 v[252:255], a[252:255], v[24:27], v[252:255]\n"
    "buffer_load_dwordx4 v111, s[64:67], s78 offen offset:1024 nt lds\n"
    "v_exp_f32 v51, v51\n"
    "v_mfma_f32_16x16x32_bf16 v[248:251], a[252:255], v[16:19], v[248:251]\n"
    "v_cvt_pk_bf16_f32 v48, v48, v49\n"
    "v_add_f32 v109, v109, v50\n"
    "v_add_f32 v109, v109, v51\n"
    "v_mfma_f32_16x16x32_bf16 v[244:247], a[252:255], v[8:11], v[244:247]\n"
    "v_cvt_pk_bf16_f32 v49, v50, v51\n"
    "v_fma_f32 v54, v54, s39, -v103\n"
    "v_fma_f32 v55, v55, s39, -v103\n"
    "v_mfma_f32_16x16x32_bf16 v[240:243], a[252:255], v[0:3], v[240:243]\n"
    "ds_read_b64_tr_b16 a[252:253], v98 offset:19968\n"
    "s_waitcnt lgkmcnt(11)\n"
    "v_mfma_f32_16x16x32_bf16 a[128:131], a[208:211], v[0:3], a[128:131]\n"
    "ds_read_b64_tr_b16 a[254:255], v98 offset:28160\n"
    "v_exp_f32 v52, v52\n"
    "v_mfma_f32_16x16x32_bf16 a[132:135], a[208:211], v[8:11], a[132:135]\n"
    "s_add_u32 m0, s70, 8192\n"
    "v_exp_f32 v53, v53\n"
    "v_mfma_f32_16x16x32_bf16 a[136:139], a[208:211], v[16:19], a[136:139]\n"
    "buffer_load_dwordx4 v110, s[64:67], s78 offen offset:2048 nt lds\n"
    "v_exp_f32 v54, v54\n"
    "v_mfma_f32_16x16x32_bf16 a[140:143], a[208:211], v[24:27], a[140:143]\n"
    "s_add_u32 m0, s38, 896\n"
    "ds_read_b128 a[208:211], v96 offset:16384\n"
    "v_cvt_pk_bf16_f32 v50, v52, v53\n"
    "v_mfma_f32_16x16x32_bf16 a[156:159], a[212:215], v[24:27], a[156:159]\n"
    "buffer_load_dwordx4 v99, s[16:19], s76 offen offset:128 nt lds\n"
    "v_add_f32 v52, v109, v52\n"
    "v_add_f32 v52, v52, v53\n"
    "v_mfma_f32_16x16x32_bf16 a[152:155], a[212:215], v[16:19], a[152:155]\n"
    "v_exp_f32 v55, v55\n"
    "v_add_f32 v52, v52, v54\n"
    "v_mfma_f32_16x16x32_bf16 a[148:151], a[212:215], v[8:11], a[148:151]\n"
    "v_cvt_pk_bf16_f32 v51, v54, v55\n"
    "v_add_f32 v52, v52, v55\n"
    "v_fma_f32 v56, v56, s39, -v104\n"
    "v_mfma_f32_16x16x32_bf16 a[144:147], a[212:215], v[0:3], a[144:147]\n"
    "s_add_u32 m0, s70, 8192\n"
    "ds_read_b128 a[212:215], v96 offset:16896\n"
    "s_waitcnt lgkmcnt(10)\n"
    "v_mfma_f32_16x16x32_bf16 a[160:163], a[216:219], v[0:3], a[160:163]\n"
    "buffer_load_dwordx4 v111, s[64:67], s78 offen offset:3072 nt lds\n"
    "v_fma_f32 v57, v57, s39, -v104\n"
    "v_fma_f32 v58, v58, s39, -v104\n"
    "v_mfma_f32_16x16x32_bf16 a[164:167], a[216:219], v[8:11], a[164:167]\n"
    "v_fma_f32 v59, v59, s39, -v104\n"
    "v_fma_f32 v60, v60, s39, -v104\n"
    "v_mfma_f32_16x16x32_bf16 a[168:171], a[216:219], v[16:19], a[168:171]\n"
    "v_exp_f32 v56, v56\n"
    "v_fma_f32 v61, v61, s39, -v104\n"
    "v_mfma_f32_16x16x32_bf16 a[172:175], a[216:219], v[24:27], a[172:175]\n"
    "ds_read_b128 a[216:219], v96 offset:24576\n"
    "v_exp_f32 v57, v57\n"
    "v_mfma_f32_16x16x32_bf16 a[188:191], a[220:223], v[24:27], a[188:191]\n"
    "v_exp_f32 v58, v58\n"
    "v_add_f32 v109, v56, v57\n"
    "v_mfma_f32_16x16x32_bf16 a[184:187], a[220:223], v[16:19], a[184:187]\n"
    "s_xor_b32 s78, s87, 28672\n"
    "s_add_u32 m0, s70, 12288\n"
    "v_cvt_pk_bf16_f32 v56, v56, v57\n"
    "v_mfma_f32_16x16x32_bf16 a[180:183], a[220:223], v[8:11], a[180:183]\n"
    "buffer_load_dwordx4 v110, s[64:67], s78 offen offset:0 nt lds\n"
    "v_exp_f32 v59, v59\n"
    "v_mfma_f32_16x16x32_bf16 a[176:179], a[220:223], v[0:3], a[176:179]\n"
    "ds_read_b128 a[220:223], v96 offset:25088\n"
    "v_add_f32 v109, v109, v58\n"
    "s_waitcnt lgkmcnt(8)\n"
    "v_mfma_f32_16x16x32_bf16 a[192:195], a[240:243], v[0:3], a[192:195]\n"
    "v_add_f32 v109, v109, v59\n"
    "v_cvt_pk_bf16_f32 v57, v58, v59\n"
    "v_fma_f32 v62, v62, s39, -v104\n"
    "v_mfma_f32_16x16x32_bf16 a[196:199], a[240:243], v[8:11], a[196:199]\n"
    "v_fma_f32 v63, v63, s39, -v104\n"
    "v_exp_f32 v60, v60\n"
    "v_mfma_f32_16x16x32_bf16 a[200:203], a[240:243], v[16:19], a[200:203]\n"
    "v_exp_f32 v61, v61\n"
    "v_mfma_f32_16x16x32_bf16 a[204:207], a[240:243], v[24:27], a[204:207]\n"
    "s_add_u32 m0, s70, 12288\n"
    "ds_read_b128 a[240:243], v96 offset:17408\n"
    "v_cvt_pk_bf16_f32 v58, v60, v61\n"
    "v_mfma_f32_16x16x32_bf16 v[76:79], a[244:247], v[24:27], v[76:79]\n"
    "buffer_load_dwordx4 v111, s[64:67], s78 offen offset:1024 nt lds\n"
    "s_add_u32 m0, s38, 1792\n"
    "v_add_f32 v60, v109, v60\n"
    "v_mfma_f32_16x16x32_bf16 v[72:75], a[244:247], v[16:19], v[72:75]\n"
    "buffer_load_dwordx4 v99, s[16:19], s76 offen offset:256 nt lds\n"
    "v_exp_f32 v62, v62\n"
    "v_mfma_f32_16x16x32_bf16 v[68:71], a[244:247], v[8:11], v[68:71]\n"
    "v_exp_f32 v63, v63\n"
    "v_add_f32 v60, v60, v61\n"
    "v_mfma_f32_16x16x32_bf16 v[64:67], a[244:247], v[0:3], v[64:67]\n"
    "ds_read_b128 a[244:247], v96 offset:17920\n"
    "v_max3_f32 v109, v36, v44, v52\n"
    "s_waitcnt lgkmcnt(6)\n"
    "v_mfma_f32_16x16x32_bf16 a[224:227], a[248:251], v[0:3], a[224:227]\n"
    "v_add_f32 v60, v60, v62\n"
    "v_cvt_pk_bf16_f32 v59, v62, v63\n"
    "v_add_f32 v60, v60, v63\n"
    "v_mfma_f32_16x16x32_bf16 a[228:231], a[248:251], v[8:11], a[228:231]\n"
    "s_add_u32 m0, s70, 12288\n"
    "v_max_f32 v109, v109, v60\n"
    "v_cmp_le_f32 vcc, 1.0, v109\n"
    "v_mfma_f32_16x16x32_bf16 a[232:235], a[248:251], v[16:19], a[232:235]\n"
    "buffer_load_dwordx4 v110, s[64:67], s78 offen offset:2048 nt lds\n"
    "v_mfma_f32_16x16x32_bf16 a[236:239], a[248:251], v[24:27], a[236:239]\n"
    "ds_read_b128 a[248:251], v96 offset:25600\n"
    "v_mfma_f32_16x16x32_bf16 v[92:95], a[252:255], v[24:27], v[92:95]\n"
    "v_mfma_f32_16x16x32_bf16 v[88:91], a[252:255], v[16:19], v[88:91]\n"
    "v_mfma_f32_16x16x32_bf16 v[84:87], a[252:255], v[8:11], v[84:87]\n"
    "v_mfma_f32_16x16x32_bf16 v[80:83], a[252:255], v[0:3], v[80:83]\n"
    "s_add_u32 m0, s70, 12288\n"
    "ds_read_b128 a[252:255], v96 offset:26112\n"
    "s_waitcnt lgkmcnt(6)\n"
    "v_mfma_f32_16x16x32_bf16 v[0:3], a[208:211], a[0:3], 0\n"
    "buffer_load_dwordx4 v111, s[64:67], s78 offen offset:3072 nt lds\n"
    "s_cbranch_vccnz L_ra5\n"
    "L_ra5_ret:\n"
    "v_add_f32 v105, v105, v36\n"
    "v_add_f32 v106, v106, v44\n"
    "v_add_f32 v107, v107, v52\n"
    "v_add_f32 v108, v108, v60\n"
    "v_mbcnt_lo_u32_b32 v114, -1, 0\n"
    "v_mfma_f32_16x16x32_bf16 v[0:3], a[212:215], a[4:7], v[0:3]\n"
    "v_mbcnt_hi_u32_b32 v114, -1, v114\n"
    "v_and_b32 v115, 15, v114\n"
    "v_lshlrev_b32 v123, 5, v115\n"
    "v_mfma_f32_16x16x32_bf16 v[8:11], a[208:211], a[32:35], 0\n"
    "ds_read_b128 a[208:211], v96 offset:18432\n"
    "v_lshrrev_b32 v115, 5, v114\n"
    "v_bfe_u32 v116, v114, 2, 1\n"
    "v_xor_b32 v116, v115, v116\n"
    "v_mfma_f32_16x16x32_bf16 v[8:11], a[212:215], a[36:39], v[8:11]\n"
    "s_add_u32 m0, s38, 2688\n"
    "ds_read_b128 a[212:215], v96 offset:18944\n"
    "s_waitcnt lgkmcnt(6)\n"
    "v_mfma_f32_16x16x32_bf16 v[4:7], a[216:219], a[0:3], 0\n"
    "buffer_load_dwordx4 v99, s[16:19], s76 offen offset:384 nt lds\n"
    "v_lshl_add_u32 v123, v116, 4, v123\n"
    "v_bfe_u32 v116, v114, 4, 1\n"
    "v_mfma_f32_16x16x32_bf16 v[4:7], a[220:223], a[4:7], v[4:7]\n"
    "v_lshl_add_u32 v123, v116, 3, v123\n"
    "v_add_u32 v123, s70, v123\n"
    "v_xor_b32 v111, 128, v123\n"
    "v_lshrrev_b32 v115, 3, v114\n"
    "v_mfma_f32_16x16x32_bf16 v[12:15], a[216:219], a[32:35], 0\n"
    "ds_read_b128 a[216:219], v96 offset:26624\n"
    "v_and_b32 v116, 7, v114\n"
    "v_lshlrev_b32 v110, 4, v116\n"
    "v_mfma_f32_16x16x32_bf16 v[12:15], a[220:223], a[36:39], v[12:15]\n"
    "ds_read_b128 a[220:223], v96 offset:27136\n"
    "v_lshl_add_u32 v110, v115, 9, v110\n"
    "s_waitcnt lgkmcnt(6)\n"
    "v_mfma_f32_16x16x32_bf16 v[0:3], a[240:243], a[8:11], v[0:3]\n"
    "v_mov_b32 v122, v105\n"
    "v_mov_b32 v109, v105\n"
    "s_nop 1\n"
    "v_permlane32_swap_b32 v122, v109\n"
    "v_add_f32 v122, v122, v109\n"
    "v_mfma_f32_16x16x32_bf16 v[0:3], a[244:247], a[12:15], v[0:3]\n"
    "s_add_u32 m0, s38, 49152\n"
    "s_mov_b32 s77, 294912\n"
    "v_mov_b32 v109, v122\n"
    "v_mfma_f32_16x16x32_bf16 v[8:11], a[240:243], a[40:43], v[8:11]\n"
    "buffer_load_dwordx4 v100, s[20:23], s77 offen offset:0 nt lds\n"
    "ds_read_b128 a[240:243], v96 offset:19456\n"
    "v_permlane16_swap_b32 v122, v109\n"
    "v_add_f32 v122, v122, v109\n"
    "v_mfma_f32_16x16x32_bf16 v[8:11], a[244:247], a[44:47], v[8:11]\n"
    "ds_read_b128 a[244:247], v96 offset:19968\n"
    "s_waitcnt lgkmcnt(6)\n"
    "v_mfma_f32_16x16x32_bf16 v[4:7], a[248:251], a[8:11], v[4:7]\n"
    "v_rcp_f32 v122, v122\n"
    "v_mov_b32 v112, v106\n"
    "v_mov_b32 v109, v106\n"
    "v_mfma_f32_16x16x32_bf16 v[4:7], a[252:255], a[12:15], v[4:7]\n"
    "s_nop 0\n"
    "v_permlane32_swap_b32 v112, v109\n"
    "v_add_f32 v112, v112, v109\n"
    "v_mov_b32 v109, v112\n"
    "s_nop 1\n"
    "v_permlane16_swap_b32 v112, v109\n"
    "v_mfma_f32_16x16x32_bf16 v[12:15], a[248:251], a[40:43], v[12:15]\n"
    "ds_read_b128 a[248:251], v96 offset:27648\n"
    "v_add_f32 v112, v112, v109\n"
    "v_mfma_f32_16x16x32_bf16 v[12:15], a[252:255], a[44:47], v[12:15]\n"
    "ds_read_b128 a[252:255], v96 offset:28160\n"
    "v_rcp_f32 v112, v112\n"
    "s_waitcnt lgkmcnt(6)\n"
    "v_mfma_f32_16x16x32_bf16 v[0:3], a[208:211], a[16:19], v[0:3]\n"
    "v_mov_b32 v124, v107\n"
    "v_mov_b32 v109, v107\n"
    "s_nop 1\n"
    "v_permlane32_swap_b32 v124, v109\n"
    "v_add_f32 v124, v124, v109\n"
    "v_mfma_f32_16x16x32_bf16 v[0:3], a[212:215], a[20:23], v[0:3]\n"
    "v_mov_b32 v109, v124\n"
    "s_nop 1\n"
    "v_permlane16_swap_b32 v124, v109\n"
    "v_add_f32 v124, v124, v109\n"
    "v_mfma_f32_16x16x32_bf16 v[8:11], a[208:211], a[48:51], v[8:11]\n"
    "s_waitcnt vmcnt(21) lgkmcnt(0)\n"
    "s_barrier\n"
    "ds_read_b64_tr_b16 a[208:209], v97 offset:32768\n"
    "ds_read_b64_tr_b16 a[210:211], v97 offset:40960\n"
    "v_mfma_f32_16x16x32_bf16 v[8:11], a[212:215], a[52:55], v[8:11]\n"
    "ds_read_b64_tr_b16 a[212:213], v98 offset:32768\n"
    "ds_read_b64_tr_b16 a[214:215], v98 offset:40960\n"
    "v_mfma_f32_16x16x32_bf16 v[4:7], a[216:219], a[16:19], v[4:7]\n"
    "v_rcp_f32 v124, v124\n"
    "v_mov_b32 v126, v108\n"
    "v_mfma_f32_16x16x32_bf16 v[4:7], a[220:223], a[20:23], v[4:7]\n"
    "v_mov_b32 v109, v108\n"
    "s_nop 1\n"
    "v_permlane32_swap_b32 v126, v109\n"
    "v_add_f32 v126, v126, v109\n"
    "v_mov_b32 v109, v126\n"
    "v_mfma_f32_16x16x32_bf16 v[12:15], a[216:219], a[48:51], v[12:15]\n"
    "s_waitcnt vmcnt(13)\n"
    "ds_read_b128 a[64:67], v118 offset:0\n"
    "ds_read_b64_tr_b16 a[216:217], v97 offset:33280\n"
    "v_mfma_f32_16x16x32_bf16 v[12:15], a[220:223], a[52:55], v[12:15]\n"
    "ds_read_b128 a[68:71], v119 offset:0\n"
    "ds_read_b64_tr_b16 a[218:219], v97 offset:41472\n"
    "v_mfma_f32_16x16x32_bf16 v[0:3], a[240:243], a[24:27], v[0:3]\n"
    "s_add_u32 m0, s38, 50048\n"
    "ds_read_b128 a[72:75], v120 offset:0\n"
    "ds_read_b64_tr_b16 a[220:221], v98 offset:33280\n"
    "ds_read_b64_tr_b16 a[222:223], v98 offset:41472\n"
    "v_mfma_f32_16x16x32_bf16 v[0:3], a[244:247], a[28:31], v[0:3]\n"
    "buffer_load_dwordx4 v100, s[20:23], s77 offen offset:128 nt lds\n"
    "ds_read_b128 a[76:79], v121 offset:0\n"
    "v_permlane16_swap_b32 v126, v109\n"
    "v_mfma_f32_16x16x32_bf16 v[8:11], a[240:243], a[56:59], v[8:11]\n"
    "ds_read_b128 a[80:83], v118 offset:256\n"
    "ds_read_b64_tr_b16 a[240:241], v97 offset:33792\n"
    "ds_read_b64_tr_b16 a[242:243], v97 offset:41984\n"
    "v_add_f32 v126, v126, v109\n"
    "v_mfma_f32_16x16x32_bf16 v[8:11], a[244:247], a[60:63], v[8:11]\n"
    "ds_read_b128 a[84:87], v119 offset:256\n"
    "ds_read_b64_tr_b16 a[244:245], v98 offset:33792\n"
    "v_mfma_f32_16x16x32_bf16 v[4:7], a[248:251], a[24:27], v[4:7]\n"
    "ds_read_b128 a[88:91], v120 offset:256\n"
    "ds_read_b64_tr_b16 a[246:247], v98 offset:41984\n"
    "v_mfma_f32_16x16x32_bf16 v[4:7], a[252:255], a[28:31], v[4:7]\n"
    "ds_read_b128 a[92:95], v121 offset:256\n"
    "v_rcp_f32 v126, v126\n"
    "v_mfma_f32_16x16x32_bf16 v[12:15], a[248:251], a[56:59], v[12:15]\n"
    "ds_read_b64_tr_b16 a[248:249], v97 offset:34304\n"
    "ds_read_b64_tr_b16 a[250:251], v97 offset:42496\n"
    "v_mfma_f32_16x16x32_bf16 v[12:15], a[252:255], a[60:63], v[12:15]\n"
    "ds_read_b64_tr_b16 a[252:253], v98 offset:34304\n"
    "ds_read_b64_tr_b16 a[254:255], v98 offset:42496\n"
    "s_waitcnt lgkmcnt(15)\n"
    "v_mfma_f32_16x16x32_bf16 v[128:131], a[208:211], v[32:35], v[128:131]\n"
    "v_mfma_f32_16x16x32_bf16 v[132:135], a[208:211], v[40:43], v[132:135]\n"
    "v_max3_f32 v101, v0, v1, v2\n"
    "v_max3_f32 v101, v101, v3, v4\n"
    "v_max3_f32 v101, v101, v5, v6\n"
    "v_max_f32 v101, v101, v7\n"
    "v_mov_b32 v125, v101\n"
    "v_mov_b32 v105, 0\n"
    "s_nop 0\n"
    "v_permlane32_swap_b32 v101, v125\n"
    "v_max_f32 v101, v101, v125\n"
    "v_mov_b32 v125, v101\n"
    "v_mfma_f32_16x16x32_bf16 v[136:139], a[208:211], v[48:51], v[136:139]\n"
    "s_nop 0\n"
    "v_permlane16_swap_b32 v101, v125\n"
    "v_max_f32 v101, v101, v125\n"
    "v_mfma_f32_16x16x32_bf16 v[140:143], a[208:211], v[56:59], v[140:143]\n"
    "ds_read_b64_tr_b16 a[208:209], v97 offset:34816\n"
    "ds_read_b64_tr_b16 a[210:211], v97 offset:43008\n"
    "v_mul_f32 v101, s39, v101\n"
    "v_add_f32 v101, s40, v101\n"
    "v_fma_f32 v0, v0, s39, -v101\n"
    "v_fma_f32 v1, v1, s39, -v101\n"
    "v_fma_f32 v2, v2, s39, -v101\n"
    "v_fma_f32 v3, v3, s39, -v101\n"
    "v_mfma_f32_16x16x32_bf16 v[156:159], a[212:215], v[56:59], v[156:159]\n"
    "v_fma_f32 v4, v4, s39, -v101\n"
    "v_fma_f32 v5, v5, s39, -v101\n"
    "v_mfma_f32_16x16x32_bf16 v[152:155], a[212:215], v[48:51], v[152:155]\n"
    "v_fma_f32 v6, v6, s39, -v101\n"
    "v_fma_f32 v7, v7, s39, -v101\n"
    "v_exp_f32 v0, v0\n"
    "v_max3_f32 v102, v8, v9, v10\n"
    "v_pk_mul_f32 v[128:129], v[128:129], v[122:123] op_sel_hi:[1,0]\n"
    "v_pk_mul_f32 v[130:131], v[130:131], v[122:123] op_sel_hi:[1,0]\n"
    "v_pk_mul_f32 v[132:133], v[132:133], v[112:113] op_sel_hi:[1,0]\n"
    "v_cvt_pk_bf16_f32 v128, v128, v129\n"
    "v_mfma_f32_16x16x32_bf16 v[148:151], a[212:215], v[40:43], v[148:151]\n"
    "v_cvt_pk_bf16_f32 v129, v130, v131\n"
    "v_add_f32 v105, v105, v0\n"
    "v_mfma_f32_16x16x32_bf16 v[144:147], a[212:215], v[32:35], v[144:147]\n"
    "ds_read_b64_tr_b16 a[212:213], v98 offset:34816\n"
    "ds_read_b64_tr_b16 a[214:215], v98 offset:43008\n"
    "v_exp_f32 v1, v1\n"
    "v_max3_f32 v102, v102, v11, v12\n"
    "v_pk_mul_f32 v[134:135], v[134:135], v[112:113] op_sel_hi:[1,0]\n"
    "v_pk_mul_f32 v[136:137], v[136:137], v[124:125] op_sel_hi:[1,0]\n"
    "v_cvt_pk_bf16_f32 v132, v132, v133\n"
    "s_waitcnt lgkmcnt(15)\n"
    "v_mfma_f32_16x16x32_bf16 v[160:163], a[216:219], v[32:35], v[160:163]\n"
    "s_add_u32 m0, s38, 50944\n"
    "v_cvt_pk_bf16_f32 v133, v134, v135\n"
    "v_mfma_f32_16x16x32_bf16 v[164:167], a[216:219], v[40:43], v[164:167]\n"
    "buffer_load_dwordx4 v100, s[20:23], s77 offen offset:256 nt lds\n"
    "v_exp_f32 v2, v2\n"
    "v_exp_f32 v3, v3\n"
    "v_pk_mul_f32 v[138:139], v[138:139], v[124:125] op_sel_hi:[1,0]\n"
    "v_pk_mul_f32 v[152:153], v[152:153], v[124:125] op_sel_hi:[1,0]\n"
    "v_cvt_pk_bf16_f32 v136, v136, v137\n"
    "v_mfma_f32_16x16x32_bf16 v[168:171], a[216:219], v[48:51], v[168:171]\n"
    "v_cvt_pk_bf16_f32 v137, v138, v139\n"
    "v_add_f32 v105, v105, v1\n"
    "v_mfma_f32_16x16x32_bf16 v[172:175], a[216:219], v[56:59], v[172:175]\n"
    "ds_read_b64_tr_b16 a[216:217], v97 offset:35328\n"
    "ds_read_b64_tr_b16 a[218:219], v97 offset:43520\n"
    "v_cvt_pk_bf16_f32 v0, v0, v1\n"
    "v_exp_f32 v4, v4\n"
    "v_pk_mul_f32 v[144:145], v[144:145], v[122:123] op_sel_hi:[1,0]\n"
    "v_pk_mul_f32 v[146:147], v[146:147], v[122:123] op_sel_hi:[1,0]\n"
    "v_cvt_pk_bf16_f32 v144, v144, v145\n"
    "v_mfma_f32_16x16x32_bf16 v[188:191], a[220:223], v[56:59], v[188:191]\n"
    "v_cvt_pk_bf16_f32 v145, v146, v147\n"
    "v_add_f32 v105, v105, v2\n"
    "v_mfma_f32_16x16x32_bf16 v[184:187], a[220:223], v[48:51], v[184:187]\n"
    "v_exp_f32 v5, v5\n"
    "v_add_f32 v105, v105, v3\n"
    "v_cvt_pk_bf16_f32 v1, v2, v3\n"
    "v_add_f32 v105, v105, v4\n"
    "v_pk_mul_f32 v[160:161], v[160:161], v[122:123] op_sel_hi:[1,0]\n"
    "v_pk_mul_f32 v[162:163], v[162:163], v[122:123] op_sel_hi:[1,0]\n"
    "v_cvt_pk_bf16_f32 v160, v160, v161\n"
    "v_mfma_f32_16x16x32_bf16 v[180:183], a[220:223], v[40:43], v[180:183]\n"
    "v_cvt_pk_bf16_f32 v161, v162, v163\n"
    "v_add_f32 v105, v105, v5\n"
    "v_mfma_f32_16x16x32_bf16 v[176:179], a[220:223], v[32:35], v[176:179]\n"
    "ds_read_b64_tr_b16 a[220:221], v98 offset:35328\n"
    "ds_read_b64_tr_b16 a[222:223], v98 offset:43520\n"
    "ds_write2st64_b64 v123, v[128:129], v[160:161] offset0:0 offset1:2\n"
    "s_waitcnt lgkmcnt(14)\n"
    "v_mfma_f32_16x16x32_bf16 v[192:195], a[240:243], v[32:35], v[192:195]\n"
    "v_exp_f32 v6, v6\n"
    "v_mfma_f32_16x16x32_bf16 v[196:199], a[240:243], v[40:43], v[196:199]\n"
    "v_exp_f32 v7, v7\n"
    "v_cvt_pk_bf16_f32 v2, v4, v5\n"
    "v_add_f32 v105, v105, v6\n"
    "v_add_f32 v105, v105, v7\n"
    "v_pk_mul_f32 v[164:165], v[164:165], v[112:113] op_sel_hi:[1,0]\n"
    "v_pk_mul_f32 v[166:167], v[166:167], v[112:113] op_sel_hi:[1,0]\n"
    "v_pk_mul_f32 v[148:149], v[148:149], v[112:113] op_sel_hi:[1,0]\n"
    "v_cvt_pk_bf16_f32 v164, v164, v165\n"
    "v_mfma_f32_16x16x32_bf16 v[200:203], a[240:243], v[48:51], v[200:203]\n"
    "v_cvt_pk_bf16_f32 v165, v166, v167\n"
    "v_cvt_pk_bf16_f32 v3, v6, v7\n"
    "v_mfma_f32_16x16x32_bf16 v[204:207], a[240:243], v[56:59], v[204:207]\n"
    "ds_read_b64_tr_b16 a[240:241], v97 offset:35840\n"
    "ds_read_b64_tr_b16 a[242:243], v97 offset:44032\n"
    "v_max3_f32 v102, v102, v13, v14\n"
    "v_max_f32 v102, v102, v15\n"
    "v_mov_b32 v125, v102\n"
    "v_pk_mul_f32 v[176:177], v[176:177], v[122:123] op_sel_hi:[1,0]\n"
    "v_pk_mul_f32 v[178:179], v[178:179], v[122:123] op_sel_hi:[1,0]\n"
    "v_cvt_pk_bf16_f32 v176, v176, v177\n"
    "v_mfma_f32_16x16x32_bf16 v[220:223], a[244:247], v[56:59], v[220:223]\n"
    "v_cvt_pk_bf16_f32 v177, v178, v179\n"
    "v_permlane32_swap_b32 v102, v125\n"
    "v_mfma_f32_16x16x32_bf16 v[216:219], a[244:247], v[48:51], v[216:219]\n"
    "ds_write2st64_b64 v111, v[144:145], v[176:177] offset0:1 offset1:3\n"
    "ds_read_b128 v[160:163], v113 offset:0\n"
    "ds_read_b128 v[176:179], v113 offset:256\n"
    "v_pk_mul_f32 v[150:151], v[150:151], v[112:113] op_sel_hi:[1,0]\n"
    "v_cvt_pk_bf16_f32 v148, v148, v149\n"
    "v_mfma_f32_16x16x32_bf16 v[212:215], a[244:247], v[40:43], v[212:215]\n"
    "v_cvt_pk_bf16_f32 v149, v150, v151\n"
    "v_max_f32 v102, v102, v125\n"
    "v_mfma_f32_16x16x32_bf16 v[208:211], a[244:247], v[32:35], v[208:211]\n"
    "ds_read_b64_tr_b16 a[244:245], v98 offset:35840\n"
    "ds_read_b64_tr_b16 a[246:247], v98 offset:44032\n"
    "v_mov_b32 v125, v102\n"
    "v_mov_b32 v106, 0\n"
    "s_nop 0\n"
    "v_permlane16_swap_b32 v102, v125\n"
    "v_pk_mul_f32 v[180:181], v[180:181], v[112:113] op_sel_hi:[1,0]\n"
    "v_max_f32 v102, v102, v125\n"
    "s_waitcnt lgkmcnt(15)\n"
    "v_mfma_f32_16x16x32_bf16 v[224:227], a[248:251], v[32:35], v[224:227]\n"
    "s_add_u32 m0, s38, 51840\n"
    "v_mul_f32 v102, s39, v102\n"
    "v_mfma_f32_16x16x32_bf16 v[228:231], a[248:251], v[40:43], v[228:231]\n"
    "buffer_load_dwordx4 v100, s[20:23], s77 offen offset:384 nt lds\n"
    "ds_write2st64_b64 v123, v[132:133], v[164:165] offset0:4 offset1:6\n"
    "v_pk_mul_f32 v[182:183], v[182:183], v[112:113] op_sel_hi:[1,0]\n"
    "v_cvt_pk_bf16_f32 v180, v180, v181\n"
    "v_mfma_f32_16x16x32_bf16 v[232:235], a[248:251], v[48:51], v[232:235]\n"
    "v_cvt_pk_bf16_f32 v181, v182, v183\n"
    "v_add_f32 v102, s40, v102\n"
    "v_mfma_f32_16x16x32_bf16 v[236:239], a[248:251], v[56:59], v[236:239]\n"
    "ds_read_b64_tr_b16 a[248:249], v97 offset:36352\n"
    "ds_read_b64_tr_b16 a[250:251], v97 offset:44544\n"
    "ds_write2st64_b64 v111, v[148:149], v[180:181] offset0:5 offset1:7\n"
    "v_mfma_f32_16x16x32_bf16 v[252:255], a[252:255], v[56:59], v[252:255]\n"
    "ds_read_b128 v[164:167], v113 offset:2048\n"
    "ds_read_b128 v[180:183], v113 offset:2304\n"
    "v_mfma_f32_16x16x32_bf16 v[248:251], a[252:255], v[48:51], v[248:251]\n"
    "v_fma_f32 v8, v8, s39, -v102\n"
    "v_fma_f32 v9, v9, s39, -v102\n"
    "v_fma_f32 v10, v10, s39, -v102\n"
    "v_fma_f32 v11, v11, s39, -v102\n"
    "v_fma_f32 v12, v12, s39, -v102\n"
    "v_pk_mul_f32 v[168:169], v[168:169], v[124:125] op_sel_hi:[1,0]\n"
    "v_pk_mul_f32 v[170:171], v[170:171], v[124:125] op_sel_hi:[1,0]\n"
    "v_pk_mul_f32 v[154:155], v[154:155], v[124:125] op_sel_hi:[1,0]\n"
    "v_pk_mul_f32 v[184:185], v[184:185], v[124:125] op_sel_hi:[1,0]\n"
    "v_cvt_pk_bf16_f32 v168, v168, v169\n"
    "v_mfma_f32_16x16x32_bf16 v[244:247], a[252:255], v[40:43], v[244:247]\n"
    "v_cvt_pk_bf16_f32 v169, v170, v171\n"
    "v_cvt_pk_bf16_f32 v152, v152, v153\n"
    "v_mfma_f32_16x16x32_bf16 v[240:243], a[252:255], v[32:35], v[240:243]\n"
    "s_add_u32 m0, s38, 65536\n"
    "s_mov_b32 s77, 311296\n"
    "ds_read_b64_tr_b16 a[252:253], v98 offset:36352\n"
    "ds_read_b64_tr_b16 a[254:255], v98 offset:44544\n"
    "v_cvt_pk_bf16_f32 v153, v154, v155\n"
    "v_pk_mul_f32 v[186:187], v[186:187], v[124:125] op_sel_hi:[1,0]\n"
    "v_cvt_pk_bf16_f32 v184, v184, v185\n"
    "s_waitcnt lgkmcnt(15)\n"
    "v_mfma_f32_16x16x32_bf16 a[128:131], a[208:211], v[32:35], a[128:131]\n"
    "buffer_load_dwordx4 v100, s[20:23], s77 offen offset:0 nt lds\n"
    "v_cvt_pk_bf16_f32 v185, v186, v187\n"
    "v_mfma_f32_16x16x32_bf16 a[132:135], a[208:211], v[40:43], a[132:135]\n"
    "ds_write2st64_b64 v123, v[136:137], v[168:169] offset0:8 offset1:10\n"
    "v_pk_mul_f32 v[140:141], v[140:141], v[126:127] op_sel_hi:[1,0]\n"
    "v_pk_mul_f32 v[142:143], v[142:143], v[126:127] op_sel_hi:[1,0]\n"
    "v_cvt_pk_bf16_f32 v140, v140, v141\n"
    "v_mfma_f32_16x16x32_bf16 a[136:139], a[208:211], v[48:51], a[136:139]\n"
    "v_cvt_pk_bf16_f32 v141, v142, v143\n"
    "v_fma_f32 v13, v13, s39, -v102\n"
    "v_mfma_f32_16x16x32_bf16 a[140:143], a[208:211], v[56:59], a[140:143]\n"
    "ds_read_b128 a[208:211], v96 offset:16384\n"
    "ds_write2st64_b64 v111, v[152:153], v[184:185] offset0:9 offset1:11\n"
    "ds_read_b128 v[168:171], v113 offset:4096\n"
    "ds_read_b128 v[184:187], v113 offset:4352\n"
    "v_mfma_f32_16x16x32_bf16 a[156:159], a[212:215], v[56:59], a[156:159]\n"
    "v_fma_f32 v14, v14, s39, -v102\n"
    "v_fma_f32 v15, v15, s39, -v102\n"
    "v_mfma_f32_16x16x32_bf16 a[152:155], a[212:215], v[48:51], a[152:155]\n"
    "s_add_u32 m0, s38, 66432\n"
    "v_exp_f32 v8, v8\n"
    "v_exp_f32 v9, v9\n"
    "v_pk_mul_f32 v[172:173], v[172:173], v[126:127] op_sel_hi:[1,0]\n"
    "v_pk_mul_f32 v[174:175], v[174:175], v[126:127] op_sel_hi:[1,0]\n"
    "v_pk_mul_f32 v[156:157], v[156:157], v[126:127] op_sel_hi:[1,0]\n"
    "v_cvt_pk_bf16_f32 v172, v172, v173\n"
    "v_mfma_f32_16x16x32_bf16 a[148:151], a[212:215], v[40:43], a[148:151]\n"
    "buffer_load_dwordx4 v100, s[20:23], s77 offen offset:128 nt lds\n"
    "v_cvt_pk_bf16_f32 v173, v174, v175\n"
    "v_mfma_f32_16x16x32_bf16 a[144:147], a[212:215], v[32:35], a[144:147]\n"
    "ds_read_b128 a[212:215], v96 offset:16896\n"
    "ds_write2st64_b64 v123, v[140:141], v[172:173] offset0:12 offset1:14\n"
    "v_mfma_f32_16x16x32_bf16 a[160:163], a[216:219], v[32:35], a[160:163]\n"
    "s_waitcnt vmcnt(7)\n"
    "ds_read_b128 a[96:99], v118 offset:8192\n"
    "v_mfma_f32_16x16x32_bf16 a[164:167], a[216:219], v[40:43], a[164:167]\n"
    "ds_read_b128 a[100:103], v119 offset:8192\n"
    "v_exp_f32 v10, v10\n"
    "v_exp_f32 v11, v11\n"
    "v_pk_mul_f32 v[158:159], v[158:159], v[126:127] op_sel_hi:[1,0]\n"
    "v_pk_mul_f32 v[188:189], v[188:189], v[126:127] op_sel_hi:[1,0]\n"
    "v_pk_mul_f32 v[190:191], v[190:191], v[126:127] op_sel_hi:[1,0]\n"
    "v_cvt_pk_bf16_f32 v156, v156, v157\n"
    "v_mfma_f32_16x16x32_bf16 a[168:171], a[216:219], v[48:51], a[168:171]\n"
    "ds_read_b128 a[104:107], v120 offset:8192\n"
    "v_cvt_pk_bf16_f32 v157, v158, v159\n"
    "v_mfma_f32_16x16x32_bf16 a[172:175], a[216:219], v[56:59], a[172:175]\n"
    "s_add_u32 m0, s38, 67328\n"
    "ds_read_b128 a[108:111], v121 offset:8192\n"
    "ds_read_b128 a[216:219], v96 offset:24576\n"
    "v_cvt_pk_bf16_f32 v188, v188, v189\n"
    "v_cvt_pk_bf16_f32 v189, v190, v191\n"
    "v_pk_mul_f32 v[192:193], v[192:193], v[122:123] op_sel_hi:[1,0]\n"
    "v_pk_mul_f32 v[194:195], v[194:195], v[122:123] op_sel_hi:[1,0]\n"
    "v_cvt_pk_bf16_f32 v192, v192, v193\n"
    "v_mfma_f32_16x16x32_bf16 a[188:191], a[220:223], v[56:59], a[188:191]\n"
    "buffer_load_dwordx4 v100, s[20:23], s77 offen offset:256 nt lds\n"
    "ds_read_b128 a[112:115], v118 offset:8448\n"
    "v_mfma_f32_16x16x32_bf16 a[184:187], a[220:223], v[48:51], a[184:187]\n"
    "ds_read_b128 a[116:119], v119 offset:8448\n"
    "ds_write2st64_b64 v111, v[156:157], v[188:189] offset0:13 offset1:15\n"
    "ds_read_b128 v[172:175], v113 offset:6144\n"
    "ds_read_b128 v[188:191], v113 offset:6400\n"
    "v_mfma_f32_16x16x32_bf16 a[180:183], a[220:223], v[40:43], a[180:183]\n"
    "ds_read_b128 a[120:123], v120 offset:8448\n"
    "v_cvt_pk_bf16_f32 v193, v194, v195\n"
    "v_mfma_f32_16x16x32_bf16 a[176:179], a[220:223], v[32:35], a[176:179]\n"
    "ds_read_b128 a[124:127], v121 offset:8448\n"
    "ds_read_b128 a[220:223], v96 offset:25088\n"
    "v_accvgpr_read_b32 v114, a128\n"
    "v_accvgpr_read_b32 v115, a129\n"
    "v_accvgpr_read_b32 v116, a130\n"
    "v_pk_mul_f32 v[224:225], v[224:225], v[122:123] op_sel_hi:[1,0]\n"
    "v_pk_mul_f32 v[226:227], v[226:227], v[122:123] op_sel_hi:[1,0]\n"
    "v_cvt_pk_bf16_f32 v224, v224, v225\n"
    "s_waitcnt lgkmcnt(15)\n"
    "v_mfma_f32_16x16x32_bf16 a[192:195], a[240:243], v[32:35], a[192:195]\n"
    "v_cvt_pk_bf16_f32 v225, v226, v227\n"
    "v_accvgpr_read_b32 v117, a131\n"
    "v_mfma_f32_16x16x32_bf16 a[196:199], a[240:243], v[40:43], a[196:199]\n"
    "s_add_u32 m0, s38, 68224\n"
    "ds_write2st64_b64 v123, v[192:193], v[224:225] offset0:16 offset1:18\n"
    "v_pk_mul_f32 v[208:209], v[208:209], v[122:123] op_sel_hi:[1,0]\n"
    "v_pk_mul_f32 v[210:211], v[210:211], v[122:123] op_sel_hi:[1,0]\n"
    "v_cvt_pk_bf16_f32 v208, v208, v209\n"
    "v_mfma_f32_16x16x32_bf16 a[200:203], a[240:243], v[48:51], a[200:203]\n"
    "buffer_load_dwordx4 v100, s[20:23], s77 offen offset:384 nt lds\n"
    "v_cvt_pk_bf16_f32 v209, v210, v211\n"
    "v_mfma_f32_16x16x32_bf16 a[204:207], a[240:243], v[56:59], a[204:207]\n"
    "ds_read_b128 a[240:243], v96 offset:17408\n"
    "v_accvgpr_read_b32 v118, a160\n"
    "v_accvgpr_read_b32 v119, a161\n"
    "v_accvgpr_read_b32 v120, a162\n"
    "v_accvgpr_read_b32 v121, a163\n"
    "v_pk_mul_f32 v[240:241], v[240:241], v[122:123] op_sel_hi:[1,0]\n"
    "v_pk_mul_f32 v[242:243], v[242:243], v[122:123] op_sel_hi:[1,0]\n"
    "v_cvt_pk_bf16_f32 v240, v240, v241\n"
    "v_mfma_f32_16x16x32_bf16 v[76:79], a[244:247], v[56:59], v[76:79]\n"
    "v_cvt_pk_bf16_f32 v241, v242, v243\n"
    "v_add_f32 v106, v106, v8\n"
    "v_mfma_f32_16x16x32_bf16 v[72:75], a[244:247], v[48:51], v[72:75]\n"
    "ds_write2st64_b64 v111, v[208:209], v[240:241] offset0:17 offset1:19\n"
    "ds_read_b128 v[224:227], v113 offset:8192\n"
    "ds_read_b128 v[240:243], v113 offset:8448\n"
    "v_mfma_f32_16x16x32_bf16 v[68:71], a[244:247], v[40:43], v[68:71]\n"
    "v_add_f32 v106, v106, v9\n"
    "v_cvt_pk_bf16_f32 v8, v8, v9\n"
    "v_mfma_f32_16x16x32_bf16 v[64:67], a[244:247], v[32:35], v[64:67]\n"
    "buffer_store_dwordx4 v[160:163], v110, s[28:31], s92 offen offset:0 sc1\n"
    "ds_read_b128 a[244:247], v96 offset:17920\n"
    "v_pk_mul_f32 v[196:197], v[196:197], v[112:113] op_sel_hi:[1,0]\n"
    "v_pk_mul_f32 v[198:199], v[198:199], v[112:113] op_sel_hi:[1,0]\n"
    "v_cvt_pk_bf16_f32 v196, v196, v197\n"
    "v_mfma_f32_16x16x32_bf16 a[224:227], a[248:251], v[32:35], a[224:227]\n"
    "v_cvt_pk_bf16_f32 v197, v198, v199\n"
    "v_add_f32 v106, v106, v10\n"
    "v_mfma_f32_16x16x32_bf16 a[228:231], a[248:251], v[40:43], a[228:231]\n"
    "v_exp_f32 v12, v12\n"
    "v_exp_f32 v13, v13\n"
    "v_add_f32 v106, v106, v11\n"
    "v_pk_mul_f32 v[228:229], v[228:229], v[112:113] op_sel_hi:[1,0]\n"
    "v_pk_mul_f32 v[230:231], v[230:231], v[112:113] op_sel_hi:[1,0]\n"
    "v_pk_mul_f32 v[212:213], v[212:213], v[112:113] op_sel_hi:[1,0]\n"
    "v_cvt_pk_bf16_f32 v228, v228, v229\n"
    "v_mfma_f32_16x16x32_bf16 a[232:235], a[248:251], v[48:51], a[232:235]\n"
    "v_cvt_pk_bf16_f32 v229, v230, v231\n"
    "v_cvt_pk_bf16_f32 v9, v10, v11\n"
    "v_mfma_f32_16x16x32_bf16 a[236:239], a[248:251], v[56:59], a[236:239]\n"
    "ds_read_b128 a[248:251], v96 offset:25600\n"
    "ds_write2st64_b64 v123, v[196:197], v[228:229] offset0:20 offset1:22\n"
    "v_pk_mul_f32 v[214:215], v[214:215], v[112:113] op_sel_hi:[1,0]\n"
    "v_cvt_pk_bf16_f32 v212, v212, v213\n"
    "v_mfma_f32_16x16x32_bf16 v[92:95], a[252:255], v[56:59], v[92:95]\n"
    "v_cvt_pk_bf16_f32 v213, v214, v215\n"
    "v_add_f32 v106, v106, v12\n"
    "v_mfma_f32_16x16x32_bf16 v[88:91], a[252:255], v[48:51], v[88:91]\n"
    "v_exp_f32 v14, v14\n"
    "v_exp_f32 v15, v15\n"
    "v_add_f32 v106, v106, v13\n"
    "v_pk_mul_f32 v[244:245], v[244:245], v[112:113] op_sel_hi:[1,0]\n"
    "v_pk_mul_f32 v[246:247], v[246:247], v[112:113] op_sel_hi:[1,0]\n"
    "v_cvt_pk_bf16_f32 v244, v244, v245\n"
    "v_mfma_f32_16x16x32_bf16 v[84:87], a[252:255], v[40:43], v[84:87]\n"
    "v_cvt_pk_bf16_f32 v245, v246, v247\n"
    "v_cvt_pk_bf16_f32 v10, v12, v13\n"
    "v_mfma_f32_16x16x32_bf16 v[80:83], a[252:255], v[32:35], v[80:83]\n"
    "buffer_store_dwordx4 v[176:179], v110, s[28:31], s93 offen offset:0 sc1\n"
    "ds_read_b128 a[252:255], v96 offset:26112\n"
    "v_pk_mul_f32 v[200:201], v[200:201], v[124:125] op_sel_hi:[1,0]\n"
    "v_pk_mul_f32 v[202:203], v[202:203], v[124:125] op_sel_hi:[1,0]\n"
    "v_cvt_pk_bf16_f32 v200, v200, v201\n"
    "s_waitcnt lgkmcnt(15)\n"
    "v_mfma_f32_16x16x32_bf16 v[16:19], a[208:211], a[64:67], 0\n"
    "v_cvt_pk_bf16_f32 v201, v202, v203\n"
    "v_add_f32 v106, v106, v14\n"
    "v_mfma_f32_16x16x32_bf16 v[16:19], a[212:215], a[68:71], v[16:19]\n"
    "ds_write2st64_b64 v111, v[212:213], v[244:245] offset0:21 offset1:23\n"
    "v_pk_mul_f32 v[232:233], v[232:233], v[124:125] op_sel_hi:[1,0]\n"
    "v_pk_mul_f32 v[234:235], v[234:235], v[124:125] op_sel_hi:[1,0]\n"
    "v_cvt_pk_bf16_f32 v232, v232, v233\n"
    "v_mfma_f32_16x16x32_bf16 v[24:27], a[208:211], a[96:99], 0\n"
    "ds_read_b128 a[208:211], v96 offset:18432\n"
    "v_cvt_pk_bf16_f32 v233, v234, v235\n"
    "v_mfma_f32_16x16x32_bf16 v[24:27], a[212:215], a[100:103], v[24:27]\n"
    "ds_read_b128 a[212:215], v96 offset:18944\n"
    "ds_write2st64_b64 v123, v[200:201], v[232:233] offset0:24 offset1:26\n"
    "s_waitcnt lgkmcnt(13)\n"
    "v_mfma_f32_16x16x32_bf16 v[20:23], a[216:219], a[64:67], 0\n"
    "ds_read_b128 v[228:231], v113 offset:10240\n"
    "ds_read_b128 v[244:247], v113 offset:10496\n"
    "v_mfma_f32_16x16x32_bf16 v[20:23], a[220:223], a[68:71], v[20:23]\n"
    "v_add_f32 v106, v106, v15\n"
    "v_cvt_pk_bf16_f32 v11, v14, v15\n"
    "v_mul_f32 v216, v216, v124\n"
    "v_mul_f32 v217, v217, v124\n"
    "v_mul_f32 v218, v218, v124\n"
    "v_mul_f32 v219, v219, v124\n"
    "v_pk_mul_f32 v[248:249], v[248:249], v[124:125] op_sel_hi:[1,0]\n"
    "v_cvt_pk_bf16_f32 v216, v216, v217\n"
    "v_mfma_f32_16x16x32_bf16 v[28:31], a[216:219], a[96:99], 0\n"
    "ds_read_b128 a[216:219], v96 offset:26624\n"
    "v_cvt_pk_bf16_f32 v217, v218, v219\n"
    "v_mfma_f32_16x16x32_bf16 v[28:31], a[220:223], a[100:103], v[28:31]\n"
    "ds_read_b128 a[220:223], v96 offset:27136\n"
    "v_mul_f32 v250, v250, v124\n"
    "v_mul_f32 v251, v251, v124\n"
    "v_mul_f32 v204, v204, v126\n"
    "v_mul_f32 v205, v205, v126\n"
    "v_pk_mul_f32 v[206:207], v[206:207], v[126:127] op_sel_hi:[1,0]\n"
    "v_cvt_pk_bf16_f32 v248, v248, v249\n"
    "s_waitcnt lgkmcnt(11)\n"
    "v_mfma_f32_16x16x32_bf16 v[16:19], a[240:243], a[72:75], v[16:19]\n"
    "v_cvt_pk_bf16_f32 v249, v250, v251\n"
    "v_cvt_pk_bf16_f32 v204, v204, v205\n"
    "v_mfma_f32_16x16x32_bf16 v[16:19], a[244:247], a[76:79], v[16:19]\n"
    "buffer_store_dwordx4 v[164:167], v110, s[28:31], s94 offen offset:0 sc1\n"
    "ds_write2st64_b64 v111, v[216:217], v[248:249] offset0:25 offset1:27\n"
    "v_mfma_f32_16x16x32_bf16 v[24:27], a[240:243], a[104:107], v[24:27]\n"
    "ds_read_b128 a[240:243], v96 offset:19456\n"
    "v_cvt_pk_bf16_f32 v205, v206, v207\n"
    "v_mfma_f32_16x16x32_bf16 v[24:27], a[244:247], a[108:111], v[24:27]\n"
    "ds_read_b128 a[244:247], v96 offset:19968\n"
    "ds_read_b128 v[232:235], v113 offset:12288\n"
    "ds_read_b128 v[248:251], v113 offset:12544\n"
    "v_mul_f32 v236, v236, v126\n"
    "v_mul_f32 v237, v237, v126\n"
    "v_pk_mul_f32 v[238:239], v[238:239], v[126:127] op_sel_hi:[1,0]\n"
    "v_cvt_pk_bf16_f32 v236, v236, v237\n"
    "s_waitcnt lgkmcnt(13)\n"
    "v_mfma_f32_16x16x32_bf16 v[20:23], a[248:251], a[72:75], v[20:23]\n"
    "v_cvt_pk_bf16_f32 v237, v238, v239\n"
    "v_mfma_f32_16x16x32_bf16 v[20:23], a[252:255], a[76:79], v[20:23]\n"
    "ds_write2st64_b64 v123, v[204:205], v[236:237] offset0:28 offset1:30\n"
    "v_pk_mul_f32 v[220:221], v[220:221], v[126:127] op_sel_hi:[1,0]\n"
    "v_pk_mul_f32 v[222:223], v[222:223], v[126:127] op_sel_hi:[1,0]\n"
    "v_cvt_pk_bf16_f32 v220, v220, v221\n"
    "v_cvt_pk_bf16_f32 v221, v222, v223\n"
    "v_pk_mul_f32 v[252:253], v[252:253], v[126:127] op_sel_hi:[1,0]\n"
    "s_nop 0\n"
    "v_mfma_f32_16x16x32_bf16 v[28:31], a[248:251], a[104:107], v[28:31]\n"
    "ds_read_b128 a[248:251], v96 offset:27648\n"
    "v_mfma_f32_16x16x32_bf16 v[28:31], a[252:255], a[108:111], v[28:31]\n"
    "ds_read_b128 a[252:255], v96 offset:28160\n"
    "v_mul_f32 v254, v254, v126\n"
    "v_mul_f32 v255, v255, v126\n"
    "v_mul_f32 v114, v114, v122\n"
    "v_mul_f32 v115, v115, v122\n"
    "v_pk_mul_f32 v[116:117], v[116:117], v[122:123] op_sel_hi:[1,0]\n"
    "v_cvt_pk_bf16_f32 v252, v252, v253\n"
    "s_waitcnt lgkmcnt(13)\n"
    "v_mfma_f32_16x16x32_bf16 v[16:19], a[208:211], a[80:83], v[16:19]\n"
    "v_cvt_pk_bf16_f32 v253, v254, v255\n"
    "v_cvt_pk_bf16_f32 v114, v114, v115\n"
    "v_mfma_f32_16x16x32_bf16 v[16:19], a[212:215], a[84:87], v[16:19]\n"
    "buffer_store_dwordx4 v[180:183], v110, s[28:31], s95 offen offset:0 sc1\n"
    "v_cvt_pk_bf16_f32 v115, v116, v117\n"
    "v_pk_mul_f32 v[118:119], v[118:119], v[122:123] op_sel_hi:[1,0]\n"
    "v_pk_mul_f32 v[120:121], v[120:121], v[122:123] op_sel_hi:[1,0]\n"
    "v_cvt_pk_bf16_f32 v118, v118, v119\n"
    "v_cvt_pk_bf16_f32 v119, v120, v121\n"
    "v_mfma_f32_16x16x32_bf16 v[24:27], a[208:211], a[112:115], v[24:27]\n"
    "ds_read_b128 a[208:211], v96 offset:32768\n"
    "v_mfma_f32_16x16x32_bf16 v[24:27], a[212:215], a[116:119], v[24:27]\n"
    "ds_read_b128 a[212:215], v96 offset:33280\n"
    "v_mul_f32 v64, v64, v122\n"
    "v_mul_f32 v65, v65, v122\n"
    "v_mul_f32 v66, v66, v122\n"
    "v_mul_f32 v67, v67, v122\n"
    "v_pk_mul_f32 v[80:81], v[80:81], v[122:123] op_sel_hi:[1,0]\n"
    "v_cvt_pk_bf16_f32 v64, v64, v65\n"
    "s_waitcnt lgkmcnt(10)\n"
    "v_mfma_f32_16x16x32_bf16 v[20:23], a[216:219], a[80:83], v[20:23]\n"
    "s_waitcnt lgkmcnt(0)\n"
    "s_barrier\n"
    "ds_write2st64_b64 v111, v[220:221], v[252:253] offset0:29 offset1:31\n"
    "v_mfma_f32_16x16x32_bf16 v[20:23], a[220:223], a[84:87], v[20:23]\n"
    "s_add_u32 m0, s38, 16384\n"
    "s_mov_b32 s76, 311296\n"
    "v_mfma_f32_16x16x32_bf16 v[28:31], a[216:219], a[112:115], v[28:31]\n"
    "buffer_load_dwordx4 v99, s[16:19], s76 offen offset:0 nt lds\n"
    "ds_read_b128 a[216:219], v96 offset:40960\n"
    "ds_write2st64_b64 v123, v[114:115], v[118:119] offset0:0 offset1:2\n"
    "v_accvgpr_read_b32 v114, a144\n"
    "v_mfma_f32_16x16x32_bf16 v[28:31], a[220:223], a[116:119], v[28:31]\n"
    "ds_read_b128 a[220:223], v96 offset:41472\n"
    "v_mfma_f32_16x16x32_bf16 v[16:19], a[240:243], a[88:91], v[16:19]\n"
    "v_accvgpr_read_b32 v115, a145\n"
    "v_accvgpr_read_b32 v116, a146\n"
    "v_accvgpr_read_b32 v117, a147\n"
    "v_accvgpr_read_b32 v118, a176\n"
    "v_accvgpr_read_b32 v119, a177\n"
    "v_pk_mul_f32 v[114:115], v[114:115], v[122:123] op_sel_hi:[1,0]\n"
    "v_pk_mul_f32 v[116:117], v[116:117], v[122:123] op_sel_hi:[1,0]\n"
    "v_cvt_pk_bf16_f32 v114, v114, v115\n"
    "v_mfma_f32_16x16x32_bf16 v[16:19], a[244:247], a[92:95], v[16:19]\n"
    "s_add_u32 m0, s38, 17280\n"
    "v_cvt_pk_bf16_f32 v115, v116, v117\n"
    "v_mfma_f32_16x16x32_bf16 v[24:27], a[240:243], a[120:123], v[24:27]\n"
    "buffer_load_dwordx4 v99, s[16:19], s76 offen offset:128 nt lds\n"
    "buffer_store_dwordx4 v[168:171], v110, s[28:31], s96 offen offset:0 sc1\n"
    "ds_read_b128 a[240:243], v96 offset:33792\n"
    "v_accvgpr_read_b32 v120, a178\n"
    "v_accvgpr_read_b32 v121, a179\n"
    "v_mfma_f32_16x16x32_bf16 v[24:27], a[244:247], a[124:127], v[24:27]\n"
    "ds_read_b128 a[244:247], v96 offset:34304\n"
    "v_mfma_f32_16x16x32_bf16 v[20:23], a[248:251], a[88:91], v[20:23]\n"
    "ds_read_b128 v[236:239], v113 offset:14336\n"
    "ds_read_b128 v[252:255], v113 offset:14592\n"
    "v_cvt_pk_bf16_f32 v65, v66, v67\n"
    "v_mul_f32 v118, v118, v122\n"
    "v_mul_f32 v119, v119, v122\n"
    "v_pk_mul_f32 v[120:121], v[120:121], v[122:123] op_sel_hi:[1,0]\n"
    "v_pk_mul_f32 v[82:83], v[82:83], v[122:123] op_sel_hi:[1,0]\n"
    "v_pk_mul_f32 v[68:69], v[68:69], v[112:113] op_sel_hi:[1,0]\n"
    "v_cvt_pk_bf16_f32 v118, v118, v119\n"
    "v_mfma_f32_16x16x32_bf16 v[20:23], a[252:255], a[92:95], v[20:23]\n"
    "s_add_u32 m0, s38, 18176\n"
    "v_cvt_pk_bf16_f32 v119, v120, v121\n"
    "v_mfma_f32_16x16x32_bf16 v[28:31], a[248:251], a[120:123], v[28:31]\n"
    "buffer_load_dwordx4 v99, s[16:19], s76 offen offset:256 nt lds\n"
    "ds_read_b128 a[248:251], v96 offset:41984\n"
    "ds_write2st64_b64 v111, v[114:115], v[118:119] offset0:1 offset1:3\n"
    "v_accvgpr_read_b32 v114, a132\n"
    "v_mfma_f32_16x16x32_bf16 v[28:31], a[252:255], a[124:127], v[28:31]\n"
    "ds_read_b128 a[252:255], v96 offset:42496\n"
    "v_mfma_f32_16x16x32_bf16 v[32:35], a[208:211], a[0:3], 0\n"
    "v_accvgpr_read_b32 v115, a133\n"
    "v_accvgpr_read_b32 v116, a134\n"
    "v_accvgpr_read_b32 v117, a135\n"
    "v_accvgpr_read_b32 v118, a164\n"
    "v_accvgpr_read_b32 v119, a165\n"
    "v_pk_mul_f32 v[114:115], v[114:115], v[112:113] op_sel_hi:[1,0]\n"
    "v_pk_mul_f32 v[116:117], v[116:117], v[112:113] op_sel_hi:[1,0]\n"
    "v_cvt_pk_bf16_f32 v114, v114, v115\n"
    "v_mfma_f32_16x16x32_bf16 v[32:35], a[212:215], a[4:7], v[32:35]\n"
    "s_add_u32 m0, s38, 19072\n"
    "v_cvt_pk_bf16_f32 v115, v116, v117\n"
    "v_mfma_f32_16x16x32_bf16 v[40:43], a[208:211], a[32:35], 0\n"
    "buffer_load_dwordx4 v99, s[16:19], s76 offen offset:384 nt lds\n"
    "v_accvgpr_read_b32 v120, a166\n"
    "v_accvgpr_read_b32 v121, a167\n"
    "v_max3_f32 v103, v16, v17, v18\n"
    "v_max3_f32 v103, v103, v19, v20\n"
    "v_pk_mul_f32 v[118:119], v[118:119], v[112:113] op_sel_hi:[1,0]\n"
    "v_pk_mul_f32 v[120:121], v[120:121], v[112:113] op_sel_hi:[1,0]\n"
    "v_cvt_pk_bf16_f32 v118, v118, v119\n"
    "v_cvt_pk_bf16_f32 v119, v120, v121\n"
    "v_mfma_f32_16x16x32_bf16 v[40:43], a[212:215], a[36:39], v[40:43]\n"
    "v_max3_f32 v103, v103, v21, v22\n"
    "v_max_f32 v103, v103, v23\n"
    "v_mfma_f32_16x16x32_bf16 v[48:51], a[208:211], a[64:67], 0\n"
    "buffer_store_dwordx4 v[184:187], v110, s[28:31], s97 offen offset:0 sc1\n"
    "v_mov_b32 v125, v103\n"
    "v_mov_b32 v107, 0\n"
    "v_max3_f32 v104, v24, v25, v26\n"
    "v_permlane32_swap_b32 v103, v125\n"
    "v_mfma_f32_16x16x32_bf16 v[48:51], a[212:215], a[68:71], v[48:51]\n"
    "v_max_f32 v103, v103, v125\n"
    "v_mov_b32 v125, v103\n"
    "v_mfma_f32_16x16x32_bf16 v[56:59], a[208:211], a[96:99], 0\n"
    "ds_read_b128 a[208:211], v96 offset:34816\n"
    "ds_write2st64_b64 v123, v[114:115], v[118:119] offset0:4 offset1:6\n"
    "v_accvgpr_read_b32 v114, a148\n"
    "v_accvgpr_read_b32 v115, a149\n"
    "v_mfma_f32_16x16x32_bf16 v[56:59], a[212:215], a[100:103], v[56:59]\n"
    "ds_read_b128 a[212:215], v96 offset:35328\n"
    "s_waitcnt lgkmcnt(10)\n"
    "v_mfma_f32_16x16x32_bf16 v[36:39], a[216:219], a[0:3], 0\n"
    "v_accvgpr_read_b32 v116, a150\n"
    "v_accvgpr_read_b32 v117, a151\n"
    "v_accvgpr_read_b32 v118, a180\n"
    "v_accvgpr_read_b32 v119, a181\n"
    "v_accvgpr_read_b32 v120, a182\n"
    "v_pk_mul_f32 v[114:115], v[114:115], v[112:113] op_sel_hi:[1,0]\n"
    "v_pk_mul_f32 v[116:117], v[116:117], v[112:113] op_sel_hi:[1,0]\n"
    "v_cvt_pk_bf16_f32 v114, v114, v115\n"
    "v_mfma_f32_16x16x32_bf16 v[36:39], a[220:223], a[4:7], v[36:39]\n"
    "v_cvt_pk_bf16_f32 v115, v116, v117\n"
    "v_accvgpr_read_b32 v121, a183\n"
    "v_mfma_f32_16x16x32_bf16 v[44:47], a[216:219], a[32:35], 0\n"
    "v_permlane16_swap_b32 v103, v125\n"
    "v_max_f32 v103, v103, v125\n"
    "v_mul_f32 v103, s39, v103\n"
    "v_add_f32 v103, s40, v103\n"
    "v_fma_f32 v16, v16, s39, -v103\n"
    "v_pk_mul_f32 v[118:119], v[118:119], v[112:113] op_sel_hi:[1,0]\n"
    "v_pk_mul_f32 v[120:121], v[120:121], v[112:113] op_sel_hi:[1,0]\n"
    "v_cvt_pk_bf16_f32 v118, v118, v119\n"
    "v_cvt_pk_bf16_f32 v119, v120, v121\n"
    "v_mfma_f32_16x16x32_bf16 v[44:47], a[220:223], a[36:39], v[44:47]\n"
    "v_fma_f32 v17, v17, s39, -v103\n"
    "v_fma_f32 v18, v18, s39, -v103\n"
    "v_mfma_f32_16x16x32_bf16 v[52:55], a[216:219], a[64:67], 0\n"
    "buffer_store_dwordx4 v[172:175], v110, s[28:31], s98 offen offset:0 sc1\n"
    "v_fma_f32 v19, v19, s39, -v103\n"
    "v_fma_f32 v20, v20, s39, -v103\n"
    "v_fma_f32 v21, v21, s39, -v103\n"
    "v_fma_f32 v22, v22, s39, -v103\n"
    "v_mfma_f32_16x16x32_bf16 v[52:55], a[220:223], a[68:71], v[52:55]\n"
    "v_fma_f32 v23, v23, s39, -v103\n"
    "v_max3_f32 v104, v104, v27, v28\n"
    "v_mfma_f32_16x16x32_bf16 v[60:63], a[216:219], a[96:99], 0\n"
    "ds_read_b128 a[216:219], v96 offset:43008\n"
    "ds_write2st64_b64 v111, v[114:115], v[118:119] offset0:5 offset1:7\n"
    "v_accvgpr_read_b32 v114, a136\n"
    "v_accvgpr_read_b32 v115, a137\n"
    "v_mfma_f32_16x16x32_bf16 v[60:63], a[220:223], a[100:103], v[60:63]\n"
    "ds_read_b128 a[220:223], v96 offset:43520\n"
    "s_waitcnt lgkmcnt(11)\n"
    "v_mfma_f32_16x16x32_bf16 v[32:35], a[240:243], a[8:11], v[32:35]\n"
    "v_accvgpr_read_b32 v116, a138\n"
    "v_accvgpr_read_b32 v117, a139\n"
    "v_accvgpr_read_b32 v118, a168\n"
    "v_accvgpr_read_b32 v119, a169\n"
    "v_accvgpr_read_b32 v120, a170\n"
    "v_pk_mul_f32 v[114:115], v[114:115], v[124:125] op_sel_hi:[1,0]\n"
    "v_pk_mul_f32 v[116:117], v[116:117], v[124:125] op_sel_hi:[1,0]\n"
    "v_cvt_pk_bf16_f32 v114, v114, v115\n"
    "v_cvt_pk_bf16_f32 v115, v116, v117\n"
    "v_mfma_f32_16x16x32_bf16 v[32:35], a[244:247], a[12:15], v[32:35]\n"
    "v_accvgpr_read_b32 v121, a171\n"
    "v_max3_f32 v104, v104, v29, v30\n"
    "v_mfma_f32_16x16x32_bf16 v[40:43], a[240:243], a[40:43], v[40:43]\n"
    "v_exp_f32 v16, v16\n"
    "v_exp_f32 v17, v17\n"
    "v_exp_f32 v18, v18\n"
    "v_pk_mul_f32 v[118:119], v[118:119], v[124:125] op_sel_hi:[1,0]\n"
    "v_pk_mul_f32 v[120:121], v[120:121], v[124:125] op_sel_hi:[1,0]\n"
    "v_cvt_pk_bf16_f32 v118, v118, v119\n"
    "v_mfma_f32_16x16x32_bf16 v[40:43], a[244:247], a[44:47], v[40:43]\n"
    "v_cvt_pk_bf16_f32 v119, v120, v121\n"
    "v_add_f32 v107, v107, v16\n"
    "v_mfma_f32_16x16x32_bf16 v[48:51], a[240:243], a[72:75], v[48:51]\n"
    "ds_write2st64_b64 v123, v[114:115], v[118:119] offset0:8 offset1:10\n"
    "v_accvgpr_read_b32 v114, a152\n"
    "v_accvgpr_read_b32 v115, a153\n"
    "v_mfma_f32_16x16x32_bf16 v[48:51], a[244:247], a[76:79], v[48:51]\n"
    "v_accvgpr_read_b32 v116, a154\n"
    "v_accvgpr_read_b32 v117, a155\n"
    "v_mfma_f32_16x16x32_bf16 v[56:59], a[240:243], a[104:107], v[56:59]\n"
    "buffer_store_dwordx4 v[188:191], v110, s[28:31], s99 offen offset:0 sc1\n"
    "ds_read_b128 a[240:243], v96 offset:35840\n"
    "v_pk_mul_f32 v[114:115], v[114:115], v[124:125] op_sel_hi:[1,0]\n"
    "v_pk_mul_f32 v[116:117], v[116:117], v[124:125] op_sel_hi:[1,0]\n"
    "v_cvt_pk_bf16_f32 v114, v114, v115\n"
    "v_mfma_f32_16x16x32_bf16 v[56:59], a[244:247], a[108:111], v[56:59]\n"
    "ds_read_b128 a[244:247], v96 offset:36352\n"
    "s_waitcnt lgkmcnt(9)\n"
    "v_mfma_f32_16x16x32_bf16 v[36:39], a[248:251], a[8:11], v[36:39]\n"
    "v_cvt_pk_bf16_f32 v115, v116, v117\n"
    "v_accvgpr_read_b32 v118, a184\n"
    "v_accvgpr_read_b32 v119, a185\n"
    "v_accvgpr_read_b32 v120, a186\n"
    "v_accvgpr_read_b32 v121, a187\n"
    "v_pk_mul_f32 v[118:119], v[118:119], v[124:125] op_sel_hi:[1,0]\n"
    "v_pk_mul_f32 v[120:121], v[120:121], v[124:125] op_sel_hi:[1,0]\n"
    "v_cvt_pk_bf16_f32 v118, v118, v119\n"
    "v_cvt_pk_bf16_f32 v119, v120, v121\n"
    "v_mfma_f32_16x16x32_bf16 v[36:39], a[252:255], a[12:15], v[36:39]\n"
    "v_exp_f32 v19, v19\n"
    "v_mfma_f32_16x16x32_bf16 v[44:47], a[248:251], a[40:43], v[44:47]\n"
    "ds_write2st64_b64 v111, v[114:115], v[118:119] offset0:9 offset1:11\n"
    "v_accvgpr_read_b32 v114, a140\n"
    "v_accvgpr_read_b32 v115, a141\n"
    "v_accvgpr_read_b32 v116, a142\n"
    "v_mfma_f32_16x16x32_bf16 v[44:47], a[252:255], a[44:47], v[44:47]\n"
    "v_accvgpr_read_b32 v117, a143\n"
    "v_accvgpr_read_b32 v118, a172\n"
    "v_mfma_f32_16x16x32_bf16 v[52:55], a[248:251], a[72:75], v[52:55]\n"
    "v_accvgpr_read_b32 v119, a173\n"
    "v_accvgpr_read_b32 v120, a174\n"
    "v_accvgpr_read_b32 v121, a175\n"
    "v_add_f32 v107, v107, v17\n"
    "v_cvt_pk_bf16_f32 v16, v16, v17\n"
    "v_pk_mul_f32 v[114:115], v[114:115], v[126:127] op_sel_hi:[1,0]\n"
    "v_pk_mul_f32 v[116:117], v[116:117], v[126:127] op_sel_hi:[1,0]\n"
    "v_pk_mul_f32 v[118:119], v[118:119], v[126:127] op_sel_hi:[1,0]\n"
    "v_cvt_pk_bf16_f32 v114, v114, v115\n"
    "v_mfma_f32_16x16x32_bf16 v[52:55], a[252:255], a[76:79], v[52:55]\n"
    "v_cvt_pk_bf16_f32 v115, v116, v117\n"
    "v_add_f32 v107, v107, v18\n"
    "v_mfma_f32_16x16x32_bf16 v[60:63], a[248:251], a[104:107], v[60:63]\n"
    "buffer_store_dwordx4 v[224:227], v110, s[28:31], s92 offen offset:128 sc1\n"
    "ds_read_b128 a[248:251], v96 offset:44032\n"
    "v_pk_mul_f32 v[120:121], v[120:121], v[126:127] op_sel_hi:[1,0]\n"
    "v_cvt_pk_bf16_f32 v118, v118, v119\n"
    "v_cvt_pk_bf16_f32 v119, v120, v121\n"
    "v_mfma_f32_16x16x32_bf16 v[60:63], a[252:255], a[108:111], v[60:63]\n"
    "ds_read_b128 a[252:255], v96 offset:44544\n"
    "s_waitcnt lgkmcnt(9)\n"
    "v_mfma_f32_16x16x32_bf16 v[32:35], a[208:211], a[16:19], v[32:35]\n"
    "ds_write2st64_b64 v123, v[114:115], v[118:119] offset0:12 offset1:14\n"
    "v_exp_f32 v20, v20\n"
    "v_add_f32 v107, v107, v19\n"
    "v_mfma_f32_16x16x32_bf16 v[32:35], a[212:215], a[20:23], v[32:35]\n"
    "v_exp_f32 v21, v21\n"
    "v_mfma_f32_16x16x32_bf16 v[40:43], a[208:211], a[48:51], v[40:43]\n"
    "v_cvt_pk_bf16_f32 v17, v18, v19\n"
    "v_exp_f32 v22, v22\n"
    "v_exp_f32 v23, v23\n"
    "v_add_f32 v107, v107, v20\n"
    "v_add_f32 v107, v107, v21\n"
    "v_cvt_pk_bf16_f32 v18, v20, v21\n"
    "v_mfma_f32_16x16x32_bf16 v[40:43], a[212:215], a[52:55], v[40:43]\n"
    "v_add_f32 v107, v107, v22\n"
    "v_add_f32 v107, v107, v23\n"
    "v_mfma_f32_16x16x32_bf16 v[48:51], a[208:211], a[80:83], v[48:51]\n"
    "v_cvt_pk_bf16_f32 v19, v22, v23\n"
    "v_max_f32 v104, v104, v31\n"
    "v_mov_b32 v125, v104\n"
    "v_mov_b32 v108, 0\n"
    "v_accvgpr_read_b32 v114, a156\n"
    "v_permlane32_swap_b32 v104, v125\n"
    "v_max_f32 v104, v104, v125\n"
    "v_mov_b32 v125, v104\n"
    "v_mfma_f32_16x16x32_bf16 v[48:51], a[212:215], a[84:87], v[48:51]\n"
    "s_nop 0\n"
    "v_permlane16_swap_b32 v104, v125\n"
    "v_max_f32 v104, v104, v125\n"
    "v_mfma_f32_16x16x32_bf16 v[56:59], a[208:211], a[112:115], v[56:59]\n"
    "v_mul_f32 v104, s39, v104\n"
    "v_add_f32 v104, s40, v104\n"
    "v_fma_f32 v24, v24, s39, -v104\n"
    "v_fma_f32 v25, v25, s39, -v104\n"
    "v_fma_f32 v26, v26, s39, -v104\n"
    "v_fma_f32 v27, v27, s39, -v104\n"
    "v_fma_f32 v28, v28, s39, -v104\n"
    "v_fma_f32 v29, v29, s39, -v104\n"
    "v_fma_f32 v30, v30, s39, -v104\n"
    "v_mfma_f32_16x16x32_bf16 v[56:59], a[212:215], a[116:119], v[56:59]\n"
    "v_fma_f32 v31, v31, s39, -v104\n"
    "s_waitcnt lgkmcnt(7)\n"
    "v_mfma_f32_16x16x32_bf16 v[36:39], a[216:219], a[16:19], v[36:39]\n"
    "buffer_store_dwordx4 v[240:243], v110, s[28:31], s93 offen offset:128 sc1\n"
    "v_exp_f32 v24, v24\n"
    "v_exp_f32 v25, v25\n"
    "v_mfma_f32_16x16x32_bf16 v[36:39], a[220:223], a[20:23], v[36:39]\n"
    "v_exp_f32 v26, v26\n"
    "v_mfma_f32_16x16x32_bf16 v[44:47], a[216:219], a[48:51], v[44:47]\n"
    "v_exp_f32 v27, v27\n"
    "v_add_f32 v108, v108, v24\n"
    "v_add_f32 v108, v108, v25\n"
    "v_cvt_pk_bf16_f32 v24, v24, v25\n"
    "v_exp_f32 v28, v28\n"
    "v_add_f32 v108, v108, v26\n"
    "v_mfma_f32_16x16x32_bf16 v[44:47], a[220:223], a[52:55], v[44:47]\n"
    "v_exp_f32 v29, v29\n"
    "v_mfma_f32_16x16x32_bf16 v[52:55], a[216:219], a[80:83], v[52:55]\n"
    "s_waitcnt vmcnt(18) lgkmcnt(0)\n"
    "s_barrier\n"
    "ds_read_b64_tr_b16 a[208:209], v97 offset:2048\n"
    "ds_read_b64_tr_b16 a[210:211], v97 offset:10240\n"
    "ds_read_b64_tr_b16 a[212:213], v98 offset:2048\n"
    "ds_read_b64_tr_b16 a[214:215], v98 offset:10240\n"
    "v_add_f32 v108, v108, v27\n"
    "v_cvt_pk_bf16_f32 v25, v26, v27\n"
    "v_add_f32 v108, v108, v28\n"
    "v_mfma_f32_16x16x32_bf16 v[52:55], a[220:223], a[84:87], v[52:55]\n"
    "v_exp_f32 v30, v30\n"
    "v_mfma_f32_16x16x32_bf16 v[60:63], a[216:219], a[112:115], v[60:63]\n"
    "s_add_u32 m0, s38, 32768\n"
    "s_mov_b32 s76, 327680\n"
    "ds_read_b64_tr_b16 a[216:217], v97 offset:2560\n"
    "ds_read_b64_tr_b16 a[218:219], v97 offset:10752\n"
    "v_exp_f32 v31, v31\n"
    "v_add_f32 v108, v108, v29\n"
    "v_cvt_pk_bf16_f32 v26, v28, v29\n"
    "v_mfma_f32_16x16x32_bf16 v[60:63], a[220:223], a[116:119], v[60:63]\n"
    "buffer_load_dwordx4 v99, s[16:19], s76 offen offset:0 nt lds\n"
    "v_mfma_f32_16x16x32_bf16 v[32:35], a[240:243], a[24:27], v[32:35]\n"
    "buffer_store_dwordx4 v[228:231], v110, s[28:31], s94 offen offset:128 sc1\n"
    "ds_read_b64_tr_b16 a[220:221], v98 offset:2560\n"
    "ds_read_b64_tr_b16 a[222:223], v98 offset:10752\n"
    "v_add_f32 v108, v108, v30\n"
    "v_add_f32 v108, v108, v31\n"
    "v_mfma_f32_16x16x32_bf16 v[32:35], a[244:247], a[28:31], v[32:35]\n"
    "v_cvt_pk_bf16_f32 v27, v30, v31\n"
    "v_accvgpr_read_b32 v115, a157\n"
    "v_mfma_f32_16x16x32_bf16 v[40:43], a[240:243], a[56:59], v[40:43]\n"
    "v_accvgpr_read_b32 v116, a158\n"
    "v_accvgpr_read_b32 v117, a159\n"
    "v_accvgpr_read_b32 v118, a188\n"
    "v_accvgpr_read_b32 v119, a189\n"
    "v_accvgpr_read_b32 v120, a190\n"
    "v_pk_mul_f32 v[114:115], v[114:115], v[126:127] op_sel_hi:[1,0]\n"
    "v_pk_mul_f32 v[116:117], v[116:117], v[126:127] op_sel_hi:[1,0]\n"
    "v_cvt_pk_bf16_f32 v114, v114, v115\n"
    "v_mfma_f32_16x16x32_bf16 v[40:43], a[244:247], a[60:63], v[40:43]\n"
    "v_cvt_pk_bf16_f32 v115, v116, v117\n"
    "v_accvgpr_read_b32 v121, a191\n"
    "v_mfma_f32_16x16x32_bf16 v[48:51], a[240:243], a[88:91], v[48:51]\n"
    "s_add_u32 m0, s38, 33664\n"
    "s_mov_b32 s24, s28\n"
    "s_mov_b32 s25, s29\n"
    "v_cvt_pk_bf16_f32 v80, v80, v81\n"
    "v_cvt_pk_bf16_f32 v81, v82, v83\n"
    "v_pk_mul_f32 v[118:119], v[118:119], v[126:127] op_sel_hi:[1,0]\n"
    "v_pk_mul_f32 v[120:121], v[120:121], v[126:127] op_sel_hi:[1,0]\n"
    "v_pk_mul_f32 v[70:71], v[70:71], v[112:113] op_sel_hi:[1,0]\n"
    "v_pk_mul_f32 v[84:85], v[84:85], v[112:113] op_sel_hi:[1,0]\n"
    "v_cvt_pk_bf16_f32 v118, v118, v119\n"
    "v_mfma_f32_16x16x32_bf16 v[48:51], a[244:247], a[92:95], v[48:51]\n"
    "buffer_load_dwordx4 v99, s[16:19], s76 offen offset:128 nt lds\n"
    "v_cvt_pk_bf16_f32 v119, v120, v121\n"
    "v_mfma_f32_16x16x32_bf16 v[56:59], a[240:243], a[120:123], v[56:59]\n"
    "ds_write2st64_b64 v111, v[114:115], v[118:119] offset0:13 offset1:15\n"
    "ds_read_b64_tr_b16 a[240:241], v97 offset:3072\n"
    "ds_read_b64_tr_b16 a[242:243], v97 offset:11264\n"
    "v_mfma_f32_16x16x32_bf16 v[56:59], a[244:247], a[124:127], v[56:59]\n"
    "ds_read_b64_tr_b16 a[244:245], v97 offset:3584\n"
    "v_mfma_f32_16x16x32_bf16 v[36:39], a[248:251], a[24:27], v[36:39]\n"
    "v_accvgpr_read_b32 v114, a192\n"
    "v_accvgpr_read_b32 v115, a193\n"
    "v_accvgpr_read_b32 v116, a194\n"
    "v_accvgpr_read_b32 v117, a195\n"
    "v_accvgpr_read_b32 v118, a224\n"
    "v_pk_mul_f32 v[114:115], v[114:115], v[122:123] op_sel_hi:[1,0]\n"
    "v_pk_mul_f32 v[116:117], v[116:117], v[122:123] op_sel_hi:[1,0]\n"
    "v_cvt_pk_bf16_f32 v114, v114, v115\n"
    "v_mfma_f32_16x16x32_bf16 v[36:39], a[252:255], a[28:31], v[36:39]\n"
    "v_cvt_pk_bf16_f32 v115, v116, v117\n"
    "v_accvgpr_read_b32 v119, a225\n"
    "v_mfma_f32_16x16x32_bf16 v[44:47], a[248:251], a[56:59], v[44:47]\n"
    "s_add_u32 m0, s38, 34560\n"
    "buffer_store_dwordx4 v[244:247], v110, s[28:31], s95 offen offset:128 sc1\n"
    "v_accvgpr_read_b32 v120, a226\n"
    "v_accvgpr_read_b32 v121, a227\n"
    "v_pk_mul_f32 v[118:119], v[118:119], v[122:123] op_sel_hi:[1,0]\n"
    "v_pk_mul_f32 v[120:121], v[120:121], v[122:123] op_sel_hi:[1,0]\n"
    "v_cvt_pk_bf16_f32 v118, v118, v119\n"
    "v_mfma_f32_16x16x32_bf16 v[44:47], a[252:255], a[60:63], v[44:47]\n"
    "buffer_load_dwordx4 v99, s[16:19], s76 offen offset:256 nt lds\n"
    "v_cvt_pk_bf16_f32 v119, v120, v121\n"
    "v_mfma_f32_16x16x32_bf16 v[52:55], a[248:251], a[88:91], v[52:55]\n"
    "ds_write2st64_b64 v123, v[114:115], v[118:119] offset0:16 offset1:18\n"
    "ds_read_b64_tr_b16 a[246:247], v97 offset:11776\n"
    "v_accvgpr_read_b32 v114, a196\n"
    "v_mfma_f32_16x16x32_bf16 v[52:55], a[252:255], a[92:95], v[52:55]\n"
    "v_accvgpr_read_b32 v115, a197\n"
    "v_accvgpr_read_b32 v116, a198\n"
    "v_mfma_f32_16x16x32_bf16 v[60:63], a[248:251], a[120:123], v[60:63]\n"
    "ds_write2st64_b64 v111, v[64:65], v[80:81] offset0:17 offset1:19\n"
    "v_accvgpr_read_b32 v117, a199\n"
    "v_pk_mul_f32 v[114:115], v[114:115], v[112:113] op_sel_hi:[1,0]\n"
    "v_pk_mul_f32 v[116:117], v[116:117], v[112:113] op_sel_hi:[1,0]\n"
    "v_cvt_pk_bf16_f32 v114, v114, v115\n"
    "v_mfma_f32_16x16x32_bf16 v[60:63], a[252:255], a[124:127], v[60:63]\n"
    "v_cvt_pk_bf16_f32 v115, v116, v117\n"
    "s_waitcnt lgkmcnt(11)\n"
    "v_mfma_f32_16x16x32_bf16 a[128:131], a[208:211], v[0:3], 0\n"
    "v_accvgpr_read_b32 v118, a228\n"
    "v_accvgpr_read_b32 v119, a229\n"
    "v_accvgpr_read_b32 v120, a230\n"
    "v_accvgpr_read_b32 v121, a231\n"
    "v_cvt_pk_bf16_f32 v68, v68, v69\n"
    "v_pk_mul_f32 v[118:119], v[118:119], v[112:113] op_sel_hi:[1,0]\n"
    "v_pk_mul_f32 v[120:121], v[120:121], v[112:113] op_sel_hi:[1,0]\n"
    "v_cvt_pk_bf16_f32 v118, v118, v119\n"
    "v_mfma_f32_16x16x32_bf16 a[132:135], a[208:211], v[8:11], 0\n"
    "s_add_u32 m0, s38, 35456\n"
    "v_cvt_pk_bf16_f32 v119, v120, v121\n"
    "v_mfma_f32_16x16x32_bf16 a[136:139], a[208:211], v[16:19], 0\n"
    "buffer_load_dwordx4 v99, s[16:19], s76 offen offset:384 nt lds\n"
    "buffer_store_dwordx4 v[232:235], v110, s[28:31], s96 offen offset:128 sc1\n"
    "v_cvt_pk_bf16_f32 v69, v70, v71\n"
    "v_pk_mul_f32 v[86:87], v[86:87], v[112:113] op_sel_hi:[1,0]\n"
    "v_cvt_pk_bf16_f32 v84, v84, v85\n"
    "v_mfma_f32_16x16x32_bf16 a[140:143], a[208:211], v[24:27], 0\n"
    "ds_read_b64_tr_b16 a[248:249], v98 offset:3072\n"
    "v_cvt_pk_bf16_f32 v85, v86, v87\n"
    "v_mfma_f32_16x16x32_bf16 a[156:159], a[212:215], v[24:27], 0\n"
    "ds_write2st64_b64 v123, v[114:115], v[118:119] offset0:20 offset1:22\n"
    "ds_read_b64_tr_b16 a[250:251], v98 offset:11264\n"
    "ds_read_b64_tr_b16 a[252:253], v98 offset:3584\n"
    "v_accvgpr_read_b32 v114, a200\n"
    "v_mfma_f32_16x16x32_bf16 a[152:155], a[212:215], v[16:19], 0\n"
    "v_accvgpr_read_b32 v115, a201\n"
    "v_accvgpr_read_b32 v116, a202\n"
    "v_mfma_f32_16x16x32_bf16 a[148:151], a[212:215], v[8:11], 0\n"
    "ds_write2st64_b64 v111, v[68:69], v[84:85] offset0:21 offset1:23\n"
    "v_accvgpr_read_b32 v117, a203\n"
    "v_pk_mul_f32 v[114:115], v[114:115], v[124:125] op_sel_hi:[1,0]\n"
    "v_pk_mul_f32 v[116:117], v[116:117], v[124:125] op_sel_hi:[1,0]\n"
    "v_cvt_pk_bf16_f32 v114, v114, v115\n"
    "v_mfma_f32_16x16x32_bf16 a[144:147], a[212:215], v[0:3], 0\n"
    "v_cvt_pk_bf16_f32 v115, v116, v117\n"
    "s_waitcnt lgkmcnt(12)\n"
    "v_mfma_f32_16x16x32_bf16 a[160:163], a[216:219], v[0:3], 0\n"
    "ds_read_b64_tr_b16 a[254:255], v98 offset:11776\n"
    "v_accvgpr_read_b32 v118, a232\n"
    "v_accvgpr_read_b32 v119, a233\n"
    "v_accvgpr_read_b32 v120, a234\n"
    "v_accvgpr_read_b32 v121, a235\n"
    "v_pk_mul_f32 v[118:119], v[118:119], v[124:125] op_sel_hi:[1,0]\n"
    "v_pk_mul_f32 v[120:121], v[120:121], v[124:125] op_sel_hi:[1,0]\n"
    "v_cvt_pk_bf16_f32 v118, v118, v119\n"
    "v_mfma_f32_16x16x32_bf16 a[164:167], a[216:219], v[8:11], 0\n"
    "s_add_u32 m0, s38, 81920\n"
    "s_mov_b32 s77, 327680\n"
    "v_mfma_f32_16x16x32_bf16 a[168:171], a[216:219], v[16:19], 0\n"
    "buffer_load_dwordx4 v100, s[20:23], s77 offen offset:0 nt lds\n"
    "ds_read_b64_tr_b16 a[208:209], v97 offset:0\n"
    "ds_read_b64_tr_b16 a[210:211], v97 offset:8192\n"
    "ds_read_b64_tr_b16 a[212:213], v98 offset:0\n"
    "v_cvt_pk_bf16_f32 v119, v120, v121\n"
    "v_pk_mul_f32 v[72:73], v[72:73], v[124:125] op_sel_hi:[1,0]\n"
    "v_pk_mul_f32 v[74:75], v[74:75], v[124:125] op_sel_hi:[1,0]\n"
    "v_pk_mul_f32 v[88:89], v[88:89], v[124:125] op_sel_hi:[1,0]\n"
    "v_cvt_pk_bf16_f32 v72, v72, v73\n"
    "v_mfma_f32_16x16x32_bf16 a[172:175], a[216:219], v[24:27], 0\n"
    "ds_read_b64_tr_b16 a[214:215], v98 offset:8192\n"
    "v_cvt_pk_bf16_f32 v73, v74, v75\n"
    "v_mfma_f32_16x16x32_bf16 a[188:191], a[220:223], v[24:27], 0\n"
    "buffer_store_dwordx4 v[248:251], v110, s[28:31], s97 offen offset:128 sc1\n"
    "ds_read_b64_tr_b16 a[216:217], v97 offset:1024\n"
    "v_pk_mul_f32 v[90:91], v[90:91], v[124:125] op_sel_hi:[1,0]\n"
    "v_pk_mul_f32 v[76:77], v[76:77], v[126:127] op_sel_hi:[1,0]\n"
    "v_cvt_pk_bf16_f32 v88, v88, v89\n"
    "v_mfma_f32_16x16x32_bf16 a[184:187], a[220:223], v[16:19], 0\n"
    "ds_read_b64_tr_b16 a[218:219], v97 offset:9216\n"
    "v_cvt_pk_bf16_f32 v89, v90, v91\n"
    "v_mfma_f32_16x16x32_bf16 a[180:183], a[220:223], v[8:11], 0\n"
    "ds_write2st64_b64 v123, v[114:115], v[118:119] offset0:24 offset1:26\n"
    "v_accvgpr_read_b32 v114, a204\n"
    "v_accvgpr_read_b32 v115, a205\n"
    "v_mfma_f32_16x16x32_bf16 a[176:179], a[220:223], v[0:3], 0\n"
    "v_accvgpr_read_b32 v116, a206\n"
    "s_waitcnt lgkmcnt(14)\n"
    "v_mfma_f32_16x16x32_bf16 a[192:195], a[240:243], v[0:3], 0\n"
    "v_accvgpr_read_b32 v117, a207\n"
    "v_accvgpr_read_b32 v118, a236\n"
    "v_accvgpr_read_b32 v119, a237\n"
    "v_accvgpr_read_b32 v120, a238\n"
    "v_accvgpr_read_b32 v121, a239\n"
    "v_pk_mul_f32 v[114:115], v[114:115], v[126:127] op_sel_hi:[1,0]\n"
    "v_pk_mul_f32 v[116:117], v[116:117], v[126:127] op_sel_hi:[1,0]\n"
    "v_cvt_pk_bf16_f32 v114, v114, v115\n"
    "v_cvt_pk_bf16_f32 v115, v116, v117\n"
    "v_mfma_f32_16x16x32_bf16 a[196:199], a[240:243], v[8:11], 0\n"
    "s_add_u32 m0, s38, 82816\n"
    "ds_read_b64_tr_b16 a[220:221], v98 offset:1024\n"
    "v_mfma_f32_16x16x32_bf16 a[200:203], a[240:243], v[16:19], 0\n"
    "buffer_load_dwordx4 v100, s[20:23], s77 offen offset:128 nt lds\n"
    "ds_write2st64_b64 v111, v[72:73], v[88:89] offset0:25 offset1:27\n"
    "v_pk_mul_f32 v[118:119], v[118:119], v[126:127] op_sel_hi:[1,0]\n"
    "v_pk_mul_f32 v[120:121], v[120:121], v[126:127] op_sel_hi:[1,0]\n"
    "v_cvt_pk_bf16_f32 v118, v118, v119\n"
    "v_mfma_f32_16x16x32_bf16 a[204:207], a[240:243], v[24:27], 0\n"
    "ds_read_b64_tr_b16 a[222:223], v98 offset:9216\n"
    "v_cvt_pk_bf16_f32 v119, v120, v121\n"
    "v_mfma_f32_16x16x32_bf16 a[236:239], a[244:247], v[24:27], 0\n"
    "buffer_store_dwordx4 v[236:239], v110, s[28:31], s98 offen offset:128 sc1\n"
    "ds_read_b64_tr_b16 a[240:241], v97 offset:512\n"
    "v_pk_mul_f32 v[78:79], v[78:79], v[126:127] op_sel_hi:[1,0]\n"
    "v_pk_mul_f32 v[92:93], v[92:93], v[126:127] op_sel_hi:[1,0]\n"
    "v_cvt_pk_bf16_f32 v76, v76, v77\n"
    "v_mfma_f32_16x16x32_bf16 a[232:235], a[244:247], v[16:19], 0\n"
    "ds_read_b64_tr_b16 a[242:243], v97 offset:8704\n"
    "v_cvt_pk_bf16_f32 v77, v78, v79\n"
    "v_mfma_f32_16x16x32_bf16 a[228:231], a[244:247], v[8:11], 0\n"
    "ds_write2st64_b64 v123, v[114:115], v[118:119] offset0:28 offset1:30\n"
    "v_pk_mul_f32 v[94:95], v[94:95], v[126:127] op_sel_hi:[1,0]\n"
    "v_cvt_pk_bf16_f32 v92, v92, v93\n"
    "v_cvt_pk_bf16_f32 v93, v94, v95\n"
    "v_mfma_f32_16x16x32_bf16 a[224:227], a[244:247], v[0:3], 0\n"
    "ds_read_b64_tr_b16 a[244:245], v98 offset:512\n"
    "ds_read_b64_tr_b16 a[246:247], v98 offset:8704\n"
    "v_fma_f32 v32, v32, s39, -v101\n"
    "v_fma_f32 v33, v33, s39, -v101\n"
    "s_waitcnt lgkmcnt(15)\n"
    "v_mfma_f32_16x16x32_bf16 v[64:67], a[248:251], v[0:3], 0\n"
    "ds_write2st64_b64 v111, v[76:77], v[92:93] offset0:29 offset1:31\n"
    "v_mfma_f32_16x16x32_bf16 v[68:71], a[248:251], v[8:11], 0\n"
    "s_add_u32 m0, s38, 83712\n"
    "v_fma_f32 v34, v34, s39, -v101\n"
    "v_fma_f32 v35, v35, s39, -v101\n"
    "v_mfma_f32_16x16x32_bf16 v[72:75], a[248:251], v[16:19], 0\n"
    "buffer_load_dwordx4 v100, s[20:23], s77 offen offset:256 nt lds\n"
    "v_exp_f32 v32, v32\n"
    "v_mfma_f32_16x16x32_bf16 v[76:79], a[248:251], v[24:27], 0\n"
    "ds_read_b64_tr_b16 a[248:249], v97 offset:1536\n"
    "ds_read_b64_tr_b16 a[250:251], v97 offset:9728\n"
    "v_exp_f32 v33, v33\n"
    "v_mfma_f32_16x16x32_bf16 v[92:95], a[252:255], v[24:27], 0\n"
    "v_exp_f32 v34, v34\n"
    "v_add_f32 v109, v32, v33\n"
    "v_mfma_f32_16x16x32_bf16 v[88:91], a[252:255], v[16:19], 0\n"
    "v_exp_f32 v35, v35\n"
    "v_cvt_pk_bf16_f32 v32, v32, v33\n"
    "v_mfma_f32_16x16x32_bf16 v[84:87], a[252:255], v[8:11], 0\n"
    "v_add_f32 v109, v109, v34\n"
    "v_add_f32 v109, v109, v35\n"
    "v_cvt_pk_bf16_f32 v33, v34, v35\n"
    "v_mfma_f32_16x16x32_bf16 v[80:83], a[252:255], v[0:3], 0\n"
    "buffer_store_dwordx4 v[252:255], v110, s[28:31], s99 offen offset:128 sc1\n"
    "s_waitcnt lgkmcnt(14)\n"
    "v_mfma_f32_16x16x32_bf16 v[128:131], a[208:211], v[0:3], 0\n"
    "ds_read_b64_tr_b16 a[252:253], v98 offset:1536\n"
    "ds_read_b64_tr_b16 a[254:255], v98 offset:9728\n"
    "v_mfma_f32_16x16x32_bf16 v[132:135], a[208:211], v[8:11], 0\n"
    "s_add_u32 m0, s38, 84608\n"
    "v_fma_f32 v36, v36, s39, -v101\n"
    "v_fma_f32 v37, v37, s39, -v101\n"
    "v_mfma_f32_16x16x32_bf16 v[136:139], a[208:211], v[16:19], 0\n"
    "buffer_load_dwordx4 v100, s[20:23], s77 offen offset:384 nt lds\n"
    "v_fma_f32 v38, v38, s39, -v101\n"
    "v_fma_f32 v39, v39, s39, -v101\n"
    "v_mfma_f32_16x16x32_bf16 v[140:143], a[208:211], v[24:27], 0\n"
    "ds_read_b128 a[208:211], v96 offset:0\n"
    "v_exp_f32 v36, v36\n"
    "v_mfma_f32_16x16x32_bf16 v[156:159], a[212:215], v[24:27], 0\n"
    "v_exp_f32 v37, v37\n"
    "v_exp_f32 v38, v38\n"
    "v_mfma_f32_16x16x32_bf16 v[152:155], a[212:215], v[16:19], 0\n"
    "v_cvt_pk_bf16_f32 v34, v36, v37\n"
    "v_add_f32 v36, v109, v36\n"
    "v_add_f32 v36, v36, v37\n"
    "v_mfma_f32_16x16x32_bf16 v[148:151], a[212:215], v[8:11], 0\n"
    "v_exp_f32 v39, v39\n"
    "v_add_f32 v36, v36, v38\n"
    "v_mfma_f32_16x16x32_bf16 v[144:147], a[212:215], v[0:3], 0\n"
    "ds_read_b128 a[212:215], v96 offset:512\n"
    "v_cvt_pk_bf16_f32 v35, v38, v39\n"
    "s_waitcnt lgkmcnt(12)\n"
    "v_mfma_f32_16x16x32_bf16 v[192:195], a[216:219], v[0:3], 0\n"
    "s_add_u32 s34, s38, 0\n"
    "s_mov_b32 s35, 16384\n"
    "s_mov_b32 s36, 344064\n"
    "v_add_f32 v36, v36, v39\n"
    "v_mfma_f32_16x16x32_bf16 v[196:199], a[216:219], v[8:11], 0\n"
    "v_fma_f32 v40, v40, s39, -v102\n"
    "v_fma_f32 v41, v41, s39, -v102\n"
    "v_fma_f32 v42, v42, s39, -v102\n"
    "v_mfma_f32_16x16x32_bf16 v[200:203], a[216:219], v[16:19], 0\n"
    "v_fma_f32 v43, v43, s39, -v102\n"
    "v_exp_f32 v40, v40\n"
    "v_mfma_f32_16x16x32_bf16 v[204:207], a[216:219], v[24:27], 0\n"
    "ds_read_b128 a[216:219], v96 offset:8192\n"
    "v_exp_f32 v41, v41\n"
    "v_mfma_f32_16x16x32_bf16 v[220:223], a[220:223], v[24:27], 0\n"
    "v_exp_f32 v42, v42\n"
    "v_add_f32 v109, v40, v41\n"
    "v_mfma_f32_16x16x32_bf16 v[216:219], a[220:223], v[16:19], 0\n"
    "v_exp_f32 v43, v43\n"
    "v_cvt_pk_bf16_f32 v40, v40, v41\n"
    "v_add_f32 v109, v109, v42\n"
    "v_mfma_f32_16x16x32_bf16 v[212:215], a[220:223], v[8:11], 0\n"
    "v_add_f32 v109, v109, v43\n"
    "v_cvt_pk_bf16_f32 v41, v42, v43\n"
    "v_fma_f32 v44, v44, s39, -v102\n"
    "v_mfma_f32_16x16x32_bf16 v[208:211], a[220:223], v[0:3], 0\n"
    "ds_read_b128 a[220:223], v96 offset:8704\n"
    "v_fma_f32 v45, v45, s39, -v102\n"
    "s_waitcnt lgkmcnt(9)\n"
    "v_mfma_f32_16x16x32_bf16 v[160:163], a[240:243], v[0:3], 0\n"
    "v_fma_f32 v46, v46, s39, -v102\n"
    "v_fma_f32 v47, v47, s39, -v102\n"
    "v_fma_f32 v48, v48, s39, -v103\n"
    "v_mfma_f32_16x16x32_bf16 v[164:167], a[240:243], v[8:11], 0\n"
    "v_exp_f32 v44, v44\n"
    "v_exp_f32 v45, v45\n"
    "v_mfma_f32_16x16x32_bf16 v[168:171], a[240:243], v[16:19], 0\n"
    "v_exp_f32 v46, v46\n"
    "v_cvt_pk_bf16_f32 v42, v44, v45\n"
    "v_mfma_f32_16x16x32_bf16 v[172:175], a[240:243], v[24:27], 0\n"
    "v_add_f32 v44, v109, v44\n"
    "v_exp_f32 v47, v47\n"
    "v_mfma_f32_16x16x32_bf16 v[188:191], a[244:247], v[24:27], 0\n"
    "v_add_f32 v44, v44, v45\n"
    "v_add_f32 v44, v44, v46\n"
    "v_cvt_pk_bf16_f32 v43, v46, v47\n"
    "v_mfma_f32_16x16x32_bf16 v[184:187], a[244:247], v[16:19], 0\n"
    "v_add_f32 v44, v44, v47\n"
    "v_fma_f32 v49, v49, s39, -v103\n"
    "v_fma_f32 v50, v50, s39, -v103\n"
    "v_fma_f32 v51, v51, s39, -v103\n"
    "v_mfma_f32_16x16x32_bf16 v[180:183], a[244:247], v[8:11], 0\n"
    "s_mov_b32 s27, s31\n"
    "v_exp_f32 v48, v48\n"
    "v_mfma_f32_16x16x32_bf16 v[176:179], a[244:247], v[0:3], 0\n"
    "v_exp_f32 v49, v49\n"
    "s_waitcnt lgkmcnt(4)\n"
    "v_mfma_f32_16x16x32_bf16 v[224:227], a[248:251], v[0:3], 0\n"
    "v_exp_f32 v50, v50\n"
    "v_add_f32 v109, v48, v49\n"
    "v_mfma_f32_16x16x32_bf16 v[228:231], a[248:251], v[8:11], 0\n"
    "v_exp_f32 v51, v51\n"
    "v_cvt_pk_bf16_f32 v48, v48, v49\n"
    "v_mfma_f32_16x16x32_bf16 v[232:235], a[248:251], v[16:19], 0\n"
    "s_mov_b32 s37, 4\n"
    "s_mov_b32 s85, s69\n"
    "v_add_f32 v109, v109, v50\n"
    "v_add_f32 v109, v109, v51\n"
    "v_mfma_f32_16x16x32_bf16 v[236:239], a[248:251], v[24:27], 0\n"
    "v_mfma_f32_16x16x32_bf16 v[252:255], a[252:255], v[24:27], 0\n"
    "v_mfma_f32_16x16x32_bf16 v[248:251], a[252:255], v[16:19], 0\n"
    "v_mfma_f32_16x16x32_bf16 v[244:247], a[252:255], v[8:11], 0\n"
    "v_mfma_f32_16x16x32_bf16 v[240:243], a[252:255], v[0:3], 0\n"
    "s_waitcnt lgkmcnt(2)\n"
    "v_subrev_u32 v113, 2048, v113\n"
    ".p2align 6\n"
    "L_loop1:\n"
    "v_mfma_f32_16x16x32_bf16 v[0:3], a[208:211], a[0:3], 0\n"
    "ds_read_b128 a[240:243], v96 offset:1024\n"
    "ds_read_b128 a[244:247], v96 offset:1536\n"
    "v_mfma_f32_16x16x32_bf16 v[0:3], a[212:215], a[4:7], v[0:3]\n"
    "ds_read_b128 a[248:251], v96 offset:9216\n"
    "ds_read_b128 a[252:255], v96 offset:9728\n"
    "v_mfma_f32_16x16x32_bf16 v[8:11], a[208:211], a[32:35], 0\n"
    "v_add_u32 v97, s35, v97\n"
    "v_add_u32 v98, s35, v98\n"
    "v_mfma_f32_16x16x32_bf16 v[8:11], a[212:215], a[36:39], v[8:11]\n"
    "v_cvt_pk_bf16_f32 v49, v50, v51\n"
    "v_fma_f32 v52, v52, s39, -v103\n"
    "v_mfma_f32_16x16x32_bf16 v[16:19], a[208:211], a[64:67], 0\n"
    "v_fma_f32 v53, v53, s39, -v103\n"
    "v_fma_f32 v54, v54, s39, -v103\n"
    "v_mfma_f32_16x16x32_bf16 v[16:19], a[212:215], a[68:71], v[16:19]\n"
    "v_fma_f32 v55, v55, s39, -v103\n"
    "v_fma_f32 v56, v56, s39, -v104\n"
    "v_mfma_f32_16x16x32_bf16 v[24:27], a[208:211], a[96:99], 0\n"
    "ds_read_b128 a[208:211], v96 offset:2048\n"
    "v_fma_f32 v57, v57, s39, -v104\n"
    "v_mfma_f32_16x16x32_bf16 v[24:27], a[212:215], a[100:103], v[24:27]\n"
    "ds_read_b128 a[212:215], v96 offset:2560\n"
    "s_waitcnt lgkmcnt(6)\n"
    "v_mfma_f32_16x16x32_bf16 v[4:7], a[216:219], a[0:3], 0\n"
    "v_exp_f32 v52, v52\n"
    "v_mfma_f32_16x16x32_bf16 v[4:7], a[220:223], a[4:7], v[4:7]\n"
    "v_exp_f32 v53, v53\n"
    "v_mfma_f32_16x16x32_bf16 v[12:15], a[216:219], a[32:35], 0\n"
    "ds_read_b128 v[114:117], v113 offset:0\n"
    "v_cvt_pk_bf16_f32 v50, v52, v53\n"
    "v_mfma_f32_16x16x32_bf16 v[12:15], a[220:223], a[36:39], v[12:15]\n"
    "v_exp_f32 v54, v54\n"
    "v_mfma_f32_16x16x32_bf16 v[20:23], a[216:219], a[64:67], 0\n"
    "v_add_f32 v52, v109, v52\n"
    "v_add_f32 v52, v52, v53\n"
    "v_mfma_f32_16x16x32_bf16 v[20:23], a[220:223], a[68:71], v[20:23]\n"
    "v_exp_f32 v55, v55\n"
    "v_mfma_f32_16x16x32_bf16 v[28:31], a[216:219], a[96:99], 0\n"
    "ds_read_b128 a[216:219], v96 offset:10240\n"
    "v_add_f32 v52, v52, v54\n"
    "v_mfma_f32_16x16x32_bf16 v[28:31], a[220:223], a[100:103], v[28:31]\n"
    "ds_read_b128 a[220:223], v96 offset:10752\n"
    "s_waitcnt lgkmcnt(7)\n"
    "v_mfma_f32_16x16x32_bf16 v[0:3], a[240:243], a[8:11], v[0:3]\n"
    "s_waitcnt lgkmcnt(2)\n"
    "buffer_store_dwordx4 v[114:117], v110, s[24:27], s85 offen offset:256 sc1\n"
    "v_mfma_f32_16x16x32_bf16 v[0:3], a[244:247], a[12:15], v[0:3]\n"
    "v_cvt_pk_bf16_f32 v51, v54, v55\n"
    "v_add_f32 v52, v52, v55\n"
    "v_mfma_f32_16x16x32_bf16 v[8:11], a[240:243], a[40:43], v[8:11]\n"
    "v_fma_f32 v58, v58, s39, -v104\n"
    "v_fma_f32 v59, v59, s39, -v104\n"
    "v_mfma_f32_16x16x32_bf16 v[8:11], a[244:247], a[44:47], v[8:11]\n"
    "v_exp_f32 v56, v56\n"
    "v_mfma_f32_16x16x32_bf16 v[16:19], a[240:243], a[72:75], v[16:19]\n"
    "v_exp_f32 v57, v57\n"
    "v_mfma_f32_16x16x32_bf16 v[16:19], a[244:247], a[76:79], v[16:19]\n"
    "v_exp_f32 v58, v58\n"
    "v_mfma_f32_16x16x32_bf16 v[24:27], a[240:243], a[104:107], v[24:27]\n"
    "ds_read_b128 a[240:243], v96 offset:3072\n"
    "v_add_f32 v109, v56, v57\n"
    "v_mfma_f32_16x16x32_bf16 v[24:27], a[244:247], a[108:111], v[24:27]\n"
    "ds_read_b128 a[244:247], v96 offset:3584\n"
    "v_mfma_f32_16x16x32_bf16 v[4:7], a[248:251], a[8:11], v[4:7]\n"
    "v_exp_f32 v59, v59\n"
    "v_mfma_f32_16x16x32_bf16 v[4:7], a[252:255], a[12:15], v[4:7]\n"
    "v_cvt_pk_bf16_f32 v56, v56, v57\n"
    "v_add_f32 v109, v109, v58\n"
    "v_mfma_f32_16x16x32_bf16 v[12:15], a[248:251], a[40:43], v[12:15]\n"
    "v_add_f32 v109, v109, v59\n"
    "v_cvt_pk_bf16_f32 v57, v58, v59\n"
    "v_mfma_f32_16x16x32_bf16 v[12:15], a[252:255], a[44:47], v[12:15]\n"
    "v_fma_f32 v60, v60, s39, -v104\n"
    "v_fma_f32 v61, v61, s39, -v104\n"
    "v_mfma_f32_16x16x32_bf16 v[20:23], a[248:251], a[72:75], v[20:23]\n"
    "v_fma_f32 v62, v62, s39, -v104\n"
    "v_fma_f32 v63, v63, s39, -v104\n"
    "v_mfma_f32_16x16x32_bf16 v[20:23], a[252:255], a[76:79], v[20:23]\n"
    "v_exp_f32 v60, v60\n"
    "v_mfma_f32_16x16x32_bf16 v[28:31], a[248:251], a[104:107], v[28:31]\n"
    "ds_read_b128 a[248:251], v96 offset:11264\n"
    "v_mfma_f32_16x16x32_bf16 v[28:31], a[252:255], a[108:111], v[28:31]\n"
    "ds_read_b128 a[252:255], v96 offset:11776\n"
    "v_mfma_f32_16x16x32_bf16 v[0:3], a[208:211], a[16:19], v[0:3]\n"
    "v_exp_f32 v61, v61\n"
    "v_mfma_f32_16x16x32_bf16 v[0:3], a[212:215], a[20:23], v[0:3]\n"
    "v_exp_f32 v62, v62\n"
    "v_mfma_f32_16x16x32_bf16 v[8:11], a[208:211], a[48:51], v[8:11]\n"
    "v_cvt_pk_bf16_f32 v58, v60, v61\n"
    "v_add_f32 v60, v109, v60\n"
    "v_mfma_f32_16x16x32_bf16 v[8:11], a[212:215], a[52:55], v[8:11]\n"
    "v_exp_f32 v63, v63\n"
    "v_mfma_f32_16x16x32_bf16 v[16:19], a[208:211], a[80:83], v[16:19]\n"
    "v_add_f32 v60, v60, v61\n"
    "v_max3_f32 v109, v36, v44, v52\n"
    "v_mfma_f32_16x16x32_bf16 v[16:19], a[212:215], a[84:87], v[16:19]\n"
    "v_add_f32 v60, v60, v62\n"
    "v_cvt_pk_bf16_f32 v59, v62, v63\n"
    "v_mfma_f32_16x16x32_bf16 v[24:27], a[208:211], a[112:115], v[24:27]\n"
    "v_add_f32 v60, v60, v63\n"
    "v_max_f32 v109, v109, v60\n"
    "v_mfma_f32_16x16x32_bf16 v[24:27], a[212:215], a[116:119], v[24:27]\n"
    "v_cmp_le_f32 vcc, 1.0, v109\n"
    "s_waitcnt lgkmcnt(4)\n"
    "v_mfma_f32_16x16x32_bf16 v[4:7], a[216:219], a[16:19], v[4:7]\n"
    "v_mfma_f32_16x16x32_bf16 v[4:7], a[220:223], a[20:23], v[4:7]\n"
    "v_mfma_f32_16x16x32_bf16 v[12:15], a[216:219], a[48:51], v[12:15]\n"
    "v_mfma_f32_16x16x32_bf16 v[12:15], a[220:223], a[52:55], v[12:15]\n"
    "v_mfma_f32_16x16x32_bf16 v[20:23], a[216:219], a[80:83], v[20:23]\n"
    "s_waitcnt vmcnt(11) lgkmcnt(0)\n"
    "s_barrier\n"
    "v_mfma_f32_16x16x32_bf16 v[20:23], a[220:223], a[84:87], v[20:23]\n"
    "ds_read_b64_tr_b16 a[208:209], v97 offset:0\n"
    "ds_read_b64_tr_b16 a[210:211], v97 offset:8192\n"
    "v_mfma_f32_16x16x32_bf16 v[28:31], a[216:219], a[112:115], v[28:31]\n"
    "s_add_u32 m0, s34, 0\n"
    "ds_read_b64_tr_b16 a[212:213], v98 offset:0\n"
    "v_mfma_f32_16x16x32_bf16 v[28:31], a[220:223], a[116:119], v[28:31]\n"
    "buffer_load_dwordx4 v99, s[16:19], s36 offen offset:0 nt lds\n"
    "v_mfma_f32_16x16x32_bf16 v[0:3], a[240:243], a[24:27], v[0:3]\n"
    "ds_read_b64_tr_b16 a[214:215], v98 offset:8192\n"
    "ds_read_b64_tr_b16 a[216:217], v97 offset:512\n"
    "v_mfma_f32_16x16x32_bf16 v[0:3], a[244:247], a[28:31], v[0:3]\n"
    "ds_read_b64_tr_b16 a[218:219], v97 offset:8704\n"
    "ds_read_b64_tr_b16 a[220:221], v98 offset:512\n"
    "v_mfma_f32_16x16x32_bf16 v[8:11], a[240:243], a[56:59], v[8:11]\n"
    "s_cbranch_vccnz L_ra6\n"
    "L_ra6_ret:\n"
    "v_add_f32 v105, v105, v36\n"
    "v_mfma_f32_16x16x32_bf16 v[8:11], a[244:247], a[60:63], v[8:11]\n"
    "v_add_f32 v106, v106, v44\n"
    "v_add_f32 v107, v107, v52\n"
    "v_mfma_f32_16x16x32_bf16 v[16:19], a[240:243], a[88:91], v[16:19]\n"
    "s_add_u32 m0, s34, 896\n"
    "v_add_f32 v108, v108, v60\n"
    "v_mfma_f32_16x16x32_bf16 v[16:19], a[244:247], a[92:95], v[16:19]\n"
    "buffer_load_dwordx4 v99, s[16:19], s36 offen offset:128 nt lds\n"
    "ds_read_b64_tr_b16 a[222:223], v98 offset:8704\n"
    "v_mfma_f32_16x16x32_bf16 v[24:27], a[240:243], a[120:123], v[24:27]\n"
    "ds_read_b64_tr_b16 a[240:241], v97 offset:1024\n"
    "ds_read_b64_tr_b16 a[242:243], v97 offset:9216\n"
    "v_mfma_f32_16x16x32_bf16 v[24:27], a[244:247], a[124:127], v[24:27]\n"
    "ds_read_b64_tr_b16 a[244:245], v98 offset:1024\n"
    "v_mfma_f32_16x16x32_bf16 v[4:7], a[248:251], a[24:27], v[4:7]\n"
    "ds_read_b64_tr_b16 a[246:247], v98 offset:9216\n"
    "v_add_u32 v96, s35, v96\n"
    "v_mfma_f32_16x16x32_bf16 v[4:7], a[252:255], a[28:31], v[4:7]\n"
    "v_fma_f32 v0, v0, s39, -v101\n"
    "v_fma_f32 v1, v1, s39, -v101\n"
    "v_mfma_f32_16x16x32_bf16 v[12:15], a[248:251], a[56:59], v[12:15]\n"
    "s_add_u32 m0, s34, 1792\n"
    "v_fma_f32 v2, v2, s39, -v101\n"
    "v_mfma_f32_16x16x32_bf16 v[12:15], a[252:255], a[60:63], v[12:15]\n"
    "buffer_load_dwordx4 v99, s[16:19], s36 offen offset:256 nt lds\n"
    "v_fma_f32 v3, v3, s39, -v101\n"
    "v_mfma_f32_16x16x32_bf16 v[20:23], a[248:251], a[88:91], v[20:23]\n"
    "v_exp_f32 v0, v0\n"
    "v_mfma_f32_16x16x32_bf16 v[20:23], a[252:255], a[92:95], v[20:23]\n"
    "v_exp_f32 v1, v1\n"
    "v_mfma_f32_16x16x32_bf16 v[28:31], a[248:251], a[120:123], v[28:31]\n"
    "ds_read_b64_tr_b16 a[248:249], v97 offset:1536\n"
    "ds_read_b64_tr_b16 a[250:251], v97 offset:9728\n"
    "v_mfma_f32_16x16x32_bf16 v[28:31], a[252:255], a[124:127], v[28:31]\n"
    "ds_read_b64_tr_b16 a[252:253], v98 offset:1536\n"
    "s_waitcnt lgkmcnt(11)\n"
    "v_mfma_f32_16x16x32_bf16 v[128:131], a[208:211], v[32:35], v[128:131]\n"
    "ds_read_b64_tr_b16 a[254:255], v98 offset:9728\n"
    "v_add_f32 v109, v0, v1\n"
    "v_mfma_f32_16x16x32_bf16 v[132:135], a[208:211], v[40:43], v[132:135]\n"
    "s_add_u32 m0, s34, 2688\n"
    "v_cvt_pk_bf16_f32 v0, v0, v1\n"
    "v_mfma_f32_16x16x32_bf16 v[136:139], a[208:211], v[48:51], v[136:139]\n"
    "buffer_load_dwordx4 v99, s[16:19], s36 offen offset:384 nt lds\n"
    "v_fma_f32 v4, v4, s39, -v101\n"
    "v_mfma_f32_16x16x32_bf16 v[140:143], a[208:211], v[56:59], v[140:143]\n"
    "ds_read_b64_tr_b16 a[208:209], v97 offset:2048\n"
    "ds_read_b64_tr_b16 a[210:211], v97 offset:10240\n"
    "v_mfma_f32_16x16x32_bf16 v[156:159], a[212:215], v[56:59], v[156:159]\n"
    "v_exp_f32 v2, v2\n"
    "v_mfma_f32_16x16x32_bf16 v[152:155], a[212:215], v[48:51], v[152:155]\n"
    "v_exp_f32 v3, v3\n"
    "v_mfma_f32_16x16x32_bf16 v[148:151], a[212:215], v[40:43], v[148:151]\n"
    "v_add_f32 v109, v109, v2\n"
    "v_add_f32 v109, v109, v3\n"
    "v_mfma_f32_16x16x32_bf16 v[144:147], a[212:215], v[32:35], v[144:147]\n"
    "ds_read_b64_tr_b16 a[212:213], v98 offset:2048\n"
    "s_waitcnt lgkmcnt(11)\n"
    "v_mfma_f32_16x16x32_bf16 v[160:163], a[216:219], v[32:35], v[160:163]\n"
    "ds_read_b64_tr_b16 a[214:215], v98 offset:10240\n"
    "v_cvt_pk_bf16_f32 v1, v2, v3\n"
    "v_mfma_f32_16x16x32_bf16 v[164:167], a[216:219], v[40:43], v[164:167]\n"
    "s_add_u32 m0, s34, 49152\n"
    "v_fma_f32 v5, v5, s39, -v101\n"
    "v_mfma_f32_16x16x32_bf16 v[168:171], a[216:219], v[48:51], v[168:171]\n"
    "buffer_load_dwordx4 v100, s[20:23], s36 offen offset:0 nt lds\n"
    "v_fma_f32 v6, v6, s39, -v101\n"
    "v_mfma_f32_16x16x32_bf16 v[172:175], a[216:219], v[56:59], v[172:175]\n"
    "ds_read_b64_tr_b16 a[216:217], v97 offset:2560\n"
    "ds_read_b64_tr_b16 a[218:219], v97 offset:10752\n"
    "v_mfma_f32_16x16x32_bf16 v[188:191], a[220:223], v[56:59], v[188:191]\n"
    "v_fma_f32 v7, v7, s39, -v101\n"
    "v_fma_f32 v8, v8, s39, -v102\n"
    "v_mfma_f32_16x16x32_bf16 v[184:187], a[220:223], v[48:51], v[184:187]\n"
    "v_exp_f32 v4, v4\n"
    "v_mfma_f32_16x16x32_bf16 v[180:183], a[220:223], v[40:43], v[180:183]\n"
    "v_exp_f32 v5, v5\n"
    "v_mfma_f32_16x16x32_bf16 v[176:179], a[220:223], v[32:35], v[176:179]\n"
    "ds_read_b64_tr_b16 a[220:221], v98 offset:2560\n"
    "s_waitcnt lgkmcnt(11)\n"
    "v_mfma_f32_16x16x32_bf16 v[192:195], a[240:243], v[32:35], v[192:195]\n"
    "ds_read_b64_tr_b16 a[222:223], v98 offset:10752\n"
    "v_cvt_pk_bf16_f32 v2, v4, v5\n"
    "v_mfma_f32_16x16x32_bf16 v[196:199], a[240:243], v[40:43], v[196:199]\n"
    "s_add_u32 m0, s34, 50048\n"
    "v_add_f32 v4, v109, v4\n"
    "v_mfma_f32_16x16x32_bf16 v[200:203], a[240:243], v[48:51], v[200:203]\n"
    "buffer_load_dwordx4 v100, s[20:23], s36 offen offset:128 nt lds\n"
    "v_add_f32 v4, v4, v5\n"
    "v_mfma_f32_16x16x32_bf16 v[204:207], a[240:243], v[56:59], v[204:207]\n"
    "ds_read_b64_tr_b16 a[240:241], v97 offset:3072\n"
    "ds_read_b64_tr_b16 a[242:243], v97 offset:11264\n"
    "v_mfma_f32_16x16x32_bf16 v[220:223], a[244:247], v[56:59], v[220:223]\n"
    "v_exp_f32 v6, v6\n"
    "v_mfma_f32_16x16x32_bf16 v[216:219], a[244:247], v[48:51], v[216:219]\n"
    "v_exp_f32 v7, v7\n"
    "v_mfma_f32_16x16x32_bf16 v[212:215], a[244:247], v[40:43], v[212:215]\n"
    "v_add_f32 v4, v4, v6\n"
    "v_cvt_pk_bf16_f32 v3, v6, v7\n"
    "v_mfma_f32_16x16x32_bf16 v[208:211], a[244:247], v[32:35], v[208:211]\n"
    "ds_read_b64_tr_b16 a[244:245], v98 offset:3072\n"
    "s_waitcnt lgkmcnt(11)\n"
    "v_mfma_f32_16x16x32_bf16 v[224:227], a[248:251], v[32:35], v[224:227]\n"
    "ds_read_b64_tr_b16 a[246:247], v98 offset:11264\n"
    "v_add_f32 v4, v4, v7\n"
    "v_mfma_f32_16x16x32_bf16 v[228:231], a[248:251], v[40:43], v[228:231]\n"
    "s_add_u32 m0, s34, 50944\n"
    "v_fma_f32 v9, v9, s39, -v102\n"
    "v_mfma_f32_16x16x32_bf16 v[232:235], a[248:251], v[48:51], v[232:235]\n"
    "buffer_load_dwordx4 v100, s[20:23], s36 offen offset:256 nt lds\n"
    "v_fma_f32 v10, v10, s39, -v102\n"
    "v_mfma_f32_16x16x32_bf16 v[236:239], a[248:251], v[56:59], v[236:239]\n"
    "ds_read_b64_tr_b16 a[248:249], v97 offset:3584\n"
    "ds_read_b64_tr_b16 a[250:251], v97 offset:11776\n"
    "v_mfma_f32_16x16x32_bf16 v[252:255], a[252:255], v[56:59], v[252:255]\n"
    "v_fma_f32 v11, v11, s39, -v102\n"
    "v_fma_f32 v12, v12, s39, -v102\n"
    "v_mfma_f32_16x16x32_bf16 v[248:251], a[252:255], v[48:51], v[248:251]\n"
    "v_exp_f32 v8, v8\n"
    "v_mfma_f32_16x16x32_bf16 v[244:247], a[252:255], v[40:43], v[244:247]\n"
    "v_exp_f32 v9, v9\n"
    "v_mfma_f32_16x16x32_bf16 v[240:243], a[252:255], v[32:35], v[240:243]\n"
    "ds_read_b64_tr_b16 a[252:253], v98 offset:3584\n"
    "s_waitcnt lgkmcnt(11)\n"
    "v_mfma_f32_16x16x32_bf16 a[128:131], a[208:211], v[32:35], a[128:131]\n"
    "ds_read_b64_tr_b16 a[254:255], v98 offset:11776\n"
    "v_add_f32 v109, v8, v9\n"
    "v_mfma_f32_16x16x32_bf16 a[132:135], a[208:211], v[40:43], a[132:135]\n"
    "s_add_u32 m0, s34, 51840\n"
    "v_cvt_pk_bf16_f32 v8, v8, v9\n"
    "v_mfma_f32_16x16x32_bf16 a[136:139], a[208:211], v[48:51], a[136:139]\n"
    "buffer_load_dwordx4 v100, s[20:23], s36 offen offset:384 nt lds\n"
    "v_fma_f32 v13, v13, s39, -v102\n"
    "v_mfma_f32_16x16x32_bf16 a[140:143], a[208:211], v[56:59], a[140:143]\n"
    "s_add_u32 s34, s34, s35\n"
    "s_add_u32 s56, s34, 16384\n"
    "v_mfma_f32_16x16x32_bf16 a[156:159], a[212:215], v[56:59], a[156:159]\n"
    "s_cmp_eq_u32 s56, s51\n"
    "s_cselect_b32 s35, -32768, s57\n"
    "v_mfma_f32_16x16x32_bf16 a[152:155], a[212:215], v[48:51], a[152:155]\n"
    "s_add_u32 s36, s36, 16384\n"
    "ds_read_b128 a[208:211], v96 offset:0\n"
    "v_mfma_f32_16x16x32_bf16 a[148:151], a[212:215], v[40:43], a[148:151]\n"
    "v_exp_f32 v10, v10\n"
    "v_mfma_f32_16x16x32_bf16 a[144:147], a[212:215], v[32:35], a[144:147]\n"
    "ds_read_b128 a[212:215], v96 offset:512\n"
    "s_waitcnt lgkmcnt(10)\n"
    "v_mfma_f32_16x16x32_bf16 a[160:163], a[216:219], v[32:35], a[160:163]\n"
    "v_exp_f32 v11, v11\n"
    "v_mfma_f32_16x16x32_bf16 a[164:167], a[216:219], v[40:43], a[164:167]\n"
    "v_add_f32 v109, v109, v10\n"
    "v_add_f32 v109, v109, v11\n"
    "v_mfma_f32_16x16x32_bf16 a[168:171], a[216:219], v[48:51], a[168:171]\n"
    "v_cvt_pk_bf16_f32 v9, v10, v11\n"
    "v_fma_f32 v14, v14, s39, -v102\n"
    "v_mfma_f32_16x16x32_bf16 a[172:175], a[216:219], v[56:59], a[172:175]\n"
    "ds_read_b128 a[216:219], v96 offset:8192\n"
    "v_fma_f32 v15, v15, s39, -v102\n"
    "v_mfma_f32_16x16x32_bf16 a[188:191], a[220:223], v[56:59], a[188:191]\n"
    "ds_read_b128 v[118:121], v113 offset:8192\n"
    "v_fma_f32 v16, v16, s39, -v103\n"
    "v_mfma_f32_16x16x32_bf16 a[184:187], a[220:223], v[48:51], a[184:187]\n"
    "v_exp_f32 v12, v12\n"
    "v_mfma_f32_16x16x32_bf16 a[180:183], a[220:223], v[40:43], a[180:183]\n"
    "v_exp_f32 v13, v13\n"
    "v_mfma_f32_16x16x32_bf16 a[176:179], a[220:223], v[32:35], a[176:179]\n"
    "ds_read_b128 a[220:223], v96 offset:8704\n"
    "s_waitcnt lgkmcnt(9)\n"
    "v_mfma_f32_16x16x32_bf16 a[192:195], a[240:243], v[32:35], a[192:195]\n"
    "v_exp_f32 v14, v14\n"
    "v_mfma_f32_16x16x32_bf16 a[196:199], a[240:243], v[40:43], a[196:199]\n"
    "v_cvt_pk_bf16_f32 v10, v12, v13\n"
    "v_add_f32 v12, v109, v12\n"
    "v_mfma_f32_16x16x32_bf16 a[200:203], a[240:243], v[48:51], a[200:203]\n"
    "s_waitcnt lgkmcnt(1)\n"
    "buffer_store_dwordx4 v[118:121], v110, s[24:27], s85 offen offset:384 sc1\n"
    "v_mfma_f32_16x16x32_bf16 a[204:207], a[240:243], v[56:59], a[204:207]\n"
    "v_exp_f32 v15, v15\n"
    "v_mfma_f32_16x16x32_bf16 v[76:79], a[244:247], v[56:59], v[76:79]\n"
    "v_add_f32 v12, v12, v13\n"
    "v_add_f32 v12, v12, v14\n"
    "v_mfma_f32_16x16x32_bf16 v[72:75], a[244:247], v[48:51], v[72:75]\n"
    "v_cvt_pk_bf16_f32 v11, v14, v15\n"
    "v_add_f32 v12, v12, v15\n"
    "v_mfma_f32_16x16x32_bf16 v[68:71], a[244:247], v[40:43], v[68:71]\n"
    "v_fma_f32 v17, v17, s39, -v103\n"
    "v_fma_f32 v18, v18, s39, -v103\n"
    "v_mfma_f32_16x16x32_bf16 v[64:67], a[244:247], v[32:35], v[64:67]\n"
    "v_fma_f32 v19, v19, s39, -v103\n"
    "v_mfma_f32_16x16x32_bf16 a[224:227], a[248:251], v[32:35], a[224:227]\n"
    "v_exp_f32 v16, v16\n"
    "v_mfma_f32_16x16x32_bf16 a[228:231], a[248:251], v[40:43], a[228:231]\n"
    "v_exp_f32 v17, v17\n"
    "v_mfma_f32_16x16x32_bf16 a[232:235], a[248:251], v[48:51], a[232:235]\n"
    "v_exp_f32 v18, v18\n"
    "v_mfma_f32_16x16x32_bf16 a[236:239], a[248:251], v[56:59], a[236:239]\n"
    "v_exp_f32 v19, v19\n"
    "v_mfma_f32_16x16x32_bf16 v[92:95], a[252:255], v[56:59], v[92:95]\n"
    "v_add_f32 v109, v16, v17\n"
    "v_cvt_pk_bf16_f32 v16, v16, v17\n"
    "v_mfma_f32_16x16x32_bf16 v[88:91], a[252:255], v[48:51], v[88:91]\n"
    "v_add_f32 v109, v109, v18\n"
    "v_add_f32 v109, v109, v19\n"
    "v_mfma_f32_16x16x32_bf16 v[84:87], a[252:255], v[40:43], v[84:87]\n"
    "s_add_u32 s69, s69, 4096\n"
    "s_and_b32 s69, s69, 0xffff\n"
    "s_xor_b32 s85, s69, s87\n"
    "v_mfma_f32_16x16x32_bf16 v[80:83], a[252:255], v[32:35], v[80:83]\n"
    "v_mfma_f32_16x16x32_bf16 v[32:35], a[208:211], a[0:3], 0\n"
    "ds_read_b128 a[240:243], v96 offset:1024\n"
    "ds_read_b128 a[244:247], v96 offset:1536\n"
    "v_mfma_f32_16x16x32_bf16 v[32:35], a[212:215], a[4:7], v[32:35]\n"
    "ds_read_b128 a[248:251], v96 offset:9216\n"
    "ds_read_b128 a[252:255], v96 offset:9728\n"
    "v_mfma_f32_16x16x32_bf16 v[40:43], a[208:211], a[32:35], 0\n"
    "v_add_u32 v97, s35, v97\n"
    "v_add_u32 v98, s35, v98\n"
    "v_mfma_f32_16x16x32_bf16 v[40:43], a[212:215], a[36:39], v[40:43]\n"
    "v_cvt_pk_bf16_f32 v17, v18, v19\n"
    "v_fma_f32 v20, v20, s39, -v103\n"
    "v_mfma_f32_16x16x32_bf16 v[48:51], a[208:211], a[64:67], 0\n"
    "v_fma_f32 v21, v21, s39, -v103\n"
    "v_fma_f32 v22, v22, s39, -v103\n"
    "v_mfma_f32_16x16x32_bf16 v[48:51], a[212:215], a[68:71], v[48:51]\n"
    "v_fma_f32 v23, v23, s39, -v103\n"
    "v_fma_f32 v24, v24, s39, -v104\n"
    "v_mfma_f32_16x16x32_bf16 v[56:59], a[208:211], a[96:99], 0\n"
    "ds_read_b128 a[208:211], v96 offset:2048\n"
    "v_fma_f32 v25, v25, s39, -v104\n"
    "v_mfma_f32_16x16x32_bf16 v[56:59], a[212:215], a[100:103], v[56:59]\n"
    "ds_read_b128 a[212:215], v96 offset:2560\n"
    "s_waitcnt lgkmcnt(6)\n"
    "v_mfma_f32_16x16x32_bf16 v[36:39], a[216:219], a[0:3], 0\n"
    "v_exp_f32 v20, v20\n"
    "v_mfma_f32_16x16x32_bf16 v[36:39], a[220:223], a[4:7], v[36:39]\n"
    "v_exp_f32 v21, v21\n"
    "v_mfma_f32_16x16x32_bf16 v[44:47], a[216:219], a[32:35], 0\n"
    "ds_read_b128 v[114:117], v113 offset:256\n"
    "v_cvt_pk_bf16_f32 v18, v20, v21\n"
    "v_mfma_f32_16x16x32_bf16 v[44:47], a[220:223], a[36:39], v[44:47]\n"
    "v_exp_f32 v22, v22\n"
    "v_mfma_f32_16x16x32_bf16 v[52:55], a[216:219], a[64:67], 0\n"
    "v_add_f32 v20, v109, v20\n"
    "v_add_f32 v20, v20, v21\n"
    "v_mfma_f32_16x16x32_bf16 v[52:55], a[220:223], a[68:71], v[52:55]\n"
    "v_exp_f32 v23, v23\n"
    "v_mfma_f32_16x16x32_bf16 v[60:63], a[216:219], a[96:99], 0\n"
    "ds_read_b128 a[216:219], v96 offset:10240\n"
    "v_add_f32 v20, v20, v22\n"
    "v_mfma_f32_16x16x32_bf16 v[60:63], a[220:223], a[100:103], v[60:63]\n"
    "ds_read_b128 a[220:223], v96 offset:10752\n"
    "s_waitcnt lgkmcnt(7)\n"
    "v_mfma_f32_16x16x32_bf16 v[32:35], a[240:243], a[8:11], v[32:35]\n"
    "s_waitcnt lgkmcnt(2)\n"
    "buffer_store_dwordx4 v[114:117], v110, s[24:27], s85 offen offset:256 sc1\n"
    "v_mfma_f32_16x16x32_bf16 v[32:35], a[244:247], a[12:15], v[32:35]\n"
    "v_cvt_pk_bf16_f32 v19, v22, v23\n"
    "v_add_f32 v20, v20, v23\n"
    "v_mfma_f32_16x16x32_bf16 v[40:43], a[240:243], a[40:43], v[40:43]\n"
    "v_fma_f32 v26, v26, s39, -v104\n"
    "v_fma_f32 v27, v27, s39, -v104\n"
    "v_mfma_f32_16x16x32_bf16 v[40:43], a[244:247], a[44:47], v[40:43]\n"
    "v_exp_f32 v24, v24\n"
    "v_mfma_f32_16x16x32_bf16 v[48:51], a[240:243], a[72:75], v[48:51]\n"
    "v_exp_f32 v25, v25\n"
    "v_mfma_f32_16x16x32_bf16 v[48:51], a[244:247], a[76:79], v[48:51]\n"
    "v_exp_f32 v26, v26\n"
    "v_mfma_f32_16x16x32_bf16 v[56:59], a[240:243], a[104:107], v[56:59]\n"
    "ds_read_b128 a[240:243], v96 offset:3072\n"
    "v_add_f32 v109, v24, v25\n"
    "v_mfma_f32_16x16x32_bf16 v[56:59], a[244:247], a[108:111], v[56:59]\n"
    "ds_read_b128 a[244:247], v96 offset:3584\n"
    "v_mfma_f32_16x16x32_bf16 v[36:39], a[248:251], a[8:11], v[36:39]\n"
    "v_exp_f32 v27, v27\n"
    "v_mfma_f32_16x16x32_bf16 v[36:39], a[252:255], a[12:15], v[36:39]\n"
    "v_cvt_pk_bf16_f32 v24, v24, v25\n"
    "v_add_f32 v109, v109, v26\n"
    "v_mfma_f32_16x16x32_bf16 v[44:47], a[248:251], a[40:43], v[44:47]\n"
    "v_add_f32 v109, v109, v27\n"
    "v_cvt_pk_bf16_f32 v25, v26, v27\n"
    "v_mfma_f32_16x16x32_bf16 v[44:47], a[252:255], a[44:47], v[44:47]\n"
    "v_fma_f32 v28, v28, s39, -v104\n"
    "v_fma_f32 v29, v29, s39, -v104\n"
    "v_mfma_f32_16x16x32_bf16 v[52:55], a[248:251], a[72:75], v[52:55]\n"
    "v_fma_f32 v30, v30, s39, -v104\n"
    "v_fma_f32 v31, v31, s39, -v104\n"
    "v_mfma_f32_16x16x32_bf16 v[52:55], a[252:255], a[76:79], v[52:55]\n"
    "v_exp_f32 v28, v28\n"
    "v_mfma_f32_16x16x32_bf16 v[60:63], a[248:251], a[104:107], v[60:63]\n"
    "ds_read_b128 a[248:251], v96 offset:11264\n"
    "v_mfma_f32_16x16x32_bf16 v[60:63], a[252:255], a[108:111], v[60:63]\n"
    "ds_read_b128 a[252:255], v96 offset:11776\n"
    "v_mfma_f32_16x16x32_bf16 v[32:35], a[208:211], a[16:19], v[32:35]\n"
    "v_exp_f32 v29, v29\n"
    "v_mfma_f32_16x16x32_bf16 v[32:35], a[212:215], a[20:23], v[32:35]\n"
    "v_exp_f32 v30, v30\n"
    "v_mfma_f32_16x16x32_bf16 v[40:43], a[208:211], a[48:51], v[40:43]\n"
    "v_cvt_pk_bf16_f32 v26, v28, v29\n"
    "v_add_f32 v28, v109, v28\n"
    "v_mfma_f32_16x16x32_bf16 v[40:43], a[212:215], a[52:55], v[40:43]\n"
    "v_exp_f32 v31, v31\n"
    "v_mfma_f32_16x16x32_bf16 v[48:51], a[208:211], a[80:83], v[48:51]\n"
    "v_add_f32 v28, v28, v29\n"
    "v_max3_f32 v109, v4, v12, v20\n"
    "v_mfma_f32_16x16x32_bf16 v[48:51], a[212:215], a[84:87], v[48:51]\n"
    "v_add_f32 v28, v28, v30\n"
    "v_cvt_pk_bf16_f32 v27, v30, v31\n"
    "v_mfma_f32_16x16x32_bf16 v[56:59], a[208:211], a[112:115], v[56:59]\n"
    "v_add_f32 v28, v28, v31\n"
    "v_max_f32 v109, v109, v28\n"
    "v_mfma_f32_16x16x32_bf16 v[56:59], a[212:215], a[116:119], v[56:59]\n"
    "v_cmp_le_f32 vcc, 1.0, v109\n"
    "s_waitcnt lgkmcnt(4)\n"
    "v_mfma_f32_16x16x32_bf16 v[36:39], a[216:219], a[16:19], v[36:39]\n"
    "v_mfma_f32_16x16x32_bf16 v[36:39], a[220:223], a[20:23], v[36:39]\n"
    "v_mfma_f32_16x16x32_bf16 v[44:47], a[216:219], a[48:51], v[44:47]\n"
    "v_mfma_f32_16x16x32_bf16 v[44:47], a[220:223], a[52:55], v[44:47]\n"
    "v_mfma_f32_16x16x32_bf16 v[52:55], a[216:219], a[80:83], v[52:55]\n"
    "s_waitcnt vmcnt(11) lgkmcnt(0)\n"
    "s_barrier\n"
    "v_mfma_f32_16x16x32_bf16 v[52:55], a[220:223], a[84:87], v[52:55]\n"
    "ds_read_b64_tr_b16 a[208:209], v97 offset:0\n"
    "ds_read_b64_tr_b16 a[210:211], v97 offset:8192\n"
    "v_mfma_f32_16x16x32_bf16 v[60:63], a[216:219], a[112:115], v[60:63]\n"
    "s_add_u32 m0, s34, 0\n"
    "ds_read_b64_tr_b16 a[212:213], v98 offset:0\n"
    "v_mfma_f32_16x16x32_bf16 v[60:63], a[220:223], a[116:119], v[60:63]\n"
    "buffer_load_dwordx4 v99, s[16:19], s36 offen offset:0 nt lds\n"
    "v_mfma_f32_16x16x32_bf16 v[32:35], a[240:243], a[24:27], v[32:35]\n"
    "ds_read_b64_tr_b16 a[214:215], v98 offset:8192\n"
    "ds_read_b64_tr_b16 a[216:217], v97 offset:512\n"
    "v_mfma_f32_16x16x32_bf16 v[32:35], a[244:247], a[28:31], v[32:35]\n"
    "ds_read_b64_tr_b16 a[218:219], v97 offset:8704\n"
    "ds_read_b64_tr_b16 a[220:221], v98 offset:512\n"
    "v_mfma_f32_16x16x32_bf16 v[40:43], a[240:243], a[56:59], v[40:43]\n"
    "s_cbranch_vccnz L_ra7\n"
    "L_ra7_ret:\n"
    "v_add_f32 v105, v105, v4\n"
    "v_mfma_f32_16x16x32_bf16 v[40:43], a[244:247], a[60:63], v[40:43]\n"
    "v_add_f32 v106, v106, v12\n"
    "v_add_f32 v107, v107, v20\n"
    "v_mfma_f32_16x16x32_bf16 v[48:51], a[240:243], a[88:91], v[48:51]\n"
    "s_add_u32 m0, s34, 896\n"
    "v_add_f32 v108, v108, v28\n"
    "v_mfma_f32_16x16x32_bf16 v[48:51], a[244:247], a[92:95], v[48:51]\n"
    "buffer_load_dwordx4 v99, s[16:19], s36 offen offset:128 nt lds\n"
    "ds_read_b64_tr_b16 a[222:223], v98 offset:8704\n"
    "v_mfma_f32_16x16x32_bf16 v[56:59], a[240:243], a[120:123], v[56:59]\n"
    "ds_read_b64_tr_b16 a[240:241], v97 offset:1024\n"
    "ds_read_b64_tr_b16 a[242:243], v97 offset:9216\n"
    "v_mfma_f32_16x16x32_bf16 v[56:59], a[244:247], a[124:127], v[56:59]\n"
    "ds_read_b64_tr_b16 a[244:245], v98 offset:1024\n"
    "v_mfma_f32_16x16x32_bf16 v[36:39], a[248:251], a[24:27], v[36:39]\n"
    "ds_read_b64_tr_b16 a[246:247], v98 offset:9216\n"
    "v_add_u32 v96, s35, v96\n"
    "v_mfma_f32_16x16x32_bf16 v[36:39], a[252:255], a[28:31], v[36:39]\n"
    "v_fma_f32 v32, v32, s39, -v101\n"
    "v_fma_f32 v33, v33, s39, -v101\n"
    "v_mfma_f32_16x16x32_bf16 v[44:47], a[248:251], a[56:59], v[44:47]\n"
    "s_add_u32 m0, s34, 1792\n"
    "v_fma_f32 v34, v34, s39, -v101\n"
    "v_mfma_f32_16x16x32_bf16 v[44:47], a[252:255], a[60:63], v[44:47]\n"
    "buffer_load_dwordx4 v99, s[16:19], s36 offen offset:256 nt lds\n"
    "v_fma_f32 v35, v35, s39, -v101\n"
    "v_mfma_f32_16x16x32_bf16 v[52:55], a[248:251], a[88:91], v[52:55]\n"
    "v_exp_f32 v32, v32\n"
    "v_mfma_f32_16x16x32_bf16 v[52:55], a[252:255], a[92:95], v[52:55]\n"
    "v_exp_f32 v33, v33\n"
    "v_mfma_f32_16x16x32_bf16 v[60:63], a[248:251], a[120:123], v[60:63]\n"
    "ds_read_b64_tr_b16 a[248:249], v97 offset:1536\n"
    "ds_read_b64_tr_b16 a[250:251], v97 offset:9728\n"
    "v_mfma_f32_16x16x32_bf16 v[60:63], a[252:255], a[124:127], v[60:63]\n"
    "ds_read_b64_tr_b16 a[252:253], v98 offset:1536\n"
    "s_waitcnt lgkmcnt(11)\n"
    "v_mfma_f32_16x16x32_bf16 v[128:131], a[208:211], v[0:3], v[128:131]\n"
    "ds_read_b64_tr_b16 a[254:255], v98 offset:9728\n"
    "v_add_f32 v109, v32, v33\n"
    "v_mfma_f32_16x16x32_bf16 v[132:135], a[208:211], v[8:11], v[132:135]\n"
    "s_add_u32 m0, s34, 2688\n"
    "v_cvt_pk_bf16_f32 v32, v32, v33\n"
    "v_mfma_f32_16x16x32_bf16 v[136:139], a[208:211], v[16:19], v[136:139]\n"
    "buffer_load_dwordx4 v99, s[16:19], s36 offen offset:384 nt lds\n"
    "v_fma_f32 v36, v36, s39, -v101\n"
    "v_mfma_f32_16x16x32_bf16 v[140:143], a[208:211], v[24:27], v[140:143]\n"
    "ds_read_b64_tr_b16 a[208:209], v97 offset:2048\n"
    "ds_read_b64_tr_b16 a[210:211], v97 offset:10240\n"
    "v_mfma_f32_16x16x32_bf16 v[156:159], a[212:215], v[24:27], v[156:159]\n"
    "v_exp_f32 v34, v34\n"
    "v_mfma_f32_16x16x32_bf16 v[152:155], a[212:215], v[16:19], v[152:155]\n"
    "v_exp_f32 v35, v35\n"
    "v_mfma_f32_16x16x32_bf16 v[148:151], a[212:215], v[8:11], v[148:151]\n"
    "v_add_f32 v109, v109, v34\n"
    "v_add_f32 v109, v109, v35\n"
    "v_mfma_f32_16x16x32_bf16 v[144:147], a[212:215], v[0:3], v[144:147]\n"
    "ds_read_b64_tr_b16 a[212:213], v98 offset:2048\n"
    "s_waitcnt lgkmcnt(11)\n"
    "v_mfma_f32_16x16x32_bf16 v[160:163], a[216:219], v[0:3], v[160:163]\n"
    "ds_read_b64_tr_b16 a[214:215], v98 offset:10240\n"
    "v_cvt_pk_bf16_f32 v33, v34, v35\n"
    "v_mfma_f32_16x16x32_bf16 v[164:167], a[216:219], v[8:11], v[164:167]\n"
    "s_add_u32 m0, s34, 49152\n"
    "v_fma_f32 v37, v37, s39, -v101\n"
    "v_mfma_f32_16x16x32_bf16 v[168:171], a[216:219], v[16:19], v[168:171]\n"
    "buffer_load_dwordx4 v100, s[20:23], s36 offen offset:0 nt lds\n"
    "v_fma_f32 v38, v38, s39, -v101\n"
    "v_mfma_f32_16x16x32_bf16 v[172:175], a[216:219], v[24:27], v[172:175]\n"
    "ds_read_b64_tr_b16 a[216:217], v97 offset:2560\n"
    "ds_read_b64_tr_b16 a[218:219], v97 offset:10752\n"
    "v_mfma_f32_16x16x32_bf16 v[188:191], a[220:223], v[24:27], v[188:191]\n"
    "v_fma_f32 v39, v39, s39, -v101\n"
    "v_fma_f32 v40, v40, s39, -v102\n"
    "v_mfma_f32_16x16x32_bf16 v[184:187], a[220:223], v[16:19], v[184:187]\n"
    "v_exp_f32 v36, v36\n"
    "v_mfma_f32_16x16x32_bf16 v[180:183], a[220:223], v[8:11], v[180:183]\n"
    "v_exp_f32 v37, v37\n"
    "v_mfma_f32_16x16x32_bf16 v[176:179], a[220:223], v[0:3], v[176:179]\n"
    "ds_read_b64_tr_b16 a[220:221], v98 offset:2560\n"
    "s_waitcnt lgkmcnt(11)\n"
    "v_mfma_f32_16x16x32_bf16 v[192:195], a[240:243], v[0:3], v[192:195]\n"
    "ds_read_b64_tr_b16 a[222:223], v98 offset:10752\n"
    "v_cvt_pk_bf16_f32 v34, v36, v37\n"
    "v_mfma_f32_16x16x32_bf16 v[196:199], a[240:243], v[8:11], v[196:199]\n"
    "s_add_u32 m0, s34, 50048\n"
    "v_add_f32 v36, v109, v36\n"
    "v_mfma_f32_16x16x32_bf16 v[200:203], a[240:243], v[16:19], v[200:203]\n"
    "buffer_load_dwordx4 v100, s[20:23], s36 offen offset:128 nt lds\n"
    "v_add_f32 v36, v36, v37\n"
    "v_mfma_f32_16x16x32_bf16 v[204:207], a[240:243], v[24:27], v[204:207]\n"
    "ds_read_b64_tr_b16 a[240:241], v97 offset:3072\n"
    "ds_read_b64_tr_b16 a[242:243], v97 offset:11264\n"
    "v_mfma_f32_16x16x32_bf16 v[220:223], a[244:247], v[24:27], v[220:223]\n"
    "v_exp_f32 v38, v38\n"
    "v_mfma_f32_16x16x32_bf16 v[216:219], a[244:247], v[16:19], v[216:219]\n"
    "v_exp_f32 v39, v39\n"
    "v_mfma_f32_16x16x32_bf16 v[212:215], a[244:247], v[8:11], v[212:215]\n"
    "v_add_f32 v36, v36, v38\n"
    "v_cvt_pk_bf16_f32 v35, v38, v39\n"
    "v_mfma_f32_16x16x32_bf16 v[208:211], a[244:247], v[0:3], v[208:211]\n"
    "ds_read_b64_tr_b16 a[244:245], v98 offset:3072\n"
    "s_waitcnt lgkmcnt(11)\n"
    "v_mfma_f32_16x16x32_bf16 v[224:227], a[248:251], v[0:3], v[224:227]\n"
    "ds_read_b64_tr_b16 a[246:247], v98 offset:11264\n"
    "v_add_f32 v36, v36, v39\n"
    "v_mfma_f32_16x16x32_bf16 v[228:231], a[248:251], v[8:11], v[228:231]\n"
    "s_add_u32 m0, s34, 50944\n"
    "v_fma_f32 v41, v41, s39, -v102\n"
    "v_mfma_f32_16x16x32_bf16 v[232:235], a[248:251], v[16:19], v[232:235]\n"
    "buffer_load_dwordx4 v100, s[20:23], s36 offen offset:256 nt lds\n"
    "v_fma_f32 v42, v42, s39, -v102\n"
    "v_mfma_f32_16x16x32_bf16 v[236:239], a[248:251], v[24:27], v[236:239]\n"
    "ds_read_b64_tr_b16 a[248:249], v97 offset:3584\n"
    "ds_read_b64_tr_b16 a[250:251], v97 offset:11776\n"
    "v_mfma_f32_16x16x32_bf16 v[252:255], a[252:255], v[24:27], v[252:255]\n"
    "v_fma_f32 v43, v43, s39, -v102\n"
    "v_fma_f32 v44, v44, s39, -v102\n"
    "v_mfma_f32_16x16x32_bf16 v[248:251], a[252:255], v[16:19], v[248:251]\n"
    "v_exp_f32 v40, v40\n"
    "v_mfma_f32_16x16x32_bf16 v[244:247], a[252:255], v[8:11], v[244:247]\n"
    "v_exp_f32 v41, v41\n"
    "v_mfma_f32_16x16x32_bf16 v[240:243], a[252:255], v[0:3], v[240:243]\n"
    "ds_read_b64_tr_b16 a[252:253], v98 offset:3584\n"
    "s_waitcnt lgkmcnt(11)\n"
    "v_mfma_f32_16x16x32_bf16 a[128:131], a[208:211], v[0:3], a[128:131]\n"
    "ds_read_b64_tr_b16 a[254:255], v98 offset:11776\n"
    "v_add_f32 v109, v40, v41\n"
    "v_mfma_f32_16x16x32_bf16 a[132:135], a[208:211], v[8:11], a[132:135]\n"
    "s_add_u32 m0, s34, 51840\n"
    "v_cvt_pk_bf16_f32 v40, v40, v41\n"
    "v_mfma_f32_16x16x32_bf16 a[136:139], a[208:211], v[16:19], a[136:139]\n"
    "buffer_load_dwordx4 v100, s[20:23], s36 offen offset:384 nt lds\n"
    "v_fma_f32 v45, v45, s39, -v102\n"
    "v_mfma_f32_16x16x32_bf16 a[140:143], a[208:211], v[24:27], a[140:143]\n"
    "s_add_u32 s34, s34, s35\n"
    "s_add_u32 s56, s34, 16384\n"
    "v_mfma_f32_16x16x32_bf16 a[156:159], a[212:215], v[24:27], a[156:159]\n"
    "s_cmp_eq_u32 s56, s51\n"
    "s_cselect_b32 s35, -32768, s57\n"
    "v_mfma_f32_16x16x32_bf16 a[152:155], a[212:215], v[16:19], a[152:155]\n"
    "s_add_u32 s36, s36, 16384\n"
    "ds_read_b128 a[208:211], v96 offset:0\n"
    "v_mfma_f32_16x16x32_bf16 a[148:151], a[212:215], v[8:11], a[148:151]\n"
    "v_exp_f32 v42, v42\n"
    "v_mfma_f32_16x16x32_bf16 a[144:147], a[212:215], v[0:3], a[144:147]\n"
    "ds_read_b128 a[212:215], v96 offset:512\n"
    "s_waitcnt lgkmcnt(10)\n"
    "v_mfma_f32_16x16x32_bf16 a[160:163], a[216:219], v[0:3], a[160:163]\n"
    "v_exp_f32 v43, v43\n"
    "v_mfma_f32_16x16x32_bf16 a[164:167], a[216:219], v[8:11], a[164:167]\n"
    "v_add_f32 v109, v109, v42\n"
    "v_add_f32 v109, v109, v43\n"
    "v_mfma_f32_16x16x32_bf16 a[168:171], a[216:219], v[16:19], a[168:171]\n"
    "v_cvt_pk_bf16_f32 v41, v42, v43\n"
    "v_fma_f32 v46, v46, s39, -v102\n"
    "v_mfma_f32_16x16x32_bf16 a[172:175], a[216:219], v[24:27], a[172:175]\n"
    "ds_read_b128 a[216:219], v96 offset:8192\n"
    "v_fma_f32 v47, v47, s39, -v102\n"
    "v_mfma_f32_16x16x32_bf16 a[188:191], a[220:223], v[24:27], a[188:191]\n"
    "ds_read_b128 v[118:121], v113 offset:8448\n"
    "v_fma_f32 v48, v48, s39, -v103\n"
    "v_mfma_f32_16x16x32_bf16 a[184:187], a[220:223], v[16:19], a[184:187]\n"
    "v_exp_f32 v44, v44\n"
    "v_mfma_f32_16x16x32_bf16 a[180:183], a[220:223], v[8:11], a[180:183]\n"
    "v_exp_f32 v45, v45\n"
    "v_mfma_f32_16x16x32_bf16 a[176:179], a[220:223], v[0:3], a[176:179]\n"
    "ds_read_b128 a[220:223], v96 offset:8704\n"
    "s_waitcnt lgkmcnt(9)\n"
    "v_mfma_f32_16x16x32_bf16 a[192:195], a[240:243], v[0:3], a[192:195]\n"
    "v_exp_f32 v46, v46\n"
    "v_mfma_f32_16x16x32_bf16 a[196:199], a[240:243], v[8:11], a[196:199]\n"
    "v_cvt_pk_bf16_f32 v42, v44, v45\n"
    "v_add_f32 v44, v109, v44\n"
    "v_mfma_f32_16x16x32_bf16 a[200:203], a[240:243], v[16:19], a[200:203]\n"
    "s_waitcnt lgkmcnt(1)\n"
    "buffer_store_dwordx4 v[118:121], v110, s[24:27], s85 offen offset:384 sc1\n"
    "v_mfma_f32_16x16x32_bf16 a[204:207], a[240:243], v[24:27], a[204:207]\n"
    "v_exp_f32 v47, v47\n"
    "v_mfma_f32_16x16x32_bf16 v[76:79], a[244:247], v[24:27], v[76:79]\n"
    "v_add_f32 v44, v44, v45\n"
    "v_add_f32 v44, v44, v46\n"
    "v_mfma_f32_16x16x32_bf16 v[72:75], a[244:247], v[16:19], v[72:75]\n"
    "v_cvt_pk_bf16_f32 v43, v46, v47\n"
    "v_add_f32 v44, v44, v47\n"
    "v_mfma_f32_16x16x32_bf16 v[68:71], a[244:247], v[8:11], v[68:71]\n"
    "v_fma_f32 v49, v49, s39, -v103\n"
    "v_fma_f32 v50, v50, s39, -v103\n"
    "v_mfma_f32_16x16x32_bf16 v[64:67], a[244:247], v[0:3], v[64:67]\n"
    "v_fma_f32 v51, v51, s39, -v103\n"
    "v_mfma_f32_16x16x32_bf16 a[224:227], a[248:251], v[0:3], a[224:227]\n"
    "v_exp_f32 v48, v48\n"
    "v_mfma_f32_16x16x32_bf16 a[228:231], a[248:251], v[8:11], a[228:231]\n"
    "v_exp_f32 v49, v49\n"
    "v_mfma_f32_16x16x32_bf16 a[232:235], a[248:251], v[16:19], a[232:235]\n"
    "v_exp_f32 v50, v50\n"
    "v_mfma_f32_16x16x32_bf16 a[236:239], a[248:251], v[24:27], a[236:239]\n"
    "v_exp_f32 v51, v51\n"
    "v_mfma_f32_16x16x32_bf16 v[92:95], a[252:255], v[24:27], v[92:95]\n"
    "v_add_f32 v109, v48, v49\n"
    "v_cvt_pk_bf16_f32 v48, v48, v49\n"
    "v_mfma_f32_16x16x32_bf16 v[88:91], a[252:255], v[16:19], v[88:91]\n"
    "v_add_f32 v109, v109, v50\n"
    "v_add_f32 v109, v109, v51\n"
    "v_mfma_f32_16x16x32_bf16 v[84:87], a[252:255], v[8:11], v[84:87]\n"
    "s_add_u32 s69, s69, 4096\n"
    "s_and_b32 s69, s69, 0xffff\n"
    "s_xor_b32 s85, s69, s87\n"
    "v_add_u32 v113, 2048, v113\n"
    "v_mfma_f32_16x16x32_bf16 v[80:83], a[252:255], v[0:3], v[80:83]\n"
    "s_sub_u32 s37, s37, 1\n"
    "s_cbranch_scc0 L_loop1\n"
    "; final\n"
    "v_subrev_u32 v96, 16384, v96\n"
    "v_subrev_u32 v97, 16384, v97\n"
    "v_subrev_u32 v98, 16384, v98\n"
    "v_subrev_u32 v113, 8192, v113\n"
    "v_mfma_f32_16x16x32_bf16 v[0:3], a[208:211], a[0:3], 0\n"
    "ds_read_b128 a[240:243], v96 offset:17408\n"
    "ds_read_b128 a[244:247], v96 offset:17920\n"
    "v_mfma_f32_16x16x32_bf16 v[0:3], a[212:215], a[4:7], v[0:3]\n"
    "ds_read_b128 a[248:251], v96 offset:25600\n"
    "ds_read_b128 a[252:255], v96 offset:26112\n"
    "v_mfma_f32_16x16x32_bf16 v[8:11], a[208:211], a[32:35], 0\n"
    "v_cvt_pk_bf16_f32 v49, v50, v51\n"
    "v_fma_f32 v52, v52, s39, -v103\n"
    "v_mfma_f32_16x16x32_bf16 v[8:11], a[212:215], a[36:39], v[8:11]\n"
    "v_fma_f32 v53, v53, s39, -v103\n"
    "v_fma_f32 v54, v54, s39, -v103\n"
    "v_mfma_f32_16x16x32_bf16 v[16:19], a[208:211], a[64:67], 0\n"
    "v_fma_f32 v55, v55, s39, -v103\n"
    "v_fma_f32 v56, v56, s39, -v104\n"
    "v_mfma_f32_16x16x32_bf16 v[16:19], a[212:215], a[68:71], v[16:19]\n"
    "v_exp_f32 v52, v52\n"
    "v_mfma_f32_16x16x32_bf16 v[24:27], a[208:211], a[96:99], 0\n"
    "ds_read_b128 a[208:211], v96 offset:18432\n"
    "v_fma_f32 v57, v57, s39, -v104\n"
    "v_mfma_f32_16x16x32_bf16 v[24:27], a[212:215], a[100:103], v[24:27]\n"
    "ds_read_b128 a[212:215], v96 offset:18944\n"
    "s_waitcnt lgkmcnt(6)\n"
    "v_mfma_f32_16x16x32_bf16 v[4:7], a[216:219], a[0:3], 0\n"
    "v_exp_f32 v53, v53\n"
    "v_mfma_f32_16x16x32_bf16 v[4:7], a[220:223], a[4:7], v[4:7]\n"
    "v_exp_f32 v54, v54\n"
    "v_mfma_f32_16x16x32_bf16 v[12:15], a[216:219], a[32:35], 0\n"
    "v_cvt_pk_bf16_f32 v50, v52, v53\n"
    "v_add_f32 v52, v109, v52\n"
    "v_mfma_f32_16x16x32_bf16 v[12:15], a[220:223], a[36:39], v[12:15]\n"
    "v_exp_f32 v55, v55\n"
    "v_mfma_f32_16x16x32_bf16 v[20:23], a[216:219], a[64:67], 0\n"
    "v_add_f32 v52, v52, v53\n"
    "v_add_f32 v52, v52, v54\n"
    "v_mfma_f32_16x16x32_bf16 v[20:23], a[220:223], a[68:71], v[20:23]\n"
    "v_cvt_pk_bf16_f32 v51, v54, v55\n"
    "v_add_f32 v52, v52, v55\n"
    "v_mfma_f32_16x16x32_bf16 v[28:31], a[216:219], a[96:99], 0\n"
    "ds_read_b128 a[216:219], v96 offset:26624\n"
    "v_fma_f32 v58, v58, s39, -v104\n"
    "v_mfma_f32_16x16x32_bf16 v[28:31], a[220:223], a[100:103], v[28:31]\n"
    "ds_read_b128 a[220:223], v96 offset:27136\n"
    "s_waitcnt lgkmcnt(6)\n"
    "v_mfma_f32_16x16x32_bf16 v[0:3], a[240:243], a[8:11], v[0:3]\n"
    "v_fma_f32 v59, v59, s39, -v104\n"
    "v_fma_f32 v60, v60, s39, -v104\n"
    "v_mfma_f32_16x16x32_bf16 v[0:3], a[244:247], a[12:15], v[0:3]\n"
    "v_exp_f32 v56, v56\n"
    "v_mfma_f32_16x16x32_bf16 v[8:11], a[240:243], a[40:43], v[8:11]\n"
    "v_exp_f32 v57, v57\n"
    "v_mfma_f32_16x16x32_bf16 v[8:11], a[244:247], a[44:47], v[8:11]\n"
    "v_exp_f32 v58, v58\n"
    "v_mfma_f32_16x16x32_bf16 v[16:19], a[240:243], a[72:75], v[16:19]\n"
    "v_exp_f32 v59, v59\n"
    "v_mfma_f32_16x16x32_bf16 v[16:19], a[244:247], a[76:79], v[16:19]\n"
    "v_add_f32 v109, v56, v57\n"
    "v_cvt_pk_bf16_f32 v56, v56, v57\n"
    "v_mfma_f32_16x16x32_bf16 v[24:27], a[240:243], a[104:107], v[24:27]\n"
    "ds_read_b128 a[240:243], v96 offset:19456\n"
    "v_add_f32 v109, v109, v58\n"
    "v_mfma_f32_16x16x32_bf16 v[24:27], a[244:247], a[108:111], v[24:27]\n"
    "ds_read_b128 a[244:247], v96 offset:19968\n"
    "s_waitcnt lgkmcnt(6)\n"
    "v_mfma_f32_16x16x32_bf16 v[4:7], a[248:251], a[8:11], v[4:7]\n"
    "v_add_f32 v109, v109, v59\n"
    "v_cvt_pk_bf16_f32 v57, v58, v59\n"
    "v_mfma_f32_16x16x32_bf16 v[4:7], a[252:255], a[12:15], v[4:7]\n"
    "v_fma_f32 v61, v61, s39, -v104\n"
    "v_fma_f32 v62, v62, s39, -v104\n"
    "v_mfma_f32_16x16x32_bf16 v[12:15], a[248:251], a[40:43], v[12:15]\n"
    "v_fma_f32 v63, v63, s39, -v104\n"
    "v_mfma_f32_16x16x32_bf16 v[12:15], a[252:255], a[44:47], v[12:15]\n"
    "v_exp_f32 v60, v60\n"
    "v_mfma_f32_16x16x32_bf16 v[20:23], a[248:251], a[72:75], v[20:23]\n"
    "v_exp_f32 v61, v61\n"
    "v_mfma_f32_16x16x32_bf16 v[20:23], a[252:255], a[76:79], v[20:23]\n"
    "v_exp_f32 v62, v62\n"
    "v_mfma_f32_16x16x32_bf16 v[28:31], a[248:251], a[104:107], v[28:31]\n"
    "ds_read_b128 a[248:251], v96 offset:27648\n"
    "v_cvt_pk_bf16_f32 v58, v60, v61\n"
    "v_mfma_f32_16x16x32_bf16 v[28:31], a[252:255], a[108:111], v[28:31]\n"
    "ds_read_b128 a[252:255], v96 offset:28160\n"
    "s_waitcnt lgkmcnt(6)\n"
    "v_mfma_f32_16x16x32_bf16 v[0:3], a[208:211], a[16:19], v[0:3]\n"
    "v_add_f32 v60, v109, v60\n"
    "v_add_f32 v60, v60, v61\n"
    "v_mfma_f32_16x16x32_bf16 v[0:3], a[212:215], a[20:23], v[0:3]\n"
    "v_exp_f32 v63, v63\n"
    "v_mfma_f32_16x16x32_bf16 v[8:11], a[208:211], a[48:51], v[8:11]\n"
    "v_max3_f32 v109, v36, v44, v52\n"
    "v_add_f32 v60, v60, v62\n"
    "v_mfma_f32_16x16x32_bf16 v[8:11], a[212:215], a[52:55], v[8:11]\n"
    "v_cvt_pk_bf16_f32 v59, v62, v63\n"
    "v_add_f32 v60, v60, v63\n"
    "v_mfma_f32_16x16x32_bf16 v[16:19], a[208:211], a[80:83], v[16:19]\n"
    "v_max_f32 v109, v109, v60\n"
    "v_cmp_le_f32 vcc, 1.0, v109\n"
    "v_mfma_f32_16x16x32_bf16 v[16:19], a[212:215], a[84:87], v[16:19]\n"
    "s_cbranch_vccnz L_ra8\n"
    "L_ra8_ret:\n"
    "v_add_f32 v105, v105, v36\n"
    "v_mfma_f32_16x16x32_bf16 v[24:27], a[208:211], a[112:115], v[24:27]\n"
    "v_add_f32 v106, v106, v44\n"
    "v_add_f32 v107, v107, v52\n"
    "v_mfma_f32_16x16x32_bf16 v[24:27], a[212:215], a[116:119], v[24:27]\n"
    "v_add_f32 v108, v108, v60\n"
    "s_waitcnt lgkmcnt(4)\n"
    "v_mfma_f32_16x16x32_bf16 v[4:7], a[216:219], a[16:19], v[4:7]\n"
    "v_mfma_f32_16x16x32_bf16 v[4:7], a[220:223], a[20:23], v[4:7]\n"
    "v_mfma_f32_16x16x32_bf16 v[12:15], a[216:219], a[48:51], v[12:15]\n"
    "v_mfma_f32_16x16x32_bf16 v[12:15], a[220:223], a[52:55], v[12:15]\n"
    "v_mfma_f32_16x16x32_bf16 v[20:23], a[216:219], a[80:83], v[20:23]\n"
    "s_waitcnt vmcnt(11) lgkmcnt(0)\n"
    "s_barrier\n"
    "v_mfma_f32_16x16x32_bf16 v[20:23], a[220:223], a[84:87], v[20:23]\n"
    "ds_read_b64_tr_b16 a[208:209], v97 offset:32768\n"
    "ds_read_b64_tr_b16 a[210:211], v97 offset:40960\n"
    "v_mfma_f32_16x16x32_bf16 v[28:31], a[216:219], a[112:115], v[28:31]\n"
    "s_add_u32 m0, s38, 16384\n"
    "s_mov_b32 s76, 507904\n"
    "v_mfma_f32_16x16x32_bf16 v[28:31], a[220:223], a[116:119], v[28:31]\n"
    "buffer_load_dwordx4 v99, s[16:19], s76 offen offset:0 nt lds\n"
    "v_mfma_f32_16x16x32_bf16 v[0:3], a[240:243], a[24:27], v[0:3]\n"
    "ds_read_b64_tr_b16 a[212:213], v98 offset:32768\n"
    "ds_read_b64_tr_b16 a[214:215], v98 offset:40960\n"
    "v_mfma_f32_16x16x32_bf16 v[0:3], a[244:247], a[28:31], v[0:3]\n"
    "ds_read_b64_tr_b16 a[216:217], v97 offset:33280\n"
    "ds_read_b64_tr_b16 a[218:219], v97 offset:41472\n"
    "v_mfma_f32_16x16x32_bf16 v[8:11], a[240:243], a[56:59], v[8:11]\n"
    "ds_read_b64_tr_b16 a[220:221], v98 offset:33280\n"
    "ds_read_b64_tr_b16 a[222:223], v98 offset:41472\n"
    "v_mfma_f32_16x16x32_bf16 v[8:11], a[244:247], a[60:63], v[8:11]\n"
    "v_mfma_f32_16x16x32_bf16 v[16:19], a[240:243], a[88:91], v[16:19]\n"
    "s_add_u32 m0, s38, 17280\n"
    "v_mfma_f32_16x16x32_bf16 v[16:19], a[244:247], a[92:95], v[16:19]\n"
    "buffer_load_dwordx4 v99, s[16:19], s76 offen offset:128 nt lds\n"
    "v_fma_f32 v0, v0, s39, -v101\n"
    "v_fma_f32 v1, v1, s39, -v101\n"
    "v_mfma_f32_16x16x32_bf16 v[24:27], a[240:243], a[120:123], v[24:27]\n"
    "ds_read_b64_tr_b16 a[240:241], v97 offset:33792\n"
    "ds_read_b64_tr_b16 a[242:243], v97 offset:41984\n"
    "v_mfma_f32_16x16x32_bf16 v[24:27], a[244:247], a[124:127], v[24:27]\n"
    "ds_read_b64_tr_b16 a[244:245], v98 offset:33792\n"
    "v_mfma_f32_16x16x32_bf16 v[4:7], a[248:251], a[24:27], v[4:7]\n"
    "ds_read_b64_tr_b16 a[246:247], v98 offset:41984\n"
    "v_fma_f32 v2, v2, s39, -v101\n"
    "v_fma_f32 v3, v3, s39, -v101\n"
    "v_mfma_f32_16x16x32_bf16 v[4:7], a[252:255], a[28:31], v[4:7]\n"
    "v_exp_f32 v0, v0\n"
    "v_mfma_f32_16x16x32_bf16 v[12:15], a[248:251], a[56:59], v[12:15]\n"
    "s_add_u32 m0, s38, 18176\n"
    "v_fma_f32 v8, v8, s39, -v102\n"
    "v_mfma_f32_16x16x32_bf16 v[12:15], a[252:255], a[60:63], v[12:15]\n"
    "buffer_load_dwordx4 v99, s[16:19], s76 offen offset:256 nt lds\n"
    "v_exp_f32 v1, v1\n"
    "v_mfma_f32_16x16x32_bf16 v[20:23], a[248:251], a[88:91], v[20:23]\n"
    "v_exp_f32 v2, v2\n"
    "v_mfma_f32_16x16x32_bf16 v[20:23], a[252:255], a[92:95], v[20:23]\n"
    "v_exp_f32 v3, v3\n"
    "v_mfma_f32_16x16x32_bf16 v[28:31], a[248:251], a[120:123], v[28:31]\n"
    "ds_read_b64_tr_b16 a[248:249], v97 offset:34304\n"
    "ds_read_b64_tr_b16 a[250:251], v97 offset:42496\n"
    "v_mfma_f32_16x16x32_bf16 v[28:31], a[252:255], a[124:127], v[28:31]\n"
    "ds_read_b64_tr_b16 a[252:253], v98 offset:34304\n"
    "ds_read_b64_tr_b16 a[254:255], v98 offset:42496\n"
    "s_waitcnt lgkmcnt(12)\n"
    "v_mfma_f32_16x16x32_bf16 v[128:131], a[208:211], v[32:35], v[128:131]\n"
    "v_add_f32 v109, v0, v1\n"
    "v_cvt_pk_bf16_f32 v0, v0, v1\n"
    "v_mfma_f32_16x16x32_bf16 v[132:135], a[208:211], v[40:43], v[132:135]\n"
    "s_add_u32 m0, s38, 19072\n"
    "v_add_f32 v109, v109, v2\n"
    "v_mfma_f32_16x16x32_bf16 v[136:139], a[208:211], v[48:51], v[136:139]\n"
    "buffer_load_dwordx4 v99, s[16:19], s76 offen offset:384 nt lds\n"
    "v_add_f32 v109, v109, v3\n"
    "v_cvt_pk_bf16_f32 v1, v2, v3\n"
    "v_mfma_f32_16x16x32_bf16 v[140:143], a[208:211], v[56:59], v[140:143]\n"
    "ds_read_b64_tr_b16 a[208:209], v97 offset:34816\n"
    "ds_read_b64_tr_b16 a[210:211], v97 offset:43008\n"
    "v_mfma_f32_16x16x32_bf16 v[156:159], a[212:215], v[56:59], v[156:159]\n"
    "v_fma_f32 v4, v4, s39, -v101\n"
    "v_fma_f32 v5, v5, s39, -v101\n"
    "v_mfma_f32_16x16x32_bf16 v[152:155], a[212:215], v[48:51], v[152:155]\n"
    "v_fma_f32 v6, v6, s39, -v101\n"
    "v_fma_f32 v7, v7, s39, -v101\n"
    "v_mfma_f32_16x16x32_bf16 v[148:151], a[212:215], v[40:43], v[148:151]\n"
    "v_exp_f32 v4, v4\n"
    "v_fma_f32 v9, v9, s39, -v102\n"
    "v_mfma_f32_16x16x32_bf16 v[144:147], a[212:215], v[32:35], v[144:147]\n"
    "ds_read_b64_tr_b16 a[212:213], v98 offset:34816\n"
    "s_waitcnt lgkmcnt(11)\n"
    "v_mfma_f32_16x16x32_bf16 v[160:163], a[216:219], v[32:35], v[160:163]\n"
    "ds_read_b64_tr_b16 a[214:215], v98 offset:43008\n"
    "v_fma_f32 v10, v10, s39, -v102\n"
    "v_mfma_f32_16x16x32_bf16 v[164:167], a[216:219], v[40:43], v[164:167]\n"
    "s_add_u32 m0, s38, 65536\n"
    "s_mov_b32 s77, 507904\n"
    "v_fma_f32 v11, v11, s39, -v102\n"
    "v_mfma_f32_16x16x32_bf16 v[168:171], a[216:219], v[48:51], v[168:171]\n"
    "buffer_load_dwordx4 v100, s[20:23], s77 offen offset:0 nt lds\n"
    "v_fma_f32 v12, v12, s39, -v102\n"
    "v_mfma_f32_16x16x32_bf16 v[172:175], a[216:219], v[56:59], v[172:175]\n"
    "ds_read_b64_tr_b16 a[216:217], v97 offset:35328\n"
    "ds_read_b64_tr_b16 a[218:219], v97 offset:43520\n"
    "v_mfma_f32_16x16x32_bf16 v[188:191], a[220:223], v[56:59], v[188:191]\n"
    "v_exp_f32 v5, v5\n"
    "v_mfma_f32_16x16x32_bf16 v[184:187], a[220:223], v[48:51], v[184:187]\n"
    "v_exp_f32 v6, v6\n"
    "v_cvt_pk_bf16_f32 v2, v4, v5\n"
    "v_mfma_f32_16x16x32_bf16 v[180:183], a[220:223], v[40:43], v[180:183]\n"
    "v_add_f32 v4, v109, v4\n"
    "v_add_f32 v4, v4, v5\n"
    "v_mfma_f32_16x16x32_bf16 v[176:179], a[220:223], v[32:35], v[176:179]\n"
    "ds_read_b64_tr_b16 a[220:221], v98 offset:35328\n"
    "s_waitcnt lgkmcnt(11)\n"
    "v_mfma_f32_16x16x32_bf16 v[192:195], a[240:243], v[32:35], v[192:195]\n"
    "ds_read_b64_tr_b16 a[222:223], v98 offset:43520\n"
    "v_exp_f32 v7, v7\n"
    "v_mfma_f32_16x16x32_bf16 v[196:199], a[240:243], v[40:43], v[196:199]\n"
    "s_add_u32 m0, s38, 66432\n"
    "v_add_f32 v4, v4, v6\n"
    "v_mfma_f32_16x16x32_bf16 v[200:203], a[240:243], v[48:51], v[200:203]\n"
    "buffer_load_dwordx4 v100, s[20:23], s77 offen offset:128 nt lds\n"
    "v_cvt_pk_bf16_f32 v3, v6, v7\n"
    "v_mfma_f32_16x16x32_bf16 v[204:207], a[240:243], v[56:59], v[204:207]\n"
    "ds_read_b64_tr_b16 a[240:241], v97 offset:35840\n"
    "ds_read_b64_tr_b16 a[242:243], v97 offset:44032\n"
    "v_mfma_f32_16x16x32_bf16 v[220:223], a[244:247], v[56:59], v[220:223]\n"
    "v_add_f32 v4, v4, v7\n"
    "v_exp_f32 v8, v8\n"
    "v_mfma_f32_16x16x32_bf16 v[216:219], a[244:247], v[48:51], v[216:219]\n"
    "v_exp_f32 v9, v9\n"
    "v_mfma_f32_16x16x32_bf16 v[212:215], a[244:247], v[40:43], v[212:215]\n"
    "v_exp_f32 v10, v10\n"
    "v_mfma_f32_16x16x32_bf16 v[208:211], a[244:247], v[32:35], v[208:211]\n"
    "ds_read_b64_tr_b16 a[244:245], v98 offset:35840\n"
    "ds_read_b64_tr_b16 a[246:247], v98 offset:44032\n"
    "s_waitcnt lgkmcnt(12)\n"
    "v_mfma_f32_16x16x32_bf16 v[224:227], a[248:251], v[32:35], v[224:227]\n"
    "v_exp_f32 v11, v11\n"
    "v_mfma_f32_16x16x32_bf16 v[228:231], a[248:251], v[40:43], v[228:231]\n"
    "s_add_u32 m0, s38, 67328\n"
    "v_add_f32 v109, v8, v9\n"
    "v_mfma_f32_16x16x32_bf16 v[232:235], a[248:251], v[48:51], v[232:235]\n"
    "buffer_load_dwordx4 v100, s[20:23], s77 offen offset:256 nt lds\n"
    "v_cvt_pk_bf16_f32 v8, v8, v9\n"
    "v_mfma_f32_16x16x32_bf16 v[236:239], a[248:251], v[56:59], v[236:239]\n"
    "ds_read_b64_tr_b16 a[248:249], v97 offset:36352\n"
    "ds_read_b64_tr_b16 a[250:251], v97 offset:44544\n"
    "v_add_f32 v109, v109, v10\n"
    "v_mfma_f32_16x16x32_bf16 v[252:255], a[252:255], v[56:59], v[252:255]\n"
    "v_add_f32 v109, v109, v11\n"
    "v_cvt_pk_bf16_f32 v9, v10, v11\n"
    "v_mfma_f32_16x16x32_bf16 v[248:251], a[252:255], v[48:51], v[248:251]\n"
    "v_fma_f32 v13, v13, s39, -v102\n"
    "v_fma_f32 v14, v14, s39, -v102\n"
    "v_mfma_f32_16x16x32_bf16 v[244:247], a[252:255], v[40:43], v[244:247]\n"
    "v_fma_f32 v15, v15, s39, -v102\n"
    "v_exp_f32 v12, v12\n"
    "v_mfma_f32_16x16x32_bf16 v[240:243], a[252:255], v[32:35], v[240:243]\n"
    "ds_read_b64_tr_b16 a[252:253], v98 offset:36352\n"
    "s_waitcnt lgkmcnt(11)\n"
    "v_mfma_f32_16x16x32_bf16 a[128:131], a[208:211], v[32:35], a[128:131]\n"
    "ds_read_b64_tr_b16 a[254:255], v98 offset:44544\n"
    "v_fma_f32 v16, v16, s39, -v103\n"
    "v_mfma_f32_16x16x32_bf16 a[132:135], a[208:211], v[40:43], a[132:135]\n"
    "s_add_u32 m0, s38, 68224\n"
    "v_fma_f32 v17, v17, s39, -v103\n"
    "v_mfma_f32_16x16x32_bf16 a[136:139], a[208:211], v[48:51], a[136:139]\n"
    "buffer_load_dwordx4 v100, s[20:23], s77 offen offset:384 nt lds\n"
    "v_exp_f32 v13, v13\n"
    "v_mfma_f32_16x16x32_bf16 a[140:143], a[208:211], v[56:59], a[140:143]\n"
    "ds_read_b128 a[208:211], v96 offset:32768\n"
    "v_cvt_pk_bf16_f32 v10, v12, v13\n"
    "v_mfma_f32_16x16x32_bf16 a[156:159], a[212:215], v[56:59], a[156:159]\n"
    "v_exp_f32 v14, v14\n"
    "v_mfma_f32_16x16x32_bf16 a[152:155], a[212:215], v[48:51], a[152:155]\n"
    "v_add_f32 v12, v109, v12\n"
    "v_exp_f32 v15, v15\n"
    "v_mfma_f32_16x16x32_bf16 a[148:151], a[212:215], v[40:43], a[148:151]\n"
    "v_add_f32 v12, v12, v13\n"
    "v_add_f32 v12, v12, v14\n"
    "v_mfma_f32_16x16x32_bf16 a[144:147], a[212:215], v[32:35], a[144:147]\n"
    "ds_read_b128 a[212:215], v96 offset:33280\n"
    "s_waitcnt lgkmcnt(10)\n"
    "v_mfma_f32_16x16x32_bf16 a[160:163], a[216:219], v[32:35], a[160:163]\n"
    "v_cvt_pk_bf16_f32 v11, v14, v15\n"
    "v_add_f32 v12, v12, v15\n"
    "v_mfma_f32_16x16x32_bf16 a[164:167], a[216:219], v[40:43], a[164:167]\n"
    "v_fma_f32 v18, v18, s39, -v103\n"
    "v_fma_f32 v19, v19, s39, -v103\n"
    "v_fma_f32 v20, v20, s39, -v103\n"
    "v_mfma_f32_16x16x32_bf16 a[168:171], a[216:219], v[48:51], a[168:171]\n"
    "v_exp_f32 v16, v16\n"
    "v_mfma_f32_16x16x32_bf16 a[172:175], a[216:219], v[56:59], a[172:175]\n"
    "ds_read_b128 a[216:219], v96 offset:40960\n"
    "v_fma_f32 v21, v21, s39, -v103\n"
    "v_mfma_f32_16x16x32_bf16 a[188:191], a[220:223], v[56:59], a[188:191]\n"
    "v_exp_f32 v17, v17\n"
    "v_fma_f32 v22, v22, s39, -v103\n"
    "v_mfma_f32_16x16x32_bf16 a[184:187], a[220:223], v[48:51], a[184:187]\n"
    "v_exp_f32 v18, v18\n"
    "v_mfma_f32_16x16x32_bf16 a[180:183], a[220:223], v[40:43], a[180:183]\n"
    "v_exp_f32 v19, v19\n"
    "v_mfma_f32_16x16x32_bf16 a[176:179], a[220:223], v[32:35], a[176:179]\n"
    "ds_read_b128 a[220:223], v96 offset:41472\n"
    "s_waitcnt lgkmcnt(8)\n"
    "v_mfma_f32_16x16x32_bf16 a[192:195], a[240:243], v[32:35], a[192:195]\n"
    "v_add_f32 v109, v16, v17\n"
    "v_cvt_pk_bf16_f32 v16, v16, v17\n"
    "v_add_f32 v109, v109, v18\n"
    "v_mfma_f32_16x16x32_bf16 a[196:199], a[240:243], v[40:43], a[196:199]\n"
    "v_add_f32 v109, v109, v19\n"
    "v_cvt_pk_bf16_f32 v17, v18, v19\n"
    "v_mfma_f32_16x16x32_bf16 a[200:203], a[240:243], v[48:51], a[200:203]\n"
    "v_fma_f32 v23, v23, s39, -v103\n"
    "v_fma_f32 v24, v24, s39, -v104\n"
    "v_mfma_f32_16x16x32_bf16 a[204:207], a[240:243], v[56:59], a[204:207]\n"
    "ds_read_b128 a[240:243], v96 offset:33792\n"
    "v_exp_f32 v20, v20\n"
    "v_mfma_f32_16x16x32_bf16 v[76:79], a[244:247], v[56:59], v[76:79]\n"
    "v_exp_f32 v21, v21\n"
    "v_mfma_f32_16x16x32_bf16 v[72:75], a[244:247], v[48:51], v[72:75]\n"
    "v_exp_f32 v22, v22\n"
    "v_mfma_f32_16x16x32_bf16 v[68:71], a[244:247], v[40:43], v[68:71]\n"
    "v_cvt_pk_bf16_f32 v18, v20, v21\n"
    "v_add_f32 v20, v109, v20\n"
    "v_mfma_f32_16x16x32_bf16 v[64:67], a[244:247], v[32:35], v[64:67]\n"
    "ds_read_b128 a[244:247], v96 offset:34304\n"
    "v_add_f32 v20, v20, v21\n"
    "s_waitcnt lgkmcnt(6)\n"
    "v_mfma_f32_16x16x32_bf16 a[224:227], a[248:251], v[32:35], a[224:227]\n"
    "v_exp_f32 v23, v23\n"
    "v_mfma_f32_16x16x32_bf16 a[228:231], a[248:251], v[40:43], a[228:231]\n"
    "v_add_f32 v20, v20, v22\n"
    "v_cvt_pk_bf16_f32 v19, v22, v23\n"
    "v_mfma_f32_16x16x32_bf16 a[232:235], a[248:251], v[48:51], a[232:235]\n"
    "v_add_f32 v20, v20, v23\n"
    "v_fma_f32 v25, v25, s39, -v104\n"
    "v_fma_f32 v26, v26, s39, -v104\n"
    "v_mfma_f32_16x16x32_bf16 a[236:239], a[248:251], v[56:59], a[236:239]\n"
    "ds_read_b128 a[248:251], v96 offset:41984\n"
    "v_fma_f32 v27, v27, s39, -v104\n"
    "v_mfma_f32_16x16x32_bf16 v[92:95], a[252:255], v[56:59], v[92:95]\n"
    "v_exp_f32 v24, v24\n"
    "v_mfma_f32_16x16x32_bf16 v[88:91], a[252:255], v[48:51], v[88:91]\n"
    "v_exp_f32 v25, v25\n"
    "v_mfma_f32_16x16x32_bf16 v[84:87], a[252:255], v[40:43], v[84:87]\n"
    "v_exp_f32 v26, v26\n"
    "v_add_f32 v109, v24, v25\n"
    "v_mfma_f32_16x16x32_bf16 v[80:83], a[252:255], v[32:35], v[80:83]\n"
    "ds_read_b128 a[252:255], v96 offset:42496\n"
    "s_waitcnt lgkmcnt(6)\n"
    "v_mfma_f32_16x16x32_bf16 v[32:35], a[208:211], a[0:3], 0\n"
    "v_exp_f32 v27, v27\n"
    "v_mfma_f32_16x16x32_bf16 v[32:35], a[212:215], a[4:7], v[32:35]\n"
    "v_cvt_pk_bf16_f32 v24, v24, v25\n"
    "v_add_f32 v109, v109, v26\n"
    "v_add_f32 v109, v109, v27\n"
    "v_mfma_f32_16x16x32_bf16 v[40:43], a[208:211], a[32:35], 0\n"
    "ds_read_b128 a[208:211], v96 offset:34816\n"
    "v_cvt_pk_bf16_f32 v25, v26, v27\n"
    "v_mfma_f32_16x16x32_bf16 v[40:43], a[212:215], a[36:39], v[40:43]\n"
    "ds_read_b128 a[212:215], v96 offset:35328\n"
    "s_waitcnt lgkmcnt(6)\n"
    "v_mfma_f32_16x16x32_bf16 v[36:39], a[216:219], a[0:3], 0\n"
    "v_fma_f32 v28, v28, s39, -v104\n"
    "v_fma_f32 v29, v29, s39, -v104\n"
    "v_mfma_f32_16x16x32_bf16 v[36:39], a[220:223], a[4:7], v[36:39]\n"
    "v_fma_f32 v30, v30, s39, -v104\n"
    "v_fma_f32 v31, v31, s39, -v104\n"
    "v_mfma_f32_16x16x32_bf16 v[44:47], a[216:219], a[32:35], 0\n"
    "ds_read_b128 a[216:219], v96 offset:43008\n"
    "v_exp_f32 v28, v28\n"
    "v_mfma_f32_16x16x32_bf16 v[44:47], a[220:223], a[36:39], v[44:47]\n"
    "ds_read_b128 a[220:223], v96 offset:43520\n"
    "s_waitcnt lgkmcnt(6)\n"
    "v_mfma_f32_16x16x32_bf16 v[32:35], a[240:243], a[8:11], v[32:35]\n"
    "v_exp_f32 v29, v29\n"
    "s_nop 0\n"
    "v_cvt_pk_bf16_f32 v26, v28, v29\n"
    "v_mfma_f32_16x16x32_bf16 v[32:35], a[244:247], a[12:15], v[32:35]\n"
    "v_exp_f32 v30, v30\n"
    "v_mfma_f32_16x16x32_bf16 v[40:43], a[240:243], a[40:43], v[40:43]\n"
    "ds_read_b128 a[240:243], v96 offset:35840\n"
    "v_add_f32 v28, v109, v28\n"
    "v_mfma_f32_16x16x32_bf16 v[40:43], a[244:247], a[44:47], v[40:43]\n"
    "ds_read_b128 a[244:247], v96 offset:36352\n"
    "s_waitcnt lgkmcnt(6)\n"
    "v_mfma_f32_16x16x32_bf16 v[36:39], a[248:251], a[8:11], v[36:39]\n"
    "v_exp_f32 v31, v31\n"
    "v_add_f32 v28, v28, v29\n"
    "v_mfma_f32_16x16x32_bf16 v[36:39], a[252:255], a[12:15], v[36:39]\n"
    "v_max3_f32 v109, v4, v12, v20\n"
    "v_add_f32 v28, v28, v30\n"
    "v_mfma_f32_16x16x32_bf16 v[44:47], a[248:251], a[40:43], v[44:47]\n"
    "ds_read_b128 a[248:251], v96 offset:44032\n"
    "v_cvt_pk_bf16_f32 v27, v30, v31\n"
    "v_mfma_f32_16x16x32_bf16 v[44:47], a[252:255], a[44:47], v[44:47]\n"
    "ds_read_b128 a[252:255], v96 offset:44544\n"
    "v_add_f32 v28, v28, v31\n"
    "s_waitcnt lgkmcnt(6)\n"
    "v_mfma_f32_16x16x32_bf16 v[32:35], a[208:211], a[16:19], v[32:35]\n"
    "v_max_f32 v109, v109, v28\n"
    "v_cmp_le_f32 vcc, 1.0, v109\n"
    "v_mfma_f32_16x16x32_bf16 v[32:35], a[212:215], a[20:23], v[32:35]\n"
    "s_cbranch_vccnz L_ra9\n"
    "L_ra9_ret:\n"
    "v_add_f32 v105, v105, v4\n"
    "v_mfma_f32_16x16x32_bf16 v[40:43], a[208:211], a[48:51], v[40:43]\n"
    "s_waitcnt vmcnt(9) lgkmcnt(0)\n"
    "s_barrier\n"
    "v_mfma_f32_16x16x32_bf16 v[40:43], a[212:215], a[52:55], v[40:43]\n"
    "s_add_u32 m0, s38, 81920\n"
    "s_mov_b32 s77, 524288\n"
    "ds_read_b64_tr_b16 a[208:209], v97 offset:0\n"
    "v_mfma_f32_16x16x32_bf16 v[36:39], a[216:219], a[16:19], v[36:39]\n"
    "buffer_load_dwordx4 v100, s[20:23], s77 offen offset:0 nt lds\n"
    "ds_read_b64_tr_b16 a[210:211], v97 offset:8192\n"
    "v_mfma_f32_16x16x32_bf16 v[36:39], a[220:223], a[20:23], v[36:39]\n"
    "ds_read_b64_tr_b16 a[212:213], v98 offset:0\n"
    "ds_read_b64_tr_b16 a[214:215], v98 offset:8192\n"
    "v_mfma_f32_16x16x32_bf16 v[44:47], a[216:219], a[48:51], v[44:47]\n"
    "ds_read_b64_tr_b16 a[216:217], v97 offset:512\n"
    "ds_read_b64_tr_b16 a[218:219], v97 offset:8704\n"
    "v_mfma_f32_16x16x32_bf16 v[44:47], a[220:223], a[52:55], v[44:47]\n"
    "s_add_u32 m0, s38, 82816\n"
    "ds_read_b64_tr_b16 a[220:221], v98 offset:512\n"
    "v_mfma_f32_16x16x32_bf16 v[32:35], a[240:243], a[24:27], v[32:35]\n"
    "buffer_load_dwordx4 v100, s[20:23], s77 offen offset:128 nt lds\n"
    "ds_read_b64_tr_b16 a[222:223], v98 offset:8704\n"
    "v_mfma_f32_16x16x32_bf16 v[32:35], a[244:247], a[28:31], v[32:35]\n"
    "v_add_f32 v106, v106, v12\n"
    "v_add_f32 v107, v107, v20\n"
    "v_mfma_f32_16x16x32_bf16 v[40:43], a[240:243], a[56:59], v[40:43]\n"
    "ds_read_b64_tr_b16 a[240:241], v97 offset:1024\n"
    "ds_read_b64_tr_b16 a[242:243], v97 offset:9216\n"
    "v_add_f32 v108, v108, v28\n"
    "v_mfma_f32_16x16x32_bf16 v[40:43], a[244:247], a[60:63], v[40:43]\n"
    "s_add_u32 m0, s38, 83712\n"
    "v_mfma_f32_16x16x32_bf16 v[36:39], a[248:251], a[24:27], v[36:39]\n"
    "buffer_load_dwordx4 v100, s[20:23], s77 offen offset:256 nt lds\n"
    "ds_read_b64_tr_b16 a[244:245], v98 offset:1024\n"
    "v_mfma_f32_16x16x32_bf16 v[36:39], a[252:255], a[28:31], v[36:39]\n"
    "ds_read_b64_tr_b16 a[246:247], v98 offset:9216\n"
    "v_fma_f32 v32, v32, s39, -v101\n"
    "v_mfma_f32_16x16x32_bf16 v[44:47], a[248:251], a[56:59], v[44:47]\n"
    "ds_read_b64_tr_b16 a[248:249], v97 offset:1536\n"
    "ds_read_b64_tr_b16 a[250:251], v97 offset:9728\n"
    "v_mfma_f32_16x16x32_bf16 v[44:47], a[252:255], a[60:63], v[44:47]\n"
    "s_add_u32 m0, s38, 84608\n"
    "s_waitcnt lgkmcnt(10)\n"
    "v_mfma_f32_16x16x32_bf16 v[128:131], a[208:211], v[0:3], v[128:131]\n"
    "buffer_load_dwordx4 v100, s[20:23], s77 offen offset:384 nt lds\n"
    "ds_read_b64_tr_b16 a[252:253], v98 offset:1536\n"
    "v_mfma_f32_16x16x32_bf16 v[132:135], a[208:211], v[8:11], v[132:135]\n"
    "ds_read_b64_tr_b16 a[254:255], v98 offset:9728\n"
    "v_fma_f32 v33, v33, s39, -v101\n"
    "v_mfma_f32_16x16x32_bf16 v[136:139], a[208:211], v[16:19], v[136:139]\n"
    "v_fma_f32 v34, v34, s39, -v101\n"
    "v_fma_f32 v35, v35, s39, -v101\n"
    "v_mfma_f32_16x16x32_bf16 v[140:143], a[208:211], v[24:27], v[140:143]\n"
    "ds_read_b64_tr_b16 a[208:209], v97 offset:2048\n"
    "ds_read_b64_tr_b16 a[210:211], v97 offset:10240\n"
    "v_mfma_f32_16x16x32_bf16 v[156:159], a[212:215], v[24:27], v[156:159]\n"
    "v_exp_f32 v32, v32\n"
    "v_mfma_f32_16x16x32_bf16 v[152:155], a[212:215], v[16:19], v[152:155]\n"
    "v_exp_f32 v33, v33\n"
    "v_mfma_f32_16x16x32_bf16 v[148:151], a[212:215], v[8:11], v[148:151]\n"
    "v_exp_f32 v34, v34\n"
    "v_mfma_f32_16x16x32_bf16 v[144:147], a[212:215], v[0:3], v[144:147]\n"
    "ds_read_b64_tr_b16 a[212:213], v98 offset:2048\n"
    "s_waitcnt lgkmcnt(11)\n"
    "v_mfma_f32_16x16x32_bf16 v[160:163], a[216:219], v[0:3], v[160:163]\n"
    "ds_read_b64_tr_b16 a[214:215], v98 offset:10240\n"
    "v_add_f32 v109, v32, v33\n"
    "v_mfma_f32_16x16x32_bf16 v[164:167], a[216:219], v[8:11], v[164:167]\n"
    "v_exp_f32 v35, v35\n"
    "v_mfma_f32_16x16x32_bf16 v[168:171], a[216:219], v[16:19], v[168:171]\n"
    "v_cvt_pk_bf16_f32 v32, v32, v33\n"
    "v_add_f32 v109, v109, v34\n"
    "v_mfma_f32_16x16x32_bf16 v[172:175], a[216:219], v[24:27], v[172:175]\n"
    "ds_read_b64_tr_b16 a[216:217], v97 offset:2560\n"
    "ds_read_b64_tr_b16 a[218:219], v97 offset:10752\n"
    "v_mfma_f32_16x16x32_bf16 v[188:191], a[220:223], v[24:27], v[188:191]\n"
    "v_add_f32 v109, v109, v35\n"
    "v_cvt_pk_bf16_f32 v33, v34, v35\n"
    "v_mfma_f32_16x16x32_bf16 v[184:187], a[220:223], v[16:19], v[184:187]\n"
    "v_fma_f32 v36, v36, s39, -v101\n"
    "v_fma_f32 v37, v37, s39, -v101\n"
    "v_mfma_f32_16x16x32_bf16 v[180:183], a[220:223], v[8:11], v[180:183]\n"
    "v_fma_f32 v38, v38, s39, -v101\n"
    "v_fma_f32 v39, v39, s39, -v101\n"
    "v_mfma_f32_16x16x32_bf16 v[176:179], a[220:223], v[0:3], v[176:179]\n"
    "ds_read_b64_tr_b16 a[220:221], v98 offset:2560\n"
    "s_waitcnt lgkmcnt(11)\n"
    "v_mfma_f32_16x16x32_bf16 v[192:195], a[240:243], v[0:3], v[192:195]\n"
    "ds_read_b64_tr_b16 a[222:223], v98 offset:10752\n"
    "v_fma_f32 v40, v40, s39, -v102\n"
    "v_mfma_f32_16x16x32_bf16 v[196:199], a[240:243], v[8:11], v[196:199]\n"
    "v_exp_f32 v36, v36\n"
    "v_mfma_f32_16x16x32_bf16 v[200:203], a[240:243], v[16:19], v[200:203]\n"
    "v_exp_f32 v37, v37\n"
    "v_mfma_f32_16x16x32_bf16 v[204:207], a[240:243], v[24:27], v[204:207]\n"
    "ds_read_b64_tr_b16 a[240:241], v97 offset:3072\n"
    "ds_read_b64_tr_b16 a[242:243], v97 offset:11264\n"
    "v_mfma_f32_16x16x32_bf16 v[220:223], a[244:247], v[24:27], v[220:223]\n"
    "v_exp_f32 v38, v38\n"
    "v_mfma_f32_16x16x32_bf16 v[216:219], a[244:247], v[16:19], v[216:219]\n"
    "v_cvt_pk_bf16_f32 v34, v36, v37\n"
    "v_add_f32 v36, v109, v36\n"
    "v_mfma_f32_16x16x32_bf16 v[212:215], a[244:247], v[8:11], v[212:215]\n"
    "v_exp_f32 v39, v39\n"
    "v_mfma_f32_16x16x32_bf16 v[208:211], a[244:247], v[0:3], v[208:211]\n"
    "ds_read_b64_tr_b16 a[244:245], v98 offset:3072\n"
    "s_waitcnt lgkmcnt(11)\n"
    "v_mfma_f32_16x16x32_bf16 v[224:227], a[248:251], v[0:3], v[224:227]\n"
    "ds_read_b64_tr_b16 a[246:247], v98 offset:11264\n"
    "v_add_f32 v36, v36, v37\n"
    "v_mfma_f32_16x16x32_bf16 v[228:231], a[248:251], v[8:11], v[228:231]\n"
    "v_add_f32 v36, v36, v38\n"
    "v_cvt_pk_bf16_f32 v35, v38, v39\n"
    "v_mfma_f32_16x16x32_bf16 v[232:235], a[248:251], v[16:19], v[232:235]\n"
    "v_add_f32 v36, v36, v39\n"
    "v_fma_f32 v41, v41, s39, -v102\n"
    "v_mfma_f32_16x16x32_bf16 v[236:239], a[248:251], v[24:27], v[236:239]\n"
    "ds_read_b64_tr_b16 a[248:249], v97 offset:3584\n"
    "ds_read_b64_tr_b16 a[250:251], v97 offset:11776\n"
    "v_mfma_f32_16x16x32_bf16 v[252:255], a[252:255], v[24:27], v[252:255]\n"
    "v_fma_f32 v42, v42, s39, -v102\n"
    "v_fma_f32 v43, v43, s39, -v102\n"
    "v_mfma_f32_16x16x32_bf16 v[248:251], a[252:255], v[16:19], v[248:251]\n"
    "v_exp_f32 v40, v40\n"
    "v_mfma_f32_16x16x32_bf16 v[244:247], a[252:255], v[8:11], v[244:247]\n"
    "v_exp_f32 v41, v41\n"
    "v_mfma_f32_16x16x32_bf16 v[240:243], a[252:255], v[0:3], v[240:243]\n"
    "ds_read_b64_tr_b16 a[252:253], v98 offset:3584\n"
    "s_waitcnt lgkmcnt(11)\n"
    "v_mfma_f32_16x16x32_bf16 a[128:131], a[208:211], v[0:3], a[128:131]\n"
    "ds_read_b64_tr_b16 a[254:255], v98 offset:11776\n"
    "v_add_f32 v109, v40, v41\n"
    "v_mfma_f32_16x16x32_bf16 a[132:135], a[208:211], v[8:11], a[132:135]\n"
    "v_exp_f32 v42, v42\n"
    "v_mfma_f32_16x16x32_bf16 a[136:139], a[208:211], v[16:19], a[136:139]\n"
    "v_exp_f32 v43, v43\n"
    "v_mfma_f32_16x16x32_bf16 a[140:143], a[208:211], v[24:27], a[140:143]\n"
    "ds_read_b128 a[208:211], v96 offset:0\n"
    "v_cvt_pk_bf16_f32 v40, v40, v41\n"
    "v_mfma_f32_16x16x32_bf16 a[156:159], a[212:215], v[24:27], a[156:159]\n"
    "v_add_f32 v109, v109, v42\n"
    "v_add_f32 v109, v109, v43\n"
    "v_mfma_f32_16x16x32_bf16 a[152:155], a[212:215], v[16:19], a[152:155]\n"
    "v_cvt_pk_bf16_f32 v41, v42, v43\n"
    "v_fma_f32 v44, v44, s39, -v102\n"
    "v_mfma_f32_16x16x32_bf16 a[148:151], a[212:215], v[8:11], a[148:151]\n"
    "v_fma_f32 v45, v45, s39, -v102\n"
    "v_fma_f32 v46, v46, s39, -v102\n"
    "v_mfma_f32_16x16x32_bf16 a[144:147], a[212:215], v[0:3], a[144:147]\n"
    "ds_read_b128 a[212:215], v96 offset:512\n"
    "s_waitcnt lgkmcnt(10)\n"
    "v_mfma_f32_16x16x32_bf16 a[160:163], a[216:219], v[0:3], a[160:163]\n"
    "v_fma_f32 v47, v47, s39, -v102\n"
    "v_mfma_f32_16x16x32_bf16 a[164:167], a[216:219], v[8:11], a[164:167]\n"
    "v_exp_f32 v44, v44\n"
    "v_mfma_f32_16x16x32_bf16 a[168:171], a[216:219], v[16:19], a[168:171]\n"
    "v_exp_f32 v45, v45\n"
    "v_mfma_f32_16x16x32_bf16 a[172:175], a[216:219], v[24:27], a[172:175]\n"
    "ds_read_b128 a[216:219], v96 offset:8192\n"
    "v_cvt_pk_bf16_f32 v42, v44, v45\n"
    "v_mfma_f32_16x16x32_bf16 a[188:191], a[220:223], v[24:27], a[188:191]\n"
    "v_exp_f32 v46, v46\n"
    "v_mfma_f32_16x16x32_bf16 a[184:187], a[220:223], v[16:19], a[184:187]\n"
    "v_add_f32 v44, v109, v44\n"
    "v_add_f32 v44, v44, v45\n"
    "v_mfma_f32_16x16x32_bf16 a[180:183], a[220:223], v[8:11], a[180:183]\n"
    "v_exp_f32 v47, v47\n"
    "v_mfma_f32_16x16x32_bf16 a[176:179], a[220:223], v[0:3], a[176:179]\n"
    "ds_read_b128 a[220:223], v96 offset:8704\n"
    "s_waitcnt lgkmcnt(8)\n"
    "v_mfma_f32_16x16x32_bf16 a[192:195], a[240:243], v[0:3], a[192:195]\n"
    "v_add_f32 v44, v44, v46\n"
    "v_cvt_pk_bf16_f32 v43, v46, v47\n"
    "v_mfma_f32_16x16x32_bf16 a[196:199], a[240:243], v[8:11], a[196:199]\n"
    "v_add_f32 v44, v44, v47\n"
    "v_max_f32 v109, v36, v44\n"
    "v_mfma_f32_16x16x32_bf16 a[200:203], a[240:243], v[16:19], a[200:203]\n"
    "v_cmp_le_f32 vcc, 1.0, v109\n"
    "v_mfma_f32_16x16x32_bf16 a[204:207], a[240:243], v[24:27], a[204:207]\n"
    "ds_read_b128 a[240:243], v96 offset:1024\n"
    "v_mfma_f32_16x16x32_bf16 v[76:79], a[244:247], v[24:27], v[76:79]\n"
    "v_mfma_f32_16x16x32_bf16 v[72:75], a[244:247], v[16:19], v[72:75]\n"
    "v_mfma_f32_16x16x32_bf16 v[68:71], a[244:247], v[8:11], v[68:71]\n"
    "v_mfma_f32_16x16x32_bf16 v[64:67], a[244:247], v[0:3], v[64:67]\n"
    "ds_read_b128 a[244:247], v96 offset:1536\n"
    "s_waitcnt lgkmcnt(6)\n"
    "v_mfma_f32_16x16x32_bf16 a[224:227], a[248:251], v[0:3], a[224:227]\n"
    "s_waitcnt vmcnt(4) lgkmcnt(0)\n"
    "s_barrier\n"
    "v_mfma_f32_16x16x32_bf16 a[228:231], a[248:251], v[8:11], a[228:231]\n"
    "s_add_u32 m0, s38, 49152\n"
    "s_mov_b32 s77, 540672\n"
    "v_mfma_f32_16x16x32_bf16 a[232:235], a[248:251], v[16:19], a[232:235]\n"
    "buffer_load_dwordx4 v100, s[20:23], s77 offen offset:0 nt lds\n"
    "v_mfma_f32_16x16x32_bf16 a[236:239], a[248:251], v[24:27], a[236:239]\n"
    "ds_read_b128 a[248:251], v96 offset:9216\n"
    "v_mfma_f32_16x16x32_bf16 v[92:95], a[252:255], v[24:27], v[92:95]\n"
    "s_add_u32 m0, s38, 50048\n"
    "v_mfma_f32_16x16x32_bf16 v[88:91], a[252:255], v[16:19], v[88:91]\n"
    "buffer_load_dwordx4 v100, s[20:23], s77 offen offset:128 nt lds\n"
    "v_mfma_f32_16x16x32_bf16 v[84:87], a[252:255], v[8:11], v[84:87]\n"
    "v_mfma_f32_16x16x32_bf16 v[80:83], a[252:255], v[0:3], v[80:83]\n"
    "s_add_u32 m0, s38, 50944\n"
    "v_mfma_f32_16x16x32_bf16 v[0:3], a[208:211], a[0:3], 0\n"
    "buffer_load_dwordx4 v100, s[20:23], s77 offen offset:256 nt lds\n"
    "ds_read_b128 a[252:255], v96 offset:9728\n"
    "v_mfma_f32_16x16x32_bf16 v[0:3], a[212:215], a[4:7], v[0:3]\n"
    "s_cbranch_vccnz L_ra10\n"
    "L_ra10_ret:\n"
    "v_add_f32 v105, v105, v36\n"
    "v_mfma_f32_16x16x32_bf16 v[8:11], a[208:211], a[32:35], 0\n"
    "s_add_u32 m0, s38, 51840\n"
    "ds_read_b128 a[208:211], v96 offset:2048\n"
    "v_mfma_f32_16x16x32_bf16 v[8:11], a[212:215], a[36:39], v[8:11]\n"
    "buffer_load_dwordx4 v100, s[20:23], s77 offen offset:384 nt lds\n"
    "v_mfma_f32_16x16x32_bf16 v[4:7], a[216:219], a[0:3], 0\n"
    "ds_read_b128 a[212:215], v96 offset:2560\n"
    "v_add_f32 v106, v106, v44\n"
    "v_mfma_f32_16x16x32_bf16 v[4:7], a[220:223], a[4:7], v[4:7]\n"
    "v_mfma_f32_16x16x32_bf16 v[12:15], a[216:219], a[32:35], 0\n"
    "ds_read_b128 a[216:219], v96 offset:10240\n"
    "v_mfma_f32_16x16x32_bf16 v[12:15], a[220:223], a[36:39], v[12:15]\n"
    "ds_read_b128 a[220:223], v96 offset:10752\n"
    "v_mfma_f32_16x16x32_bf16 v[0:3], a[240:243], a[8:11], v[0:3]\n"
    "v_mfma_f32_16x16x32_bf16 v[0:3], a[244:247], a[12:15], v[0:3]\n"
    "v_mfma_f32_16x16x32_bf16 v[8:11], a[240:243], a[40:43], v[8:11]\n"
    "ds_read_b128 a[240:243], v96 offset:3072\n"
    "v_mfma_f32_16x16x32_bf16 v[8:11], a[244:247], a[44:47], v[8:11]\n"
    "ds_read_b128 a[244:247], v96 offset:3584\n"
    "s_waitcnt lgkmcnt(6)\n"
    "v_mfma_f32_16x16x32_bf16 v[4:7], a[248:251], a[8:11], v[4:7]\n"
    "v_mfma_f32_16x16x32_bf16 v[4:7], a[252:255], a[12:15], v[4:7]\n"
    "v_mfma_f32_16x16x32_bf16 v[12:15], a[248:251], a[40:43], v[12:15]\n"
    "ds_read_b128 a[248:251], v96 offset:11264\n"
    "v_mfma_f32_16x16x32_bf16 v[12:15], a[252:255], a[44:47], v[12:15]\n"
    "ds_read_b128 a[252:255], v96 offset:11776\n"
    "s_waitcnt lgkmcnt(6)\n"
    "v_mfma_f32_16x16x32_bf16 v[0:3], a[208:211], a[16:19], v[0:3]\n"
    "v_mfma_f32_16x16x32_bf16 v[0:3], a[212:215], a[20:23], v[0:3]\n"
    "v_mfma_f32_16x16x32_bf16 v[8:11], a[208:211], a[48:51], v[8:11]\n"
    "ds_read_b64_tr_b16 a[208:209], v97 offset:16384\n"
    "ds_read_b64_tr_b16 a[210:211], v97 offset:24576\n"
    "v_mfma_f32_16x16x32_bf16 v[8:11], a[212:215], a[52:55], v[8:11]\n"
    "ds_read_b64_tr_b16 a[212:213], v98 offset:16384\n"
    "s_waitcnt lgkmcnt(7)\n"
    "v_mfma_f32_16x16x32_bf16 v[4:7], a[216:219], a[16:19], v[4:7]\n"
    "ds_read_b64_tr_b16 a[214:215], v98 offset:24576\n"
    "v_mfma_f32_16x16x32_bf16 v[4:7], a[220:223], a[20:23], v[4:7]\n"
    "v_mfma_f32_16x16x32_bf16 v[12:15], a[216:219], a[48:51], v[12:15]\n"
    "ds_read_b64_tr_b16 a[216:217], v97 offset:16896\n"
    "ds_read_b64_tr_b16 a[218:219], v97 offset:25088\n"
    "v_mfma_f32_16x16x32_bf16 v[12:15], a[220:223], a[52:55], v[12:15]\n"
    "ds_read_b64_tr_b16 a[220:221], v98 offset:16896\n"
    "s_waitcnt lgkmcnt(9)\n"
    "v_mfma_f32_16x16x32_bf16 v[0:3], a[240:243], a[24:27], v[0:3]\n"
    "ds_read_b64_tr_b16 a[222:223], v98 offset:25088\n"
    "v_mfma_f32_16x16x32_bf16 v[0:3], a[244:247], a[28:31], v[0:3]\n"
    "v_mfma_f32_16x16x32_bf16 v[8:11], a[240:243], a[56:59], v[8:11]\n"
    "ds_read_b64_tr_b16 a[240:241], v97 offset:17408\n"
    "ds_read_b64_tr_b16 a[242:243], v97 offset:25600\n"
    "v_mfma_f32_16x16x32_bf16 v[8:11], a[244:247], a[60:63], v[8:11]\n"
    "ds_read_b64_tr_b16 a[244:245], v98 offset:17408\n"
    "s_waitcnt lgkmcnt(11)\n"
    "v_mfma_f32_16x16x32_bf16 v[4:7], a[248:251], a[24:27], v[4:7]\n"
    "ds_read_b64_tr_b16 a[246:247], v98 offset:25600\n"
    "v_mfma_f32_16x16x32_bf16 v[4:7], a[252:255], a[28:31], v[4:7]\n"
    "v_fma_f32 v0, v0, s39, -v101\n"
    "v_fma_f32 v1, v1, s39, -v101\n"
    "v_fma_f32 v2, v2, s39, -v101\n"
    "v_mfma_f32_16x16x32_bf16 v[12:15], a[248:251], a[56:59], v[12:15]\n"
    "ds_read_b64_tr_b16 a[248:249], v97 offset:17920\n"
    "ds_read_b64_tr_b16 a[250:251], v97 offset:26112\n"
    "v_mfma_f32_16x16x32_bf16 v[12:15], a[252:255], a[60:63], v[12:15]\n"
    "ds_read_b64_tr_b16 a[252:253], v98 offset:17920\n"
    "s_waitcnt lgkmcnt(11)\n"
    "v_mfma_f32_16x16x32_bf16 v[128:131], a[208:211], v[32:35], v[128:131]\n"
    "ds_read_b64_tr_b16 a[254:255], v98 offset:26112\n"
    "v_fma_f32 v3, v3, s39, -v101\n"
    "v_fma_f32 v8, v8, s39, -v102\n"
    "v_mfma_f32_16x16x32_bf16 v[132:135], a[208:211], v[40:43], v[132:135]\n"
    "ds_read_b64_tr_b16 a[208:209], v97 offset:18432\n"
    "ds_read_b64_tr_b16 a[210:211], v97 offset:26624\n"
    "v_mfma_f32_16x16x32_bf16 v[148:151], a[212:215], v[40:43], v[148:151]\n"
    "v_exp_f32 v0, v0\n"
    "v_mfma_f32_16x16x32_bf16 v[144:147], a[212:215], v[32:35], v[144:147]\n"
    "ds_read_b64_tr_b16 a[212:213], v98 offset:18432\n"
    "ds_read_b64_tr_b16 a[214:215], v98 offset:26624\n"
    "s_waitcnt lgkmcnt(12)\n"
    "v_mfma_f32_16x16x32_bf16 v[160:163], a[216:219], v[32:35], v[160:163]\n"
    "v_exp_f32 v1, v1\n"
    "v_mfma_f32_16x16x32_bf16 v[164:167], a[216:219], v[40:43], v[164:167]\n"
    "ds_read_b64_tr_b16 a[216:217], v97 offset:18944\n"
    "ds_read_b64_tr_b16 a[218:219], v97 offset:27136\n"
    "v_mfma_f32_16x16x32_bf16 v[180:183], a[220:223], v[40:43], v[180:183]\n"
    "v_exp_f32 v2, v2\n"
    "v_add_f32 v109, v0, v1\n"
    "v_mfma_f32_16x16x32_bf16 v[176:179], a[220:223], v[32:35], v[176:179]\n"
    "ds_read_b64_tr_b16 a[220:221], v98 offset:18944\n"
    "s_waitcnt lgkmcnt(11)\n"
    "v_mfma_f32_16x16x32_bf16 v[192:195], a[240:243], v[32:35], v[192:195]\n"
    "ds_read_b64_tr_b16 a[222:223], v98 offset:27136\n"
    "v_cvt_pk_bf16_f32 v0, v0, v1\n"
    "v_mfma_f32_16x16x32_bf16 v[196:199], a[240:243], v[40:43], v[196:199]\n"
    "ds_read_b64_tr_b16 a[240:241], v97 offset:19456\n"
    "ds_read_b64_tr_b16 a[242:243], v97 offset:27648\n"
    "v_mfma_f32_16x16x32_bf16 v[212:215], a[244:247], v[40:43], v[212:215]\n"
    "v_exp_f32 v3, v3\n"
    "v_add_f32 v109, v109, v2\n"
    "v_mfma_f32_16x16x32_bf16 v[208:211], a[244:247], v[32:35], v[208:211]\n"
    "ds_read_b64_tr_b16 a[244:245], v98 offset:19456\n"
    "s_waitcnt lgkmcnt(11)\n"
    "v_mfma_f32_16x16x32_bf16 v[224:227], a[248:251], v[32:35], v[224:227]\n"
    "ds_read_b64_tr_b16 a[246:247], v98 offset:27648\n"
    "v_add_f32 v109, v109, v3\n"
    "v_mfma_f32_16x16x32_bf16 v[228:231], a[248:251], v[40:43], v[228:231]\n"
    "ds_read_b64_tr_b16 a[248:249], v97 offset:19968\n"
    "ds_read_b64_tr_b16 a[250:251], v97 offset:28160\n"
    "v_cvt_pk_bf16_f32 v1, v2, v3\n"
    "v_mfma_f32_16x16x32_bf16 v[244:247], a[252:255], v[40:43], v[244:247]\n"
    "v_fma_f32 v4, v4, s39, -v101\n"
    "v_fma_f32 v5, v5, s39, -v101\n"
    "v_mfma_f32_16x16x32_bf16 v[240:243], a[252:255], v[32:35], v[240:243]\n"
    "ds_read_b64_tr_b16 a[252:253], v98 offset:19968\n"
    "s_waitcnt lgkmcnt(11)\n"
    "v_mfma_f32_16x16x32_bf16 a[128:131], a[208:211], v[32:35], a[128:131]\n"
    "ds_read_b64_tr_b16 a[254:255], v98 offset:28160\n"
    "v_fma_f32 v6, v6, s39, -v101\n"
    "v_fma_f32 v7, v7, s39, -v101\n"
    "v_mfma_f32_16x16x32_bf16 a[132:135], a[208:211], v[40:43], a[132:135]\n"
    "ds_read_b128 a[208:211], v96 offset:16384\n"
    "v_fma_f32 v9, v9, s39, -v102\n"
    "v_mfma_f32_16x16x32_bf16 a[148:151], a[212:215], v[40:43], a[148:151]\n"
    "v_exp_f32 v4, v4\n"
    "v_mfma_f32_16x16x32_bf16 a[144:147], a[212:215], v[32:35], a[144:147]\n"
    "ds_read_b128 a[212:215], v96 offset:16896\n"
    "s_waitcnt lgkmcnt(10)\n"
    "v_mfma_f32_16x16x32_bf16 a[160:163], a[216:219], v[32:35], a[160:163]\n"
    "v_exp_f32 v5, v5\n"
    "v_fma_f32 v10, v10, s39, -v102\n"
    "v_mfma_f32_16x16x32_bf16 a[164:167], a[216:219], v[40:43], a[164:167]\n"
    "ds_read_b128 a[216:219], v96 offset:24576\n"
    "v_cvt_pk_bf16_f32 v2, v4, v5\n"
    "v_mfma_f32_16x16x32_bf16 a[180:183], a[220:223], v[40:43], a[180:183]\n"
    "v_exp_f32 v6, v6\n"
    "v_mfma_f32_16x16x32_bf16 a[176:179], a[220:223], v[32:35], a[176:179]\n"
    "ds_read_b128 a[220:223], v96 offset:25088\n"
    "v_add_f32 v4, v109, v4\n"
    "s_waitcnt lgkmcnt(8)\n"
    "v_mfma_f32_16x16x32_bf16 a[192:195], a[240:243], v[32:35], a[192:195]\n"
    "v_exp_f32 v7, v7\n"
    "v_mfma_f32_16x16x32_bf16 a[196:199], a[240:243], v[40:43], a[196:199]\n"
    "ds_read_b128 a[240:243], v96 offset:17408\n"
    "v_add_f32 v4, v4, v5\n"
    "v_mfma_f32_16x16x32_bf16 v[68:71], a[244:247], v[40:43], v[68:71]\n"
    "v_add_f32 v4, v4, v6\n"
    "v_cvt_pk_bf16_f32 v3, v6, v7\n"
    "v_add_f32 v4, v4, v7\n"
    "v_mfma_f32_16x16x32_bf16 v[64:67], a[244:247], v[32:35], v[64:67]\n"
    "ds_read_b128 a[244:247], v96 offset:17920\n"
    "s_waitcnt lgkmcnt(6)\n"
    "v_mfma_f32_16x16x32_bf16 a[224:227], a[248:251], v[32:35], a[224:227]\n"
    "v_fma_f32 v11, v11, s39, -v102\n"
    "v_fma_f32 v12, v12, s39, -v102\n"
    "v_mfma_f32_16x16x32_bf16 a[228:231], a[248:251], v[40:43], a[228:231]\n"
    "ds_read_b128 a[248:251], v96 offset:25600\n"
    "v_exp_f32 v8, v8\n"
    "v_mfma_f32_16x16x32_bf16 v[84:87], a[252:255], v[40:43], v[84:87]\n"
    "v_exp_f32 v9, v9\n"
    "v_mfma_f32_16x16x32_bf16 v[80:83], a[252:255], v[32:35], v[80:83]\n"
    "ds_read_b128 a[252:255], v96 offset:26112\n"
    "s_waitcnt lgkmcnt(6)\n"
    "v_mfma_f32_16x16x32_bf16 v[32:35], a[208:211], a[0:3], 0\n"
    "v_exp_f32 v10, v10\n"
    "v_mfma_f32_16x16x32_bf16 v[32:35], a[212:215], a[4:7], v[32:35]\n"
    "v_exp_f32 v11, v11\n"
    "v_add_f32 v109, v8, v9\n"
    "v_mfma_f32_16x16x32_bf16 v[40:43], a[208:211], a[32:35], 0\n"
    "ds_read_b128 a[208:211], v96 offset:18432\n"
    "v_cvt_pk_bf16_f32 v8, v8, v9\n"
    "v_mfma_f32_16x16x32_bf16 v[40:43], a[212:215], a[36:39], v[40:43]\n"
    "ds_read_b128 a[212:215], v96 offset:18944\n"
    "s_waitcnt lgkmcnt(6)\n"
    "v_mfma_f32_16x16x32_bf16 v[36:39], a[216:219], a[0:3], 0\n"
    "v_add_f32 v109, v109, v10\n"
    "v_add_f32 v109, v109, v11\n"
    "v_cvt_pk_bf16_f32 v9, v10, v11\n"
    "v_mfma_f32_16x16x32_bf16 v[36:39], a[220:223], a[4:7], v[36:39]\n"
    "v_fma_f32 v13, v13, s39, -v102\n"
    "v_fma_f32 v14, v14, s39, -v102\n"
    "v_mfma_f32_16x16x32_bf16 v[44:47], a[216:219], a[32:35], 0\n"
    "ds_read_b128 a[216:219], v96 offset:26624\n"
    "v_fma_f32 v15, v15, s39, -v102\n"
    "v_mfma_f32_16x16x32_bf16 v[44:47], a[220:223], a[36:39], v[44:47]\n"
    "ds_read_b128 a[220:223], v96 offset:27136\n"
    "s_waitcnt lgkmcnt(6)\n"
    "v_mfma_f32_16x16x32_bf16 v[32:35], a[240:243], a[8:11], v[32:35]\n"
    "v_exp_f32 v12, v12\n"
    "v_mfma_f32_16x16x32_bf16 v[32:35], a[244:247], a[12:15], v[32:35]\n"
    "v_exp_f32 v13, v13\n"
    "s_nop 0\n"
    "v_cvt_pk_bf16_f32 v10, v12, v13\n"
    "v_mfma_f32_16x16x32_bf16 v[40:43], a[240:243], a[40:43], v[40:43]\n"
    "ds_read_b128 a[240:243], v96 offset:19456\n"
    "v_add_f32 v12, v109, v12\n"
    "v_mfma_f32_16x16x32_bf16 v[40:43], a[244:247], a[44:47], v[40:43]\n"
    "ds_read_b128 a[244:247], v96 offset:19968\n"
    "v_add_f32 v12, v12, v13\n"
    "s_waitcnt lgkmcnt(6)\n"
    "v_mfma_f32_16x16x32_bf16 v[36:39], a[248:251], a[8:11], v[36:39]\n"
    "s_xor_b32 s92, s87, 262144\n"
    "s_xor_b32 s93, s87, 266240\n"
    "s_xor_b32 s94, s87, 270336\n"
    "s_xor_b32 s95, s87, 274432\n"
    "s_xor_b32 s96, s87, 278528\n"
    "s_xor_b32 s97, s87, 282624\n"
    "s_xor_b32 s98, s87, 286720\n"
    "s_xor_b32 s99, s87, 290816\n"
    "v_mfma_f32_16x16x32_bf16 v[36:39], a[252:255], a[12:15], v[36:39]\n"
    "v_exp_f32 v14, v14\n"
    "v_mfma_f32_16x16x32_bf16 v[44:47], a[248:251], a[40:43], v[44:47]\n"
    "ds_read_b128 a[248:251], v96 offset:27648\n"
    "v_add_f32 v12, v12, v14\n"
    "v_mfma_f32_16x16x32_bf16 v[44:47], a[252:255], a[44:47], v[44:47]\n"
    "ds_read_b128 a[252:255], v96 offset:28160\n"
    "s_waitcnt lgkmcnt(6)\n"
    "v_mfma_f32_16x16x32_bf16 v[32:35], a[208:211], a[16:19], v[32:35]\n"
    "v_exp_f32 v15, v15\n"
    "v_mfma_f32_16x16x32_bf16 v[32:35], a[212:215], a[20:23], v[32:35]\n"
    "v_cvt_pk_bf16_f32 v11, v14, v15\n"
    "v_add_f32 v12, v12, v15\n"
    "v_mfma_f32_16x16x32_bf16 v[40:43], a[208:211], a[48:51], v[40:43]\n"
    "s_waitcnt vmcnt(4) lgkmcnt(0)\n"
    "s_barrier\n"
    "v_mfma_f32_16x16x32_bf16 v[40:43], a[212:215], a[52:55], v[40:43]\n"
    "ds_read_b64_tr_b16 a[208:209], v97 offset:32768\n"
    "v_mfma_f32_16x16x32_bf16 v[36:39], a[216:219], a[16:19], v[36:39]\n"
    "ds_read_b64_tr_b16 a[210:211], v97 offset:40960\n"
    "ds_read_b64_tr_b16 a[212:213], v98 offset:32768\n"
    "v_mfma_f32_16x16x32_bf16 v[36:39], a[220:223], a[20:23], v[36:39]\n"
    "ds_read_b64_tr_b16 a[214:215], v98 offset:40960\n"
    "v_max_f32 v109, v4, v12\n"
    "v_mfma_f32_16x16x32_bf16 v[44:47], a[216:219], a[48:51], v[44:47]\n"
    "ds_read_b64_tr_b16 a[216:217], v97 offset:33280\n"
    "v_cmp_le_f32 vcc, 1.0, v109\n"
    "v_mfma_f32_16x16x32_bf16 v[44:47], a[220:223], a[52:55], v[44:47]\n"
    "ds_read_b64_tr_b16 a[218:219], v97 offset:41472\n"
    "v_mfma_f32_16x16x32_bf16 v[32:35], a[240:243], a[24:27], v[32:35]\n"
    "ds_read_b64_tr_b16 a[220:221], v98 offset:33280\n"
    "s_cbranch_vccnz L_ra11\n"
    "L_ra11_ret:\n"
    "v_mfma_f32_16x16x32_bf16 v[32:35], a[244:247], a[28:31], v[32:35]\n"
    "ds_read_b64_tr_b16 a[222:223], v98 offset:41472\n"
    "v_add_f32 v105, v105, v4\n"
    "v_mfma_f32_16x16x32_bf16 v[40:43], a[240:243], a[56:59], v[40:43]\n"
    "ds_read_b64_tr_b16 a[240:241], v97 offset:33792\n"
    "ds_read_b64_tr_b16 a[242:243], v97 offset:41984\n"
    "v_mfma_f32_16x16x32_bf16 v[40:43], a[244:247], a[60:63], v[40:43]\n"
    "ds_read_b64_tr_b16 a[244:245], v98 offset:33792\n"
    "v_mfma_f32_16x16x32_bf16 v[36:39], a[248:251], a[24:27], v[36:39]\n"
    "ds_read_b64_tr_b16 a[246:247], v98 offset:41984\n"
    "v_add_f32 v106, v106, v12\n"
    "v_mfma_f32_16x16x32_bf16 v[36:39], a[252:255], a[28:31], v[36:39]\n"
    "v_fma_f32 v32, v32, s39, -v101\n"
    "v_fma_f32 v33, v33, s39, -v101\n"
    "v_mfma_f32_16x16x32_bf16 v[44:47], a[248:251], a[56:59], v[44:47]\n"
    "ds_read_b64_tr_b16 a[248:249], v97 offset:34304\n"
    "ds_read_b64_tr_b16 a[250:251], v97 offset:42496\n"
    "v_mfma_f32_16x16x32_bf16 v[44:47], a[252:255], a[60:63], v[44:47]\n"
    "ds_read_b64_tr_b16 a[252:253], v98 offset:34304\n"
    "ds_read_b64_tr_b16 a[254:255], v98 offset:42496\n"
    "s_waitcnt lgkmcnt(12)\n"
    "v_mfma_f32_16x16x32_bf16 v[128:131], a[208:211], v[0:3], v[128:131]\n"
    "v_fma_f32 v34, v34, s39, -v101\n"
    "v_fma_f32 v35, v35, s39, -v101\n"
    "v_mfma_f32_16x16x32_bf16 v[132:135], a[208:211], v[8:11], v[132:135]\n"
    "ds_read_b64_tr_b16 a[208:209], v97 offset:34816\n"
    "ds_read_b64_tr_b16 a[210:211], v97 offset:43008\n"
    "v_fma_f32 v36, v36, s39, -v101\n"
    "v_mfma_f32_16x16x32_bf16 v[148:151], a[212:215], v[8:11], v[148:151]\n"
    "v_exp_f32 v32, v32\n"
    "v_mfma_f32_16x16x32_bf16 v[144:147], a[212:215], v[0:3], v[144:147]\n"
    "ds_read_b64_tr_b16 a[212:213], v98 offset:34816\n"
    "ds_read_b64_tr_b16 a[214:215], v98 offset:43008\n"
    "s_waitcnt lgkmcnt(12)\n"
    "v_mfma_f32_16x16x32_bf16 v[160:163], a[216:219], v[0:3], v[160:163]\n"
    "v_exp_f32 v33, v33\n"
    "v_fma_f32 v37, v37, s39, -v101\n"
    "v_mfma_f32_16x16x32_bf16 v[164:167], a[216:219], v[8:11], v[164:167]\n"
    "ds_read_b64_tr_b16 a[216:217], v97 offset:35328\n"
    "ds_read_b64_tr_b16 a[218:219], v97 offset:43520\n"
    "v_mfma_f32_16x16x32_bf16 v[180:183], a[220:223], v[8:11], v[180:183]\n"
    "v_exp_f32 v34, v34\n"
    "v_add_f32 v109, v32, v33\n"
    "v_mfma_f32_16x16x32_bf16 v[176:179], a[220:223], v[0:3], v[176:179]\n"
    "ds_read_b64_tr_b16 a[220:221], v98 offset:35328\n"
    "s_waitcnt lgkmcnt(11)\n"
    "v_mfma_f32_16x16x32_bf16 v[192:195], a[240:243], v[0:3], v[192:195]\n"
    "ds_read_b64_tr_b16 a[222:223], v98 offset:43520\n"
    "v_exp_f32 v35, v35\n"
    "v_mfma_f32_16x16x32_bf16 v[196:199], a[240:243], v[8:11], v[196:199]\n"
    "ds_read_b64_tr_b16 a[240:241], v97 offset:35840\n"
    "ds_read_b64_tr_b16 a[242:243], v97 offset:44032\n"
    "v_mfma_f32_16x16x32_bf16 v[212:215], a[244:247], v[8:11], v[212:215]\n"
    "v_cvt_pk_bf16_f32 v32, v32, v33\n"
    "v_add_f32 v109, v109, v34\n"
    "v_add_f32 v109, v109, v35\n"
    "v_mfma_f32_16x16x32_bf16 v[208:211], a[244:247], v[0:3], v[208:211]\n"
    "ds_read_b64_tr_b16 a[244:245], v98 offset:35840\n"
    "ds_read_b64_tr_b16 a[246:247], v98 offset:44032\n"
    "s_waitcnt lgkmcnt(12)\n"
    "v_mfma_f32_16x16x32_bf16 v[224:227], a[248:251], v[0:3], v[224:227]\n"
    "v_cvt_pk_bf16_f32 v33, v34, v35\n"
    "v_fma_f32 v38, v38, s39, -v101\n"
    "v_mfma_f32_16x16x32_bf16 v[228:231], a[248:251], v[8:11], v[228:231]\n"
    "ds_read_b64_tr_b16 a[248:249], v97 offset:36352\n"
    "ds_read_b64_tr_b16 a[250:251], v97 offset:44544\n"
    "v_fma_f32 v39, v39, s39, -v101\n"
    "v_mfma_f32_16x16x32_bf16 v[244:247], a[252:255], v[8:11], v[244:247]\n"
    "v_exp_f32 v36, v36\n"
    "v_mfma_f32_16x16x32_bf16 v[240:243], a[252:255], v[0:3], v[240:243]\n"
    "ds_read_b64_tr_b16 a[252:253], v98 offset:36352\n"
    "ds_read_b64_tr_b16 a[254:255], v98 offset:44544\n"
    "s_waitcnt lgkmcnt(12)\n"
    "v_mfma_f32_16x16x32_bf16 a[128:131], a[208:211], v[0:3], a[128:131]\n"
    "v_exp_f32 v37, v37\n"
    "v_fma_f32 v40, v40, s39, -v102\n"
    "v_mfma_f32_16x16x32_bf16 a[132:135], a[208:211], v[8:11], a[132:135]\n"
    "ds_read_b128 a[208:211], v96 offset:32768\n"
    "v_cvt_pk_bf16_f32 v34, v36, v37\n"
    "v_mfma_f32_16x16x32_bf16 a[148:151], a[212:215], v[8:11], a[148:151]\n"
    "v_exp_f32 v38, v38\n"
    "v_add_f32 v36, v109, v36\n"
    "v_mfma_f32_16x16x32_bf16 a[144:147], a[212:215], v[0:3], a[144:147]\n"
    "ds_read_b128 a[212:215], v96 offset:33280\n"
    "s_waitcnt lgkmcnt(10)\n"
    "v_mfma_f32_16x16x32_bf16 a[160:163], a[216:219], v[0:3], a[160:163]\n"
    "v_exp_f32 v39, v39\n"
    "v_add_f32 v36, v36, v37\n"
    "v_mfma_f32_16x16x32_bf16 a[164:167], a[216:219], v[8:11], a[164:167]\n"
    "ds_read_b128 a[216:219], v96 offset:40960\n"
    "v_add_f32 v36, v36, v38\n"
    "v_cvt_pk_bf16_f32 v35, v38, v39\n"
    "v_mfma_f32_16x16x32_bf16 a[180:183], a[220:223], v[8:11], a[180:183]\n"
    "v_add_f32 v36, v36, v39\n"
    "v_fma_f32 v41, v41, s39, -v102\n"
    "v_mfma_f32_16x16x32_bf16 a[176:179], a[220:223], v[0:3], a[176:179]\n"
    "ds_read_b128 a[220:223], v96 offset:41472\n"
    "v_fma_f32 v42, v42, s39, -v102\n"
    "s_waitcnt lgkmcnt(8)\n"
    "v_mfma_f32_16x16x32_bf16 a[192:195], a[240:243], v[0:3], a[192:195]\n"
    "v_fma_f32 v43, v43, s39, -v102\n"
    "v_fma_f32 v44, v44, s39, -v102\n"
    "v_mfma_f32_16x16x32_bf16 a[196:199], a[240:243], v[8:11], a[196:199]\n"
    "ds_read_b128 a[240:243], v96 offset:33792\n"
    "v_exp_f32 v40, v40\n"
    "v_mfma_f32_16x16x32_bf16 v[68:71], a[244:247], v[8:11], v[68:71]\n"
    "v_exp_f32 v41, v41\n"
    "v_mfma_f32_16x16x32_bf16 v[64:67], a[244:247], v[0:3], v[64:67]\n"
    "ds_read_b128 a[244:247], v96 offset:34304\n"
    "v_add_f32 v109, v40, v41\n"
    "s_waitcnt lgkmcnt(6)\n"
    "v_mfma_f32_16x16x32_bf16 a[224:227], a[248:251], v[0:3], a[224:227]\n"
    "v_exp_f32 v42, v42\n"
    "v_cvt_pk_bf16_f32 v40, v40, v41\n"
    "v_mfma_f32_16x16x32_bf16 a[228:231], a[248:251], v[8:11], a[228:231]\n"
    "ds_read_b128 a[248:251], v96 offset:41984\n"
    "v_add_f32 v109, v109, v42\n"
    "v_mfma_f32_16x16x32_bf16 v[84:87], a[252:255], v[8:11], v[84:87]\n"
    "v_exp_f32 v43, v43\n"
    "v_fma_f32 v45, v45, s39, -v102\n"
    "v_mfma_f32_16x16x32_bf16 v[80:83], a[252:255], v[0:3], v[80:83]\n"
    "ds_read_b128 a[252:255], v96 offset:42496\n"
    "s_waitcnt lgkmcnt(6)\n"
    "v_mfma_f32_16x16x32_bf16 v[48:51], a[208:211], a[64:67], 0\n"
    "v_add_f32 v109, v109, v43\n"
    "v_cvt_pk_bf16_f32 v41, v42, v43\n"
    "v_fma_f32 v46, v46, s39, -v102\n"
    "v_mfma_f32_16x16x32_bf16 v[48:51], a[212:215], a[68:71], v[48:51]\n"
    "v_fma_f32 v47, v47, s39, -v102\n"
    "v_exp_f32 v44, v44\n"
    "v_mfma_f32_16x16x32_bf16 v[56:59], a[208:211], a[96:99], 0\n"
    "ds_read_b128 a[208:211], v96 offset:34816\n"
    "v_mfma_f32_16x16x32_bf16 v[56:59], a[212:215], a[100:103], v[56:59]\n"
    "ds_read_b128 a[212:215], v96 offset:35328\n"
    "v_exp_f32 v45, v45\n"
    "s_waitcnt lgkmcnt(6)\n"
    "v_mfma_f32_16x16x32_bf16 v[52:55], a[216:219], a[64:67], 0\n"
    "v_exp_f32 v46, v46\n"
    "v_mfma_f32_16x16x32_bf16 v[52:55], a[220:223], a[68:71], v[52:55]\n"
    "v_cvt_pk_bf16_f32 v42, v44, v45\n"
    "v_add_f32 v44, v109, v44\n"
    "v_add_f32 v44, v44, v45\n"
    "v_mfma_f32_16x16x32_bf16 v[60:63], a[216:219], a[96:99], 0\n"
    "ds_read_b128 a[216:219], v96 offset:43008\n"
    "v_add_f32 v44, v44, v46\n"
    "v_mfma_f32_16x16x32_bf16 v[60:63], a[220:223], a[100:103], v[60:63]\n"
    "ds_read_b128 a[220:223], v96 offset:43520\n"
    "s_waitcnt lgkmcnt(6)\n"
    "v_mfma_f32_16x16x32_bf16 v[48:51], a[240:243], a[72:75], v[48:51]\n"
    "v_exp_f32 v47, v47\n"
    "s_nop 0\n"
    "v_cvt_pk_bf16_f32 v43, v46, v47\n"
    "v_add_f32 v44, v44, v47\n"
    "v_mfma_f32_16x16x32_bf16 v[48:51], a[244:247], a[76:79], v[48:51]\n"
    "v_max_f32 v109, v36, v44\n"
    "v_cmp_le_f32 vcc, 1.0, v109\n"
    "v_mfma_f32_16x16x32_bf16 v[56:59], a[240:243], a[104:107], v[56:59]\n"
    "ds_read_b128 a[240:243], v96 offset:35840\n"
    "s_cbranch_vccnz L_ra12\n"
    "L_ra12_ret:\n"
    "v_add_f32 v105, v105, v36\n"
    "v_mfma_f32_16x16x32_bf16 v[56:59], a[244:247], a[108:111], v[56:59]\n"
    "ds_read_b128 a[244:247], v96 offset:36352\n"
    "s_waitcnt lgkmcnt(6)\n"
    "v_mfma_f32_16x16x32_bf16 v[52:55], a[248:251], a[72:75], v[52:55]\n"
    "v_add_f32 v106, v106, v44\n"
    "v_mov_b32 v122, v105\n"
    "v_mov_b32 v125, v105\n"
    "v_mfma_f32_16x16x32_bf16 v[52:55], a[252:255], a[76:79], v[52:55]\n"
    "s_nop 0\n"
    "v_permlane32_swap_b32 v122, v125\n"
    "v_add_f32 v122, v122, v125\n"
    "v_mov_b32 v125, v122\n"
    "v_mfma_f32_16x16x32_bf16 v[60:63], a[248:251], a[104:107], v[60:63]\n"
    "ds_read_b128 a[248:251], v96 offset:44032\n"
    "v_permlane16_swap_b32 v122, v125\n"
    "v_mfma_f32_16x16x32_bf16 v[60:63], a[252:255], a[108:111], v[60:63]\n"
    "ds_read_b128 a[252:255], v96 offset:44544\n"
    "v_add_f32 v122, v122, v125\n"
    "s_waitcnt lgkmcnt(6)\n"
    "v_mfma_f32_16x16x32_bf16 v[48:51], a[208:211], a[80:83], v[48:51]\n"
    "v_rcp_f32 v122, v122\n"
    "v_mfma_f32_16x16x32_bf16 v[48:51], a[212:215], a[84:87], v[48:51]\n"
    "v_mov_b32 v112, v106\n"
    "v_mov_b32 v125, v106\n"
    "s_nop 1\n"
    "v_permlane32_swap_b32 v112, v125\n"
    "v_mfma_f32_16x16x32_bf16 v[56:59], a[208:211], a[112:115], v[56:59]\n"
    "s_waitcnt vmcnt(0) lgkmcnt(0)\n"
    "s_barrier\n"
    "v_mfma_f32_16x16x32_bf16 v[56:59], a[212:215], a[116:119], v[56:59]\n"
    "ds_read_b64_tr_b16 a[208:209], v97 offset:0\n"
    "ds_read_b64_tr_b16 a[210:211], v97 offset:8192\n"
    "v_mfma_f32_16x16x32_bf16 v[52:55], a[216:219], a[80:83], v[52:55]\n"
    "ds_read_b64_tr_b16 a[212:213], v98 offset:0\n"
    "ds_read_b64_tr_b16 a[214:215], v98 offset:8192\n"
    "v_add_f32 v112, v112, v125\n"
    "v_mfma_f32_16x16x32_bf16 v[52:55], a[220:223], a[84:87], v[52:55]\n"
    "v_mov_b32 v125, v112\n"
    "s_nop 1\n"
    "v_permlane16_swap_b32 v112, v125\n"
    "v_mfma_f32_16x16x32_bf16 v[60:63], a[216:219], a[112:115], v[60:63]\n"
    "ds_read_b64_tr_b16 a[216:217], v97 offset:512\n"
    "ds_read_b64_tr_b16 a[218:219], v97 offset:8704\n"
    "v_add_f32 v112, v112, v125\n"
    "v_mfma_f32_16x16x32_bf16 v[60:63], a[220:223], a[116:119], v[60:63]\n"
    "ds_read_b64_tr_b16 a[220:221], v98 offset:512\n"
    "v_mfma_f32_16x16x32_bf16 v[48:51], a[240:243], a[88:91], v[48:51]\n"
    "ds_read_b64_tr_b16 a[222:223], v98 offset:8704\n"
    "v_rcp_f32 v112, v112\n"
    "v_mfma_f32_16x16x32_bf16 v[48:51], a[244:247], a[92:95], v[48:51]\n"
    "v_mfma_f32_16x16x32_bf16 v[56:59], a[240:243], a[120:123], v[56:59]\n"
    "ds_read_b64_tr_b16 a[240:241], v97 offset:1024\n"
    "ds_read_b64_tr_b16 a[242:243], v97 offset:9216\n"
    "v_mfma_f32_16x16x32_bf16 v[56:59], a[244:247], a[124:127], v[56:59]\n"
    "ds_read_b64_tr_b16 a[244:245], v98 offset:1024\n"
    "ds_read_b64_tr_b16 a[246:247], v98 offset:9216\n"
    "v_mfma_f32_16x16x32_bf16 v[52:55], a[248:251], a[88:91], v[52:55]\n"
    "v_mfma_f32_16x16x32_bf16 v[52:55], a[252:255], a[92:95], v[52:55]\n"
    "v_fma_f32 v48, v48, s39, -v103\n"
    "v_fma_f32 v49, v49, s39, -v103\n"
    "v_fma_f32 v50, v50, s39, -v103\n"
    "v_fma_f32 v51, v51, s39, -v103\n"
    "v_exp_f32 v48, v48\n"
    "v_exp_f32 v49, v49\n"
    "s_nop 0\n"
    "v_add_f32 v109, v48, v49\n"
    "v_mfma_f32_16x16x32_bf16 v[60:63], a[248:251], a[120:123], v[60:63]\n"
    "ds_read_b64_tr_b16 a[248:249], v97 offset:1536\n"
    "ds_read_b64_tr_b16 a[250:251], v97 offset:9728\n"
    "v_exp_f32 v50, v50\n"
    "v_exp_f32 v51, v51\n"
    "v_mfma_f32_16x16x32_bf16 v[60:63], a[252:255], a[124:127], v[60:63]\n"
    "ds_read_b64_tr_b16 a[252:253], v98 offset:1536\n"
    "ds_read_b64_tr_b16 a[254:255], v98 offset:9728\n"
    "v_cvt_pk_bf16_f32 v48, v48, v49\n"
    "v_add_f32 v109, v109, v50\n"
    "s_waitcnt lgkmcnt(12)\n"
    "v_mfma_f32_16x16x32_bf16 v[128:131], a[208:211], v[32:35], v[128:131]\n"
    "v_add_f32 v109, v109, v51\n"
    "v_cvt_pk_bf16_f32 v49, v50, v51\n"
    "v_fma_f32 v56, v56, s39, -v104\n"
    "v_fma_f32 v57, v57, s39, -v104\n"
    "v_fma_f32 v58, v58, s39, -v104\n"
    "v_fma_f32 v59, v59, s39, -v104\n"
    "v_mfma_f32_16x16x32_bf16 v[132:135], a[208:211], v[40:43], v[132:135]\n"
    "ds_read_b64_tr_b16 a[208:209], v97 offset:2048\n"
    "ds_read_b64_tr_b16 a[210:211], v97 offset:10240\n"
    "v_fma_f32 v52, v52, s39, -v103\n"
    "v_fma_f32 v53, v53, s39, -v103\n"
    "v_fma_f32 v54, v54, s39, -v103\n"
    "v_fma_f32 v55, v55, s39, -v103\n"
    "v_mfma_f32_16x16x32_bf16 v[148:151], a[212:215], v[40:43], v[148:151]\n"
    "v_exp_f32 v52, v52\n"
    "v_mfma_f32_16x16x32_bf16 v[144:147], a[212:215], v[32:35], v[144:147]\n"
    "ds_read_b64_tr_b16 a[212:213], v98 offset:2048\n"
    "ds_read_b64_tr_b16 a[214:215], v98 offset:10240\n"
    "v_exp_f32 v53, v53\n"
    "v_exp_f32 v54, v54\n"
    "v_cvt_pk_bf16_f32 v50, v52, v53\n"
    "v_add_f32 v52, v109, v52\n"
    "s_waitcnt lgkmcnt(12)\n"
    "v_mfma_f32_16x16x32_bf16 v[160:163], a[216:219], v[32:35], v[160:163]\n"
    "v_exp_f32 v55, v55\n"
    "v_mfma_f32_16x16x32_bf16 v[164:167], a[216:219], v[40:43], v[164:167]\n"
    "ds_read_b64_tr_b16 a[216:217], v97 offset:2560\n"
    "ds_read_b64_tr_b16 a[218:219], v97 offset:10752\n"
    "v_add_f32 v52, v52, v53\n"
    "v_add_f32 v52, v52, v54\n"
    "v_cvt_pk_bf16_f32 v51, v54, v55\n"
    "v_pk_mul_f32 v[128:129], v[128:129], v[122:123] op_sel_hi:[1,0]\n"
    "v_pk_mul_f32 v[130:131], v[130:131], v[122:123] op_sel_hi:[1,0]\n"
    "v_pk_mul_f32 v[132:133], v[132:133], v[112:113] op_sel_hi:[1,0]\n"
    "v_pk_mul_f32 v[134:135], v[134:135], v[112:113] op_sel_hi:[1,0]\n"
    "v_cvt_pk_bf16_f32 v128, v128, v129\n"
    "v_mfma_f32_16x16x32_bf16 v[180:183], a[220:223], v[40:43], v[180:183]\n"
    "v_cvt_pk_bf16_f32 v129, v130, v131\n"
    "v_cvt_pk_bf16_f32 v132, v132, v133\n"
    "v_mfma_f32_16x16x32_bf16 v[176:179], a[220:223], v[32:35], v[176:179]\n"
    "ds_read_b64_tr_b16 a[220:221], v98 offset:2560\n"
    "ds_read_b64_tr_b16 a[222:223], v98 offset:10752\n"
    "v_cvt_pk_bf16_f32 v133, v134, v135\n"
    "v_add_f32 v52, v52, v55\n"
    "v_fma_f32 v60, v60, s39, -v104\n"
    "v_pk_mul_f32 v[144:145], v[144:145], v[122:123] op_sel_hi:[1,0]\n"
    "v_pk_mul_f32 v[146:147], v[146:147], v[122:123] op_sel_hi:[1,0]\n"
    "v_cvt_pk_bf16_f32 v144, v144, v145\n"
    "s_waitcnt lgkmcnt(12)\n"
    "v_mfma_f32_16x16x32_bf16 v[192:195], a[240:243], v[32:35], v[192:195]\n"
    "v_cvt_pk_bf16_f32 v145, v146, v147\n"
    "v_fma_f32 v61, v61, s39, -v104\n"
    "v_mfma_f32_16x16x32_bf16 v[196:199], a[240:243], v[40:43], v[196:199]\n"
    "ds_read_b64_tr_b16 a[240:241], v97 offset:3072\n"
    "ds_read_b64_tr_b16 a[242:243], v97 offset:11264\n"
    "v_exp_f32 v56, v56\n"
    "v_fma_f32 v62, v62, s39, -v104\n"
    "v_pk_mul_f32 v[160:161], v[160:161], v[122:123] op_sel_hi:[1,0]\n"
    "v_pk_mul_f32 v[162:163], v[162:163], v[122:123] op_sel_hi:[1,0]\n"
    "v_pk_mul_f32 v[164:165], v[164:165], v[112:113] op_sel_hi:[1,0]\n"
    "v_cvt_pk_bf16_f32 v160, v160, v161\n"
    "v_mfma_f32_16x16x32_bf16 v[212:215], a[244:247], v[40:43], v[212:215]\n"
    "v_cvt_pk_bf16_f32 v161, v162, v163\n"
    "v_fma_f32 v63, v63, s39, -v104\n"
    "v_mfma_f32_16x16x32_bf16 v[208:211], a[244:247], v[32:35], v[208:211]\n"
    "ds_write2st64_b64 v123, v[128:129], v[160:161] offset0:0 offset1:2\n"
    "v_pk_mul_f32 v[176:177], v[176:177], v[122:123] op_sel_hi:[1,0]\n"
    "v_pk_mul_f32 v[178:179], v[178:179], v[122:123] op_sel_hi:[1,0]\n"
    "v_cvt_pk_bf16_f32 v176, v176, v177\n"
    "s_waitcnt lgkmcnt(11)\n"
    "v_mfma_f32_16x16x32_bf16 v[224:227], a[248:251], v[32:35], v[224:227]\n"
    "ds_read_b64_tr_b16 a[244:245], v98 offset:3072\n"
    "v_cvt_pk_bf16_f32 v177, v178, v179\n"
    "v_mfma_f32_16x16x32_bf16 v[228:231], a[248:251], v[40:43], v[228:231]\n"
    "ds_write2st64_b64 v111, v[144:145], v[176:177] offset0:1 offset1:3\n"
    "ds_read_b64_tr_b16 a[246:247], v98 offset:11264\n"
    "ds_read_b128 v[160:163], v113 offset:0\n"
    "ds_read_b64_tr_b16 a[248:249], v97 offset:3584\n"
    "ds_read_b64_tr_b16 a[250:251], v97 offset:11776\n"
    "v_mfma_f32_16x16x32_bf16 v[244:247], a[252:255], v[40:43], v[244:247]\n"
    "ds_read_b128 v[176:179], v113 offset:256\n"
    "v_mfma_f32_16x16x32_bf16 v[240:243], a[252:255], v[32:35], v[240:243]\n"
    "ds_read_b64_tr_b16 a[252:253], v98 offset:3584\n"
    "ds_read_b64_tr_b16 a[254:255], v98 offset:11776\n"
    "v_exp_f32 v57, v57\n"
    "s_nop 0\n"
    "v_add_f32 v109, v56, v57\n"
    "v_pk_mul_f32 v[166:167], v[166:167], v[112:113] op_sel_hi:[1,0]\n"
    "v_pk_mul_f32 v[148:149], v[148:149], v[112:113] op_sel_hi:[1,0]\n"
    "v_pk_mul_f32 v[150:151], v[150:151], v[112:113] op_sel_hi:[1,0]\n"
    "v_pk_mul_f32 v[180:181], v[180:181], v[112:113] op_sel_hi:[1,0]\n"
    "v_cvt_pk_bf16_f32 v164, v164, v165\n"
    "s_waitcnt lgkmcnt(15)\n"
    "v_mfma_f32_16x16x32_bf16 a[128:131], a[208:211], v[32:35], a[128:131]\n"
    "v_cvt_pk_bf16_f32 v165, v166, v167\n"
    "v_cvt_pk_bf16_f32 v148, v148, v149\n"
    "v_mfma_f32_16x16x32_bf16 a[132:135], a[208:211], v[40:43], a[132:135]\n"
    "s_waitcnt lgkmcnt(5)\n"
    "buffer_store_dwordx4 v[160:163], v110, s[28:31], s92 offen offset:0 sc1\n"
    "ds_read_b128 a[208:211], v96 offset:0\n"
    "v_cvt_pk_bf16_f32 v149, v150, v151\n"
    "v_pk_mul_f32 v[182:183], v[182:183], v[112:113] op_sel_hi:[1,0]\n"
    "v_cvt_pk_bf16_f32 v180, v180, v181\n"
    "v_mfma_f32_16x16x32_bf16 a[148:151], a[212:215], v[40:43], a[148:151]\n"
    "v_cvt_pk_bf16_f32 v181, v182, v183\n"
    "v_cvt_pk_bf16_f32 v56, v56, v57\n"
    "v_mfma_f32_16x16x32_bf16 a[144:147], a[212:215], v[32:35], a[144:147]\n"
    "ds_read_b128 a[212:215], v96 offset:512\n"
    "ds_write2st64_b64 v123, v[132:133], v[164:165] offset0:4 offset1:6\n"
    "v_pk_mul_f32 v[192:193], v[192:193], v[122:123] op_sel_hi:[1,0]\n"
    "v_pk_mul_f32 v[194:195], v[194:195], v[122:123] op_sel_hi:[1,0]\n"
    "v_cvt_pk_bf16_f32 v192, v192, v193\n"
    "v_mfma_f32_16x16x32_bf16 a[160:163], a[216:219], v[32:35], a[160:163]\n"
    "v_cvt_pk_bf16_f32 v193, v194, v195\n"
    "v_accvgpr_read_b32 v114, a128\n"
    "v_mfma_f32_16x16x32_bf16 a[164:167], a[216:219], v[40:43], a[164:167]\n"
    "ds_read_b128 a[216:219], v96 offset:8192\n"
    "ds_write2st64_b64 v111, v[148:149], v[180:181] offset0:5 offset1:7\n"
    "ds_read_b128 v[164:167], v113 offset:2048\n"
    "ds_read_b128 v[180:183], v113 offset:2304\n"
    "v_pk_mul_f32 v[224:225], v[224:225], v[122:123] op_sel_hi:[1,0]\n"
    "v_accvgpr_read_b32 v115, a129\n"
    "v_mfma_f32_16x16x32_bf16 a[180:183], a[220:223], v[40:43], a[180:183]\n"
    "v_exp_f32 v58, v58\n"
    "v_mfma_f32_16x16x32_bf16 a[176:179], a[220:223], v[32:35], a[176:179]\n"
    "s_waitcnt lgkmcnt(9)\n"
    "buffer_store_dwordx4 v[176:179], v110, s[28:31], s93 offen offset:0 sc1\n"
    "ds_read_b128 a[220:223], v96 offset:8704\n"
    "v_pk_mul_f32 v[226:227], v[226:227], v[122:123] op_sel_hi:[1,0]\n"
    "v_cvt_pk_bf16_f32 v224, v224, v225\n"
    "v_mfma_f32_16x16x32_bf16 a[192:195], a[240:243], v[32:35], a[192:195]\n"
    "v_cvt_pk_bf16_f32 v225, v226, v227\n"
    "v_add_f32 v109, v109, v58\n"
    "v_mfma_f32_16x16x32_bf16 a[196:199], a[240:243], v[40:43], a[196:199]\n"
    "ds_read_b128 a[240:243], v96 offset:1024\n"
    "ds_write2st64_b64 v123, v[192:193], v[224:225] offset0:8 offset1:10\n"
    "v_pk_mul_f32 v[208:209], v[208:209], v[122:123] op_sel_hi:[1,0]\n"
    "v_pk_mul_f32 v[210:211], v[210:211], v[122:123] op_sel_hi:[1,0]\n"
    "v_cvt_pk_bf16_f32 v208, v208, v209\n"
    "v_mfma_f32_16x16x32_bf16 v[68:71], a[244:247], v[40:43], v[68:71]\n"
    "v_cvt_pk_bf16_f32 v209, v210, v211\n"
    "v_accvgpr_read_b32 v116, a130\n"
    "v_mfma_f32_16x16x32_bf16 v[64:67], a[244:247], v[32:35], v[64:67]\n"
    "ds_read_b128 a[244:247], v96 offset:1536\n"
    "v_exp_f32 v59, v59\n"
    "v_exp_f32 v60, v60\n"
    "v_pk_mul_f32 v[240:241], v[240:241], v[122:123] op_sel_hi:[1,0]\n"
    "v_pk_mul_f32 v[242:243], v[242:243], v[122:123] op_sel_hi:[1,0]\n"
    "v_pk_mul_f32 v[196:197], v[196:197], v[112:113] op_sel_hi:[1,0]\n"
    "v_cvt_pk_bf16_f32 v240, v240, v241\n"
    "s_waitcnt lgkmcnt(11)\n"
    "v_mfma_f32_16x16x32_bf16 a[224:227], a[248:251], v[32:35], a[224:227]\n"
    "v_cvt_pk_bf16_f32 v241, v242, v243\n"
    "v_add_f32 v109, v109, v59\n"
    "v_mfma_f32_16x16x32_bf16 a[228:231], a[248:251], v[40:43], a[228:231]\n"
    "s_waitcnt lgkmcnt(5)\n"
    "buffer_store_dwordx4 v[164:167], v110, s[28:31], s94 offen offset:0 sc1\n"
    "ds_read_b128 a[248:251], v96 offset:9216\n"
    "v_pk_mul_f32 v[198:199], v[198:199], v[112:113] op_sel_hi:[1,0]\n"
    "v_cvt_pk_bf16_f32 v196, v196, v197\n"
    "v_cvt_pk_bf16_f32 v197, v198, v199\n"
    "v_pk_mul_f32 v[228:229], v[228:229], v[112:113] op_sel_hi:[1,0]\n"
    "v_cvt_pk_bf16_f32 v57, v58, v59\n"
    "v_mfma_f32_16x16x32_bf16 v[84:87], a[252:255], v[40:43], v[84:87]\n"
    "v_exp_f32 v61, v61\n"
    "v_mfma_f32_16x16x32_bf16 v[80:83], a[252:255], v[32:35], v[80:83]\n"
    "ds_read_b128 a[252:255], v96 offset:9728\n"
    "ds_write2st64_b64 v111, v[208:209], v[240:241] offset0:9 offset1:11\n"
    "ds_read_b128 v[224:227], v113 offset:4096\n"
    "v_mfma_f32_16x16x32_bf16 v[16:19], a[208:211], a[64:67], 0\n"
    "ds_read_b128 v[240:243], v113 offset:4352\n"
    "v_cvt_pk_bf16_f32 v58, v60, v61\n"
    "v_mfma_f32_16x16x32_bf16 v[16:19], a[212:215], a[68:71], v[16:19]\n"
    "v_exp_f32 v62, v62\n"
    "v_add_f32 v60, v109, v60\n"
    "v_exp_f32 v63, v63\n"
    "v_add_f32 v60, v60, v61\n"
    "v_pk_mul_f32 v[230:231], v[230:231], v[112:113] op_sel_hi:[1,0]\n"
    "v_cvt_pk_bf16_f32 v228, v228, v229\n"
    "v_cvt_pk_bf16_f32 v229, v230, v231\n"
    "v_mfma_f32_16x16x32_bf16 v[24:27], a[208:211], a[96:99], 0\n"
    "ds_read_b128 a[208:211], v96 offset:2048\n"
    "v_add_f32 v60, v60, v62\n"
    "v_mfma_f32_16x16x32_bf16 v[24:27], a[212:215], a[100:103], v[24:27]\n"
    "s_waitcnt lgkmcnt(10)\n"
    "buffer_store_dwordx4 v[180:183], v110, s[28:31], s95 offen offset:0 sc1\n"
    "ds_read_b128 a[212:215], v96 offset:2560\n"
    "v_cvt_pk_bf16_f32 v59, v62, v63\n"
    "v_add_f32 v60, v60, v63\n"
    "v_max_f32 v109, v52, v60\n"
    "s_waitcnt lgkmcnt(10)\n"
    "v_mfma_f32_16x16x32_bf16 v[20:23], a[216:219], a[64:67], 0\n"
    "v_cmp_le_f32 vcc, 1.0, v109\n"
    "s_nop 0\n"
    "s_cbranch_vccnz L_ra13\n"
    "L_ra13_ret:\n"
    "v_mfma_f32_16x16x32_bf16 v[20:23], a[220:223], a[68:71], v[20:23]\n"
    "ds_write2st64_b64 v123, v[196:197], v[228:229] offset0:12 offset1:14\n"
    "v_pk_mul_f32 v[212:213], v[212:213], v[112:113] op_sel_hi:[1,0]\n"
    "v_pk_mul_f32 v[214:215], v[214:215], v[112:113] op_sel_hi:[1,0]\n"
    "v_cvt_pk_bf16_f32 v212, v212, v213\n"
    "v_cvt_pk_bf16_f32 v213, v214, v215\n"
    "v_pk_mul_f32 v[244:245], v[244:245], v[112:113] op_sel_hi:[1,0]\n"
    "v_add_f32 v107, v107, v52\n"
    "v_mfma_f32_16x16x32_bf16 v[28:31], a[216:219], a[96:99], 0\n"
    "ds_read_b128 a[216:219], v96 offset:10240\n"
    "v_add_f32 v108, v108, v60\n"
    "v_mfma_f32_16x16x32_bf16 v[28:31], a[220:223], a[100:103], v[28:31]\n"
    "ds_read_b128 a[220:223], v96 offset:10752\n"
    "v_accvgpr_read_b32 v117, a131\n"
    "v_accvgpr_read_b32 v118, a160\n"
    "v_accvgpr_read_b32 v119, a161\n"
    "v_accvgpr_read_b32 v120, a162\n"
    "v_pk_mul_f32 v[246:247], v[246:247], v[112:113] op_sel_hi:[1,0]\n"
    "v_pk_mul_f32 v[114:115], v[114:115], v[122:123] op_sel_hi:[1,0]\n"
    "v_cvt_pk_bf16_f32 v244, v244, v245\n"
    "s_waitcnt lgkmcnt(10)\n"
    "v_mfma_f32_16x16x32_bf16 v[16:19], a[240:243], a[72:75], v[16:19]\n"
    "v_cvt_pk_bf16_f32 v245, v246, v247\n"
    "v_accvgpr_read_b32 v121, a163\n"
    "v_mfma_f32_16x16x32_bf16 v[16:19], a[244:247], a[76:79], v[16:19]\n"
    "s_waitcnt lgkmcnt(6)\n"
    "buffer_store_dwordx4 v[224:227], v110, s[28:31], s92 offen offset:128 sc1\n"
    "v_pk_mul_f32 v[116:117], v[116:117], v[122:123] op_sel_hi:[1,0]\n"
    "v_pk_mul_f32 v[118:119], v[118:119], v[122:123] op_sel_hi:[1,0]\n"
    "v_pk_mul_f32 v[120:121], v[120:121], v[122:123] op_sel_hi:[1,0]\n"
    "v_cvt_pk_bf16_f32 v114, v114, v115\n"
    "v_mfma_f32_16x16x32_bf16 v[24:27], a[240:243], a[104:107], v[24:27]\n"
    "ds_read_b128 a[240:243], v96 offset:3072\n"
    "v_cvt_pk_bf16_f32 v115, v116, v117\n"
    "v_mfma_f32_16x16x32_bf16 v[24:27], a[244:247], a[108:111], v[24:27]\n"
    "ds_read_b128 a[244:247], v96 offset:3584\n"
    "ds_write2st64_b64 v111, v[212:213], v[244:245] offset0:13 offset1:15\n"
    "ds_read_b128 v[228:231], v113 offset:6144\n"
    "ds_read_b128 v[244:247], v113 offset:6400\n"
    "v_mfma_f32_16x16x32_bf16 v[20:23], a[248:251], a[72:75], v[20:23]\n"
    "v_cvt_pk_bf16_f32 v118, v118, v119\n"
    "v_cvt_pk_bf16_f32 v119, v120, v121\n"
    "v_mfma_f32_16x16x32_bf16 v[20:23], a[252:255], a[76:79], v[20:23]\n"
    "ds_write2st64_b64 v123, v[114:115], v[118:119] offset0:16 offset1:18\n"
    "v_accvgpr_read_b32 v114, a144\n"
    "v_accvgpr_read_b32 v115, a145\n"
    "v_accvgpr_read_b32 v116, a146\n"
    "v_accvgpr_read_b32 v117, a147\n"
    "v_mfma_f32_16x16x32_bf16 v[28:31], a[248:251], a[104:107], v[28:31]\n"
    "ds_read_b128 a[248:251], v96 offset:11264\n"
    "v_accvgpr_read_b32 v118, a176\n"
    "v_mfma_f32_16x16x32_bf16 v[28:31], a[252:255], a[108:111], v[28:31]\n"
    "s_waitcnt lgkmcnt(12)\n"
    "buffer_store_dwordx4 v[240:243], v110, s[28:31], s93 offen offset:128 sc1\n"
    "ds_read_b128 a[252:255], v96 offset:11776\n"
    "v_pk_mul_f32 v[114:115], v[114:115], v[122:123] op_sel_hi:[1,0]\n"
    "v_pk_mul_f32 v[116:117], v[116:117], v[122:123] op_sel_hi:[1,0]\n"
    "v_cvt_pk_bf16_f32 v114, v114, v115\n"
    "v_cvt_pk_bf16_f32 v115, v116, v117\n"
    "s_waitcnt lgkmcnt(11)\n"
    "v_mfma_f32_16x16x32_bf16 v[16:19], a[208:211], a[80:83], v[16:19]\n"
    "v_accvgpr_read_b32 v119, a177\n"
    "v_accvgpr_read_b32 v120, a178\n"
    "v_mfma_f32_16x16x32_bf16 v[16:19], a[212:215], a[84:87], v[16:19]\n"
    "v_accvgpr_read_b32 v121, a179\n"
    "v_mul_f32 v118, v118, v122\n"
    "v_mul_f32 v119, v119, v122\n"
    "v_mul_f32 v120, v120, v122\n"
    "v_mul_f32 v121, v121, v122\n"
    "v_pk_mul_f32 v[64:65], v[64:65], v[122:123] op_sel_hi:[1,0]\n"
    "v_pk_mul_f32 v[66:67], v[66:67], v[122:123] op_sel_hi:[1,0]\n"
    "v_cvt_pk_bf16_f32 v118, v118, v119\n"
    "v_mfma_f32_16x16x32_bf16 v[24:27], a[208:211], a[112:115], v[24:27]\n"
    "ds_read_b128 a[208:211], v96 offset:16384\n"
    "v_cvt_pk_bf16_f32 v119, v120, v121\n"
    "v_mfma_f32_16x16x32_bf16 v[24:27], a[212:215], a[116:119], v[24:27]\n"
    "ds_read_b128 a[212:215], v96 offset:16896\n"
    "ds_write2st64_b64 v111, v[114:115], v[118:119] offset0:17 offset1:19\n"
    "ds_read_b128 a[160:163], v113 offset:8192\n"
    "ds_read_b128 a[176:179], v113 offset:8448\n"
    "v_accvgpr_read_b32 v114, a132\n"
    "s_waitcnt lgkmcnt(13)\n"
    "v_mfma_f32_16x16x32_bf16 v[20:23], a[216:219], a[80:83], v[20:23]\n"
    "v_accvgpr_read_b32 v115, a133\n"
    "v_accvgpr_read_b32 v116, a134\n"
    "v_mfma_f32_16x16x32_bf16 v[20:23], a[220:223], a[84:87], v[20:23]\n"
    "s_waitcnt lgkmcnt(9)\n"
    "buffer_store_dwordx4 v[228:231], v110, s[28:31], s94 offen offset:128 sc1\n"
    "v_accvgpr_read_b32 v117, a135\n"
    "v_pk_mul_f32 v[114:115], v[114:115], v[112:113] op_sel_hi:[1,0]\n"
    "v_pk_mul_f32 v[116:117], v[116:117], v[112:113] op_sel_hi:[1,0]\n"
    "v_cvt_pk_bf16_f32 v114, v114, v115\n"
    "v_cvt_pk_bf16_f32 v115, v116, v117\n"
    "v_mfma_f32_16x16x32_bf16 v[28:31], a[216:219], a[112:115], v[28:31]\n"
    "ds_read_b128 a[216:219], v96 offset:24576\n"
    "v_accvgpr_read_b32 v118, a164\n"
    "v_mfma_f32_16x16x32_bf16 v[28:31], a[220:223], a[116:119], v[28:31]\n"
    "ds_read_b128 a[220:223], v96 offset:25088\n"
    "v_accvgpr_read_b32 v119, a165\n"
    "v_accvgpr_read_b32 v120, a166\n"
    "v_accvgpr_read_b32 v121, a167\n"
    "v_cvt_pk_bf16_f32 v64, v64, v65\n"
    "v_pk_mul_f32 v[118:119], v[118:119], v[112:113] op_sel_hi:[1,0]\n"
    "v_pk_mul_f32 v[120:121], v[120:121], v[112:113] op_sel_hi:[1,0]\n"
    "v_cvt_pk_bf16_f32 v118, v118, v119\n"
    "v_mfma_f32_16x16x32_bf16 v[16:19], a[240:243], a[88:91], v[16:19]\n"
    "v_cvt_pk_bf16_f32 v119, v120, v121\n"
    "v_cvt_pk_bf16_f32 v65, v66, v67\n"
    "v_mfma_f32_16x16x32_bf16 v[16:19], a[244:247], a[92:95], v[16:19]\n"
    "ds_write2st64_b64 v123, v[114:115], v[118:119] offset0:20 offset1:22\n"
    "v_accvgpr_read_b32 v114, a148\n"
    "v_accvgpr_read_b32 v115, a149\n"
    "v_accvgpr_read_b32 v116, a150\n"
    "v_accvgpr_read_b32 v117, a151\n"
    "v_pk_mul_f32 v[114:115], v[114:115], v[112:113] op_sel_hi:[1,0]\n"
    "v_accvgpr_read_b32 v118, a180\n"
    "v_mfma_f32_16x16x32_bf16 v[24:27], a[240:243], a[120:123], v[24:27]\n"
    "ds_read_b128 a[240:243], v96 offset:17408\n"
    "v_accvgpr_read_b32 v119, a181\n"
    "v_mfma_f32_16x16x32_bf16 v[24:27], a[244:247], a[124:127], v[24:27]\n"
    "s_waitcnt lgkmcnt(12)\n"
    "buffer_store_dwordx4 v[244:247], v110, s[28:31], s95 offen offset:128 sc1\n"
    "ds_read_b128 a[244:247], v96 offset:17920\n"
    "v_pk_mul_f32 v[116:117], v[116:117], v[112:113] op_sel_hi:[1,0]\n"
    "v_cvt_pk_bf16_f32 v114, v114, v115\n"
    "s_waitcnt lgkmcnt(10)\n"
    "v_mfma_f32_16x16x32_bf16 v[20:23], a[248:251], a[88:91], v[20:23]\n"
    "v_cvt_pk_bf16_f32 v115, v116, v117\n"
    "v_accvgpr_read_b32 v120, a182\n"
    "v_mfma_f32_16x16x32_bf16 v[20:23], a[252:255], a[92:95], v[20:23]\n"
    "v_accvgpr_read_b32 v121, a183\n"
    "v_fma_f32 v16, v16, s39, -v103\n"
    "v_fma_f32 v17, v17, s39, -v103\n"
    "v_fma_f32 v18, v18, s39, -v103\n"
    "v_fma_f32 v19, v19, s39, -v103\n"
    "v_pk_mul_f32 v[118:119], v[118:119], v[112:113] op_sel_hi:[1,0]\n"
    "v_pk_mul_f32 v[120:121], v[120:121], v[112:113] op_sel_hi:[1,0]\n"
    "v_pk_mul_f32 v[80:81], v[80:81], v[122:123] op_sel_hi:[1,0]\n"
    "v_pk_mul_f32 v[82:83], v[82:83], v[122:123] op_sel_hi:[1,0]\n"
    "v_cvt_pk_bf16_f32 v118, v118, v119\n"
    "v_mfma_f32_16x16x32_bf16 v[28:31], a[248:251], a[120:123], v[28:31]\n"
    "ds_read_b128 a[248:251], v96 offset:25600\n"
    "v_cvt_pk_bf16_f32 v119, v120, v121\n"
    "v_mfma_f32_16x16x32_bf16 v[28:31], a[252:255], a[124:127], v[28:31]\n"
    "ds_read_b128 a[252:255], v96 offset:26112\n"
    "ds_write2st64_b64 v111, v[114:115], v[118:119] offset0:21 offset1:23\n"
    "ds_read_b128 a[164:167], v113 offset:10240\n"
    "s_waitcnt lgkmcnt(12)\n"
    "v_mfma_f32_16x16x32_bf16 v[32:35], a[208:211], a[64:67], 0\n"
    "ds_read_b128 a[180:183], v113 offset:10496\n"
    "v_accvgpr_read_b32 v114, a192\n"
    "v_mfma_f32_16x16x32_bf16 v[32:35], a[212:215], a[68:71], v[32:35]\n"
    "s_waitcnt lgkmcnt(11)\n"
    "buffer_store_dwordx4 a[160:163], v110, s[28:31], s92 offen offset:256 sc1\n"
    "v_accvgpr_read_b32 v115, a193\n"
    "v_accvgpr_read_b32 v116, a194\n"
    "v_accvgpr_read_b32 v117, a195\n"
    "v_pk_mul_f32 v[114:115], v[114:115], v[122:123] op_sel_hi:[1,0]\n"
    "v_pk_mul_f32 v[116:117], v[116:117], v[122:123] op_sel_hi:[1,0]\n"
    "v_cvt_pk_bf16_f32 v114, v114, v115\n"
    "v_mfma_f32_16x16x32_bf16 v[40:43], a[208:211], a[96:99], 0\n"
    "ds_read_b128 a[208:211], v96 offset:18432\n"
    "v_cvt_pk_bf16_f32 v115, v116, v117\n"
    "v_mfma_f32_16x16x32_bf16 v[40:43], a[212:215], a[100:103], v[40:43]\n"
    "ds_read_b128 a[212:215], v96 offset:18944\n"
    "v_accvgpr_read_b32 v118, a224\n"
    "v_accvgpr_read_b32 v119, a225\n"
    "v_accvgpr_read_b32 v120, a226\n"
    "v_accvgpr_read_b32 v121, a227\n"
    "v_pk_mul_f32 v[118:119], v[118:119], v[122:123] op_sel_hi:[1,0]\n"
    "v_pk_mul_f32 v[120:121], v[120:121], v[122:123] op_sel_hi:[1,0]\n"
    "v_cvt_pk_bf16_f32 v118, v118, v119\n"
    "s_waitcnt lgkmcnt(10)\n"
    "v_mfma_f32_16x16x32_bf16 v[36:39], a[216:219], a[64:67], 0\n"
    "v_cvt_pk_bf16_f32 v119, v120, v121\n"
    "v_cvt_pk_bf16_f32 v80, v80, v81\n"
    "v_mfma_f32_16x16x32_bf16 v[36:39], a[220:223], a[68:71], v[36:39]\n"
    "ds_write2st64_b64 v123, v[114:115], v[118:119] offset0:24 offset1:26\n"
    "v_cvt_pk_bf16_f32 v81, v82, v83\n"
    "v_accvgpr_read_b32 v114, a196\n"
    "v_accvgpr_read_b32 v115, a197\n"
    "v_accvgpr_read_b32 v116, a198\n"
    "v_mfma_f32_16x16x32_bf16 v[44:47], a[216:219], a[96:99], 0\n"
    "ds_read_b128 a[216:219], v96 offset:26624\n"
    "v_accvgpr_read_b32 v117, a199\n"
    "v_mfma_f32_16x16x32_bf16 v[44:47], a[220:223], a[100:103], v[44:47]\n"
    "buffer_store_dwordx4 a[176:179], v110, s[28:31], s93 offen offset:256 sc1\n"
    "ds_read_b128 a[220:223], v96 offset:27136\n"
    "v_pk_mul_f32 v[114:115], v[114:115], v[112:113] op_sel_hi:[1,0]\n"
    "v_pk_mul_f32 v[116:117], v[116:117], v[112:113] op_sel_hi:[1,0]\n"
    "v_cvt_pk_bf16_f32 v114, v114, v115\n"
    "s_waitcnt lgkmcnt(10)\n"
    "v_mfma_f32_16x16x32_bf16 v[32:35], a[240:243], a[72:75], v[32:35]\n"
    "v_cvt_pk_bf16_f32 v115, v116, v117\n"
    "v_accvgpr_read_b32 v118, a228\n"
    "v_mfma_f32_16x16x32_bf16 v[32:35], a[244:247], a[76:79], v[32:35]\n"
    "ds_write2st64_b64 v111, v[64:65], v[80:81] offset0:25 offset1:27\n"
    "ds_read_b128 a[224:227], v113 offset:12288\n"
    "ds_read_b128 v[80:83], v113 offset:12544\n"
    "v_accvgpr_read_b32 v119, a229\n"
    "v_accvgpr_read_b32 v120, a230\n"
    "v_mfma_f32_16x16x32_bf16 v[40:43], a[240:243], a[104:107], v[40:43]\n"
    "ds_read_b128 a[240:243], v96 offset:19456\n"
    "v_accvgpr_read_b32 v121, a231\n"
    "v_mfma_f32_16x16x32_bf16 v[40:43], a[244:247], a[108:111], v[40:43]\n"
    "ds_read_b128 a[244:247], v96 offset:19968\n"
    "v_exp_f32 v16, v16\n"
    "v_exp_f32 v17, v17\n"
    "v_pk_mul_f32 v[118:119], v[118:119], v[112:113] op_sel_hi:[1,0]\n"
    "v_pk_mul_f32 v[120:121], v[120:121], v[112:113] op_sel_hi:[1,0]\n"
    "v_cvt_pk_bf16_f32 v118, v118, v119\n"
    "v_cvt_pk_bf16_f32 v119, v120, v121\n"
    "s_waitcnt lgkmcnt(13)\n"
    "v_mfma_f32_16x16x32_bf16 v[36:39], a[248:251], a[72:75], v[36:39]\n"
    "v_exp_f32 v18, v18\n"
    "v_mfma_f32_16x16x32_bf16 v[36:39], a[252:255], a[76:79], v[36:39]\n"
    "s_waitcnt lgkmcnt(11)\n"
    "buffer_store_dwordx4 a[164:167], v110, s[28:31], s94 offen offset:256 sc1\n"
    "ds_write2st64_b64 v123, v[114:115], v[118:119] offset0:28 offset1:30\n"
    "v_mfma_f32_16x16x32_bf16 v[44:47], a[248:251], a[104:107], v[44:47]\n"
    "ds_read_b128 a[248:251], v96 offset:27648\n"
    "v_add_f32 v109, v16, v17\n"
    "v_mfma_f32_16x16x32_bf16 v[44:47], a[252:255], a[108:111], v[44:47]\n"
    "ds_read_b128 a[252:255], v96 offset:28160\n"
    "v_exp_f32 v19, v19\n"
    "v_cvt_pk_bf16_f32 v16, v16, v17\n"
    "v_add_f32 v109, v109, v18\n"
    "v_add_f32 v109, v109, v19\n"
    "v_cvt_pk_bf16_f32 v17, v18, v19\n"
    "v_fma_f32 v20, v20, s39, -v103\n"
    "s_waitcnt lgkmcnt(11)\n"
    "v_mfma_f32_16x16x32_bf16 v[32:35], a[208:211], a[80:83], v[32:35]\n"
    "v_fma_f32 v21, v21, s39, -v103\n"
    "v_fma_f32 v22, v22, s39, -v103\n"
    "v_mfma_f32_16x16x32_bf16 v[32:35], a[212:215], a[84:87], v[32:35]\n"
    "v_fma_f32 v23, v23, s39, -v103\n"
    "v_exp_f32 v20, v20\n"
    "v_exp_f32 v21, v21\n"
    "v_exp_f32 v22, v22\n"
    "v_cvt_pk_bf16_f32 v18, v20, v21\n"
    "v_add_f32 v20, v109, v20\n"
    "v_mfma_f32_16x16x32_bf16 v[40:43], a[208:211], a[112:115], v[40:43]\n"
    "ds_read_b64_tr_b16 a[208:209], v97 offset:16384\n"
    "ds_read_b64_tr_b16 a[210:211], v97 offset:24576\n"
    "v_mfma_f32_16x16x32_bf16 v[40:43], a[212:215], a[116:119], v[40:43]\n"
    "buffer_store_dwordx4 a[180:183], v110, s[28:31], s95 offen offset:256 sc1\n"
    "ds_read_b64_tr_b16 a[212:213], v98 offset:16384\n"
    "ds_read_b64_tr_b16 a[214:215], v98 offset:24576\n"
    "v_exp_f32 v23, v23\n"
    "v_add_f32 v20, v20, v21\n"
    "s_waitcnt lgkmcnt(12)\n"
    "v_mfma_f32_16x16x32_bf16 v[36:39], a[216:219], a[80:83], v[36:39]\n"
    "v_add_f32 v20, v20, v22\n"
    "v_cvt_pk_bf16_f32 v19, v22, v23\n"
    "v_mfma_f32_16x16x32_bf16 v[36:39], a[220:223], a[84:87], v[36:39]\n"
    "v_add_f32 v20, v20, v23\n"
    "v_fma_f32 v24, v24, s39, -v104\n"
    "v_fma_f32 v25, v25, s39, -v104\n"
    "v_fma_f32 v26, v26, s39, -v104\n"
    "v_fma_f32 v27, v27, s39, -v104\n"
    "v_exp_f32 v24, v24\n"
    "v_exp_f32 v25, v25\n"
    "v_mfma_f32_16x16x32_bf16 v[44:47], a[216:219], a[112:115], v[44:47]\n"
    "ds_read_b64_tr_b16 a[216:217], v97 offset:16896\n"
    "ds_read_b64_tr_b16 a[218:219], v97 offset:25088\n"
    "v_mfma_f32_16x16x32_bf16 v[44:47], a[220:223], a[116:119], v[44:47]\n"
    "ds_read_b64_tr_b16 a[220:221], v98 offset:16896\n"
    "ds_read_b64_tr_b16 a[222:223], v98 offset:25088\n"
    "v_exp_f32 v26, v26\n"
    "v_exp_f32 v27, v27\n"
    "v_add_f32 v109, v24, v25\n"
    "v_cvt_pk_bf16_f32 v24, v24, v25\n"
    "s_waitcnt lgkmcnt(11)\n"
    "v_mfma_f32_16x16x32_bf16 v[32:35], a[240:243], a[88:91], v[32:35]\n"
    "v_add_f32 v109, v109, v26\n"
    "v_add_f32 v109, v109, v27\n"
    "v_mfma_f32_16x16x32_bf16 v[32:35], a[244:247], a[92:95], v[32:35]\n"
    "buffer_store_dwordx4 a[224:227], v110, s[28:31], s92 offen offset:384 sc1\n"
    "v_cvt_pk_bf16_f32 v25, v26, v27\n"
    "v_fma_f32 v28, v28, s39, -v104\n"
    "v_fma_f32 v29, v29, s39, -v104\n"
    "v_fma_f32 v30, v30, s39, -v104\n"
    "v_fma_f32 v31, v31, s39, -v104\n"
    "v_mfma_f32_16x16x32_bf16 v[40:43], a[240:243], a[120:123], v[40:43]\n"
    "ds_read_b64_tr_b16 a[240:241], v97 offset:17408\n"
    "ds_read_b64_tr_b16 a[242:243], v97 offset:25600\n"
    "v_mfma_f32_16x16x32_bf16 v[40:43], a[244:247], a[124:127], v[40:43]\n"
    "ds_read_b64_tr_b16 a[244:245], v98 offset:17408\n"
    "ds_read_b64_tr_b16 a[246:247], v98 offset:25600\n"
    "v_exp_f32 v28, v28\n"
    "v_exp_f32 v29, v29\n"
    "v_exp_f32 v30, v30\n"
    "s_waitcnt lgkmcnt(12)\n"
    "v_mfma_f32_16x16x32_bf16 v[36:39], a[248:251], a[88:91], v[36:39]\n"
    "v_cvt_pk_bf16_f32 v26, v28, v29\n"
    "v_add_f32 v28, v109, v28\n"
    "v_mfma_f32_16x16x32_bf16 v[36:39], a[252:255], a[92:95], v[36:39]\n"
    "v_exp_f32 v31, v31\n"
    "v_add_f32 v28, v28, v29\n"
    "v_add_f32 v28, v28, v30\n"
    "v_cvt_pk_bf16_f32 v27, v30, v31\n"
    "v_add_f32 v28, v28, v31\n"
    "v_max_f32 v109, v20, v28\n"
    "v_pk_mul_f32 v[68:69], v[68:69], v[112:113] op_sel_hi:[1,0]\n"
    "v_pk_mul_f32 v[70:71], v[70:71], v[112:113] op_sel_hi:[1,0]\n"
    "v_cvt_pk_bf16_f32 v68, v68, v69\n"
    "v_mfma_f32_16x16x32_bf16 v[44:47], a[248:251], a[120:123], v[44:47]\n"
    "ds_read_b64_tr_b16 a[248:249], v97 offset:17920\n"
    "v_cvt_pk_bf16_f32 v69, v70, v71\n"
    "v_mfma_f32_16x16x32_bf16 v[44:47], a[252:255], a[124:127], v[44:47]\n"
    "buffer_store_dwordx4 v[80:83], v110, s[28:31], s93 offen offset:384 sc1\n"
    "v_cmp_le_f32 vcc, 1.0, v109\n"
    "v_pk_mul_f32 v[84:85], v[84:85], v[112:113] op_sel_hi:[1,0]\n"
    "v_pk_mul_f32 v[86:87], v[86:87], v[112:113] op_sel_hi:[1,0]\n"
    "v_cvt_pk_bf16_f32 v84, v84, v85\n"
    "s_waitcnt lgkmcnt(9)\n"
    "v_mfma_f32_16x16x32_bf16 v[136:139], a[208:211], v[48:51], v[136:139]\n"
    "ds_read_b64_tr_b16 a[250:251], v97 offset:26112\n"
    "ds_read_b64_tr_b16 a[252:253], v98 offset:17920\n"
    "s_cbranch_vccnz L_ra14\n"
    "L_ra14_ret:\n"
    "ds_read_b64_tr_b16 a[254:255], v98 offset:26112\n"
    "v_cvt_pk_bf16_f32 v85, v86, v87\n"
    "v_mfma_f32_16x16x32_bf16 v[140:143], a[208:211], v[56:59], v[140:143]\n"
    "ds_write2st64_b64 v111, v[68:69], v[84:85] offset0:29 offset1:31\n"
    "ds_read_b128 a[228:231], v113 offset:14336\n"
    "v_mfma_f32_16x16x32_bf16 v[156:159], a[212:215], v[56:59], v[156:159]\n"
    "ds_read_b64_tr_b16 a[208:209], v97 offset:18432\n"
    "ds_read_b64_tr_b16 a[210:211], v97 offset:26624\n"
    "ds_read_b128 v[84:87], v113 offset:14592\n"
    "v_add_f32 v107, v107, v20\n"
    "v_add_f32 v108, v108, v28\n"
    "v_fma_f32 v32, v32, s39, -v103\n"
    "v_mfma_f32_16x16x32_bf16 v[152:155], a[212:215], v[48:51], v[152:155]\n"
    "ds_read_b64_tr_b16 a[212:213], v98 offset:18432\n"
    "ds_read_b64_tr_b16 a[214:215], v98 offset:26624\n"
    "v_fma_f32 v33, v33, s39, -v103\n"
    "s_waitcnt lgkmcnt(15)\n"
    "v_mfma_f32_16x16x32_bf16 v[168:171], a[216:219], v[48:51], v[168:171]\n"
    "v_fma_f32 v34, v34, s39, -v103\n"
    "v_fma_f32 v35, v35, s39, -v103\n"
    "v_exp_f32 v32, v32\n"
    "v_mfma_f32_16x16x32_bf16 v[172:175], a[216:219], v[56:59], v[172:175]\n"
    "s_waitcnt lgkmcnt(5)\n"
    "buffer_store_dwordx4 a[228:231], v110, s[28:31], s94 offen offset:384 sc1\n"
    "v_mfma_f32_16x16x32_bf16 v[188:191], a[220:223], v[56:59], v[188:191]\n"
    "ds_read_b64_tr_b16 a[216:217], v97 offset:18944\n"
    "ds_read_b64_tr_b16 a[218:219], v97 offset:27136\n"
    "v_fma_f32 v36, v36, s39, -v103\n"
    "v_mfma_f32_16x16x32_bf16 v[184:187], a[220:223], v[48:51], v[184:187]\n"
    "ds_read_b64_tr_b16 a[220:221], v98 offset:18944\n"
    "ds_read_b64_tr_b16 a[222:223], v98 offset:27136\n"
    "v_fma_f32 v37, v37, s39, -v103\n"
    "v_mfma_f32_16x16x32_bf16 v[200:203], a[240:243], v[48:51], v[200:203]\n"
    "v_exp_f32 v33, v33\n"
    "v_exp_f32 v34, v34\n"
    "v_mfma_f32_16x16x32_bf16 v[204:207], a[240:243], v[56:59], v[204:207]\n"
    "ds_read_b64_tr_b16 a[240:241], v97 offset:19456\n"
    "ds_read_b64_tr_b16 a[242:243], v97 offset:27648\n"
    "v_exp_f32 v35, v35\n"
    "v_mfma_f32_16x16x32_bf16 v[220:223], a[244:247], v[56:59], v[220:223]\n"
    "s_waitcnt lgkmcnt(8)\n"
    "buffer_store_dwordx4 v[84:87], v110, s[28:31], s95 offen offset:384 sc1\n"
    "v_mfma_f32_16x16x32_bf16 v[216:219], a[244:247], v[48:51], v[216:219]\n"
    "ds_read_b64_tr_b16 a[244:245], v98 offset:19456\n"
    "ds_read_b64_tr_b16 a[246:247], v98 offset:27648\n"
    "v_add_f32 v109, v32, v33\n"
    "v_mfma_f32_16x16x32_bf16 v[232:235], a[248:251], v[48:51], v[232:235]\n"
    "v_cvt_pk_bf16_f32 v32, v32, v33\n"
    "v_add_f32 v109, v109, v34\n"
    "v_add_f32 v109, v109, v35\n"
    "v_cvt_pk_bf16_f32 v33, v34, v35\n"
    "v_mfma_f32_16x16x32_bf16 v[236:239], a[248:251], v[56:59], v[236:239]\n"
    "ds_read_b64_tr_b16 a[248:249], v97 offset:19968\n"
    "ds_read_b64_tr_b16 a[250:251], v97 offset:28160\n"
    "v_fma_f32 v38, v38, s39, -v103\n"
    "v_fma_f32 v39, v39, s39, -v103\n"
    "v_mfma_f32_16x16x32_bf16 v[252:255], a[252:255], v[56:59], v[252:255]\n"
    "v_exp_f32 v36, v36\n"
    "v_exp_f32 v37, v37\n"
    "v_mfma_f32_16x16x32_bf16 v[248:251], a[252:255], v[48:51], v[248:251]\n"
    "ds_read_b64_tr_b16 a[252:253], v98 offset:19968\n"
    "ds_read_b64_tr_b16 a[254:255], v98 offset:28160\n"
    "s_waitcnt lgkmcnt(12)\n"
    "v_mfma_f32_16x16x32_bf16 a[136:139], a[208:211], v[48:51], a[136:139]\n"
    "v_exp_f32 v38, v38\n"
    "v_cvt_pk_bf16_f32 v34, v36, v37\n"
    "v_add_f32 v36, v109, v36\n"
    "v_mfma_f32_16x16x32_bf16 a[140:143], a[208:211], v[56:59], a[140:143]\n"
    "ds_read_b64_tr_b16 a[208:209], v97 offset:34816\n"
    "ds_read_b64_tr_b16 a[210:211], v97 offset:43008\n"
    "v_exp_f32 v39, v39\n"
    "v_mfma_f32_16x16x32_bf16 a[156:159], a[212:215], v[56:59], a[156:159]\n"
    "v_add_f32 v36, v36, v37\n"
    "v_add_f32 v36, v36, v38\n"
    "v_cvt_pk_bf16_f32 v35, v38, v39\n"
    "v_add_f32 v36, v36, v39\n"
    "v_mfma_f32_16x16x32_bf16 a[152:155], a[212:215], v[48:51], a[152:155]\n"
    "ds_read_b64_tr_b16 a[212:213], v98 offset:34816\n"
    "ds_read_b64_tr_b16 a[214:215], v98 offset:43008\n"
    "v_fma_f32 v40, v40, s39, -v104\n"
    "s_waitcnt lgkmcnt(12)\n"
    "v_mfma_f32_16x16x32_bf16 a[168:171], a[216:219], v[48:51], a[168:171]\n"
    "v_fma_f32 v41, v41, s39, -v104\n"
    "v_fma_f32 v42, v42, s39, -v104\n"
    "v_fma_f32 v43, v43, s39, -v104\n"
    "v_fma_f32 v44, v44, s39, -v104\n"
    "v_mfma_f32_16x16x32_bf16 a[172:175], a[216:219], v[56:59], a[172:175]\n"
    "ds_read_b64_tr_b16 a[216:217], v97 offset:35328\n"
    "ds_read_b64_tr_b16 a[218:219], v97 offset:43520\n"
    "v_exp_f32 v40, v40\n"
    "v_mfma_f32_16x16x32_bf16 a[188:191], a[220:223], v[56:59], a[188:191]\n"
    "v_exp_f32 v41, v41\n"
    "v_exp_f32 v42, v42\n"
    "v_mfma_f32_16x16x32_bf16 a[184:187], a[220:223], v[48:51], a[184:187]\n"
    "ds_read_b64_tr_b16 a[220:221], v98 offset:35328\n"
    "ds_read_b64_tr_b16 a[222:223], v98 offset:43520\n"
    "v_add_f32 v109, v40, v41\n"
    "s_waitcnt lgkmcnt(12)\n"
    "v_mfma_f32_16x16x32_bf16 a[200:203], a[240:243], v[48:51], a[200:203]\n"
    "v_exp_f32 v43, v43\n"
    "v_cvt_pk_bf16_f32 v40, v40, v41\n"
    "v_mfma_f32_16x16x32_bf16 a[204:207], a[240:243], v[56:59], a[204:207]\n"
    "ds_read_b64_tr_b16 a[240:241], v97 offset:2048\n"
    "ds_read_b64_tr_b16 a[242:243], v97 offset:10240\n"
    "v_add_f32 v109, v109, v42\n"
    "v_add_f32 v109, v109, v43\n"
    "v_mfma_f32_16x16x32_bf16 v[76:79], a[244:247], v[56:59], v[76:79]\n"
    "v_cvt_pk_bf16_f32 v41, v42, v43\n"
    "v_fma_f32 v45, v45, s39, -v104\n"
    "v_fma_f32 v46, v46, s39, -v104\n"
    "v_fma_f32 v47, v47, s39, -v104\n"
    "v_mfma_f32_16x16x32_bf16 v[72:75], a[244:247], v[48:51], v[72:75]\n"
    "ds_read_b64_tr_b16 a[244:245], v98 offset:2048\n"
    "ds_read_b64_tr_b16 a[246:247], v98 offset:10240\n"
    "s_waitcnt lgkmcnt(12)\n"
    "v_mfma_f32_16x16x32_bf16 a[232:235], a[248:251], v[48:51], a[232:235]\n"
    "v_exp_f32 v44, v44\n"
    "v_exp_f32 v45, v45\n"
    "s_nop 0\n"
    "v_cvt_pk_bf16_f32 v42, v44, v45\n"
    "v_mfma_f32_16x16x32_bf16 a[236:239], a[248:251], v[56:59], a[236:239]\n"
    "ds_read_b64_tr_b16 a[248:249], v97 offset:2560\n"
    "ds_read_b64_tr_b16 a[250:251], v97 offset:10752\n"
    "v_exp_f32 v46, v46\n"
    "v_mfma_f32_16x16x32_bf16 v[92:95], a[252:255], v[56:59], v[92:95]\n"
    "v_add_f32 v44, v109, v44\n"
    "v_exp_f32 v47, v47\n"
    "v_add_f32 v44, v44, v45\n"
    "v_mfma_f32_16x16x32_bf16 v[88:91], a[252:255], v[48:51], v[88:91]\n"
    "ds_read_b64_tr_b16 a[252:253], v98 offset:2560\n"
    "v_add_f32 v44, v44, v46\n"
    "v_cvt_pk_bf16_f32 v43, v46, v47\n"
    "s_waitcnt lgkmcnt(11)\n"
    "v_mfma_f32_16x16x32_bf16 a[136:139], a[208:211], v[16:19], a[136:139]\n"
    "ds_read_b64_tr_b16 a[254:255], v98 offset:10752\n"
    "v_add_f32 v44, v44, v47\n"
    "v_max_f32 v109, v36, v44\n"
    "v_cmp_le_f32 vcc, 1.0, v109\n"
    "v_mfma_f32_16x16x32_bf16 a[140:143], a[208:211], v[24:27], a[140:143]\n"
    "s_cbranch_vccnz L_ra15\n"
    "L_ra15_ret:\n"
    "ds_read_b64_tr_b16 a[208:209], v97 offset:35840\n"
    "ds_read_b64_tr_b16 a[210:211], v97 offset:44032\n"
    "v_mfma_f32_16x16x32_bf16 a[156:159], a[212:215], v[24:27], a[156:159]\n"
    "v_add_f32 v107, v107, v36\n"
    "v_add_f32 v108, v108, v44\n"
    "v_mov_b32 v124, v107\n"
    "v_mov_b32 v125, v107\n"
    "v_mfma_f32_16x16x32_bf16 a[152:155], a[212:215], v[16:19], a[152:155]\n"
    "ds_read_b64_tr_b16 a[212:213], v98 offset:35840\n"
    "ds_read_b64_tr_b16 a[214:215], v98 offset:44032\n"
    "v_permlane32_swap_b32 v124, v125\n"
    "s_waitcnt lgkmcnt(12)\n"
    "v_mfma_f32_16x16x32_bf16 a[168:171], a[216:219], v[16:19], a[168:171]\n"
    "v_add_f32 v124, v124, v125\n"
    "v_mov_b32 v125, v124\n"
    "s_nop 1\n"
    "v_permlane16_swap_b32 v124, v125\n"
    "v_add_f32 v124, v124, v125\n"
    "v_mfma_f32_16x16x32_bf16 a[172:175], a[216:219], v[24:27], a[172:175]\n"
    "ds_read_b64_tr_b16 a[216:217], v97 offset:36352\n"
    "ds_read_b64_tr_b16 a[218:219], v97 offset:44544\n"
    "v_mfma_f32_16x16x32_bf16 a[188:191], a[220:223], v[24:27], a[188:191]\n"
    "v_rcp_f32 v124, v124\n"
    "v_mov_b32 v126, v108\n"
    "v_mov_b32 v125, v108\n"
    "s_nop 1\n"
    "v_permlane32_swap_b32 v126, v125\n"
    "v_mfma_f32_16x16x32_bf16 a[184:187], a[220:223], v[16:19], a[184:187]\n"
    "ds_read_b64_tr_b16 a[220:221], v98 offset:36352\n"
    "ds_read_b64_tr_b16 a[222:223], v98 offset:44544\n"
    "s_waitcnt lgkmcnt(12)\n"
    "v_mfma_f32_16x16x32_bf16 a[136:139], a[240:243], v[32:35], a[136:139]\n"
    "v_add_f32 v126, v126, v125\n"
    "v_mov_b32 v125, v126\n"
    "s_nop 1\n"
    "v_permlane16_swap_b32 v126, v125\n"
    "v_add_f32 v126, v126, v125\n"
    "v_rcp_f32 v126, v126\n"
    "v_mfma_f32_16x16x32_bf16 a[140:143], a[240:243], v[40:43], a[140:143]\n"
    "ds_read_b64_tr_b16 a[240:241], v97 offset:3072\n"
    "ds_read_b64_tr_b16 a[242:243], v97 offset:11264\n"
    "v_mfma_f32_16x16x32_bf16 a[156:159], a[244:247], v[40:43], a[156:159]\n"
    "v_mfma_f32_16x16x32_bf16 a[152:155], a[244:247], v[32:35], a[152:155]\n"
    "ds_read_b64_tr_b16 a[244:245], v98 offset:3072\n"
    "ds_read_b64_tr_b16 a[246:247], v98 offset:11264\n"
    "s_waitcnt lgkmcnt(12)\n"
    "v_mfma_f32_16x16x32_bf16 a[168:171], a[248:251], v[32:35], a[168:171]\n"
    "v_accvgpr_read_b32 v114, a136\n"
    "v_accvgpr_read_b32 v115, a137\n"
    "v_accvgpr_read_b32 v116, a138\n"
    "v_accvgpr_read_b32 v117, a139\n"
    "v_mul_f32 v114, v114, v124\n"
    "v_mul_f32 v115, v115, v124\n"
    "v_pk_mul_f32 v[116:117], v[116:117], v[124:125] op_sel_hi:[1,0]\n"
    "v_cvt_pk_bf16_f32 v114, v114, v115\n"
    "v_mfma_f32_16x16x32_bf16 a[172:175], a[248:251], v[40:43], a[172:175]\n"
    "ds_read_b64_tr_b16 a[248:249], v97 offset:3584\n"
    "ds_read_b64_tr_b16 a[250:251], v97 offset:11776\n"
    "v_cvt_pk_bf16_f32 v115, v116, v117\n"
    "v_mfma_f32_16x16x32_bf16 a[188:191], a[252:255], v[40:43], a[188:191]\n"
    "v_mfma_f32_16x16x32_bf16 a[184:187], a[252:255], v[32:35], a[184:187]\n"
    "ds_read_b64_tr_b16 a[252:253], v98 offset:3584\n"
    "ds_read_b64_tr_b16 a[254:255], v98 offset:11776\n"
    "s_waitcnt lgkmcnt(12)\n"
    "v_mfma_f32_16x16x32_bf16 a[200:203], a[208:211], v[16:19], a[200:203]\n"
    "v_accvgpr_read_b32 v118, a168\n"
    "v_accvgpr_read_b32 v119, a169\n"
    "v_accvgpr_read_b32 v120, a170\n"
    "v_accvgpr_read_b32 v121, a171\n"
    "v_mul_f32 v118, v118, v124\n"
    "v_mul_f32 v119, v119, v124\n"
    "v_pk_mul_f32 v[120:121], v[120:121], v[124:125] op_sel_hi:[1,0]\n"
    "v_cvt_pk_bf16_f32 v118, v118, v119\n"
    "v_mfma_f32_16x16x32_bf16 a[204:207], a[208:211], v[24:27], a[204:207]\n"
    "ds_read_b64_tr_b16 a[208:209], v97 offset:32768\n"
    "ds_read_b64_tr_b16 a[210:211], v97 offset:40960\n"
    "v_cvt_pk_bf16_f32 v119, v120, v121\n"
    "v_mfma_f32_16x16x32_bf16 v[76:79], a[212:215], v[24:27], v[76:79]\n"
    "ds_write2st64_b64 v123, v[114:115], v[118:119] offset0:16 offset1:18\n"
    "v_accvgpr_read_b32 v114, a152\n"
    "v_accvgpr_read_b32 v115, a153\n"
    "v_accvgpr_read_b32 v116, a154\n"
    "v_accvgpr_read_b32 v117, a155\n"
    "v_pk_mul_f32 v[114:115], v[114:115], v[124:125] op_sel_hi:[1,0]\n"
    "v_pk_mul_f32 v[116:117], v[116:117], v[124:125] op_sel_hi:[1,0]\n"
    "v_cvt_pk_bf16_f32 v114, v114, v115\n"
    "v_cvt_pk_bf16_f32 v115, v116, v117\n"
    "v_mfma_f32_16x16x32_bf16 v[72:75], a[212:215], v[16:19], v[72:75]\n"
    "ds_read_b64_tr_b16 a[212:213], v98 offset:32768\n"
    "ds_read_b64_tr_b16 a[214:215], v98 offset:40960\n"
    "v_accvgpr_read_b32 v118, a184\n"
    "v_accvgpr_read_b32 v119, a185\n"
    "v_accvgpr_read_b32 v120, a186\n"
    "v_accvgpr_read_b32 v121, a187\n"
    "v_pk_mul_f32 v[118:119], v[118:119], v[124:125] op_sel_hi:[1,0]\n"
    "v_pk_mul_f32 v[120:121], v[120:121], v[124:125] op_sel_hi:[1,0]\n"
    "v_cvt_pk_bf16_f32 v118, v118, v119\n"
    "v_cvt_pk_bf16_f32 v119, v120, v121\n"
    "s_waitcnt lgkmcnt(13)\n"
    "v_mfma_f32_16x16x32_bf16 a[232:235], a[216:219], v[16:19], a[232:235]\n"
    "ds_write2st64_b64 v111, v[114:115], v[118:119] offset0:17 offset1:19\n"
    "v_accvgpr_read_b32 v114, a140\n"
    "v_accvgpr_read_b32 v115, a141\n"
    "v_accvgpr_read_b32 v116, a142\n"
    "v_accvgpr_read_b32 v117, a143\n"
    "v_accvgpr_read_b32 v118, a172\n"
    "v_accvgpr_read_b32 v119, a173\n"
    "v_accvgpr_read_b32 v120, a174\n"
    "v_pk_mul_f32 v[114:115], v[114:115], v[126:127] op_sel_hi:[1,0]\n"
    "v_pk_mul_f32 v[116:117], v[116:117], v[126:127] op_sel_hi:[1,0]\n"
    "v_cvt_pk_bf16_f32 v114, v114, v115\n"
    "v_cvt_pk_bf16_f32 v115, v116, v117\n"
    "v_mfma_f32_16x16x32_bf16 a[236:239], a[216:219], v[24:27], a[236:239]\n"
    "ds_read_b64_tr_b16 a[216:217], v97 offset:33280\n"
    "ds_read_b64_tr_b16 a[218:219], v97 offset:41472\n"
    "ds_read_b128 a[168:171], v113 offset:8192\n"
    "ds_read_b128 a[184:187], v113 offset:8448\n"
    "v_accvgpr_read_b32 v121, a175\n"
    "v_pk_mul_f32 v[118:119], v[118:119], v[126:127] op_sel_hi:[1,0]\n"
    "v_pk_mul_f32 v[120:121], v[120:121], v[126:127] op_sel_hi:[1,0]\n"
    "v_cvt_pk_bf16_f32 v118, v118, v119\n"
    "v_cvt_pk_bf16_f32 v119, v120, v121\n"
    "v_mfma_f32_16x16x32_bf16 v[92:95], a[220:223], v[24:27], v[92:95]\n"
    "ds_write2st64_b64 v123, v[114:115], v[118:119] offset0:20 offset1:22\n"
    "v_accvgpr_read_b32 v114, a156\n"
    "v_accvgpr_read_b32 v115, a157\n"
    "v_accvgpr_read_b32 v116, a158\n"
    "v_accvgpr_read_b32 v117, a159\n"
    "v_accvgpr_read_b32 v118, a188\n"
    "v_accvgpr_read_b32 v119, a189\n"
    "v_accvgpr_read_b32 v120, a190\n"
    "v_pk_mul_f32 v[114:115], v[114:115], v[126:127] op_sel_hi:[1,0]\n"
    "v_pk_mul_f32 v[116:117], v[116:117], v[126:127] op_sel_hi:[1,0]\n"
    "v_cvt_pk_bf16_f32 v114, v114, v115\n"
    "v_cvt_pk_bf16_f32 v115, v116, v117\n"
    "v_mfma_f32_16x16x32_bf16 v[88:91], a[220:223], v[16:19], v[88:91]\n"
    "ds_read_b64_tr_b16 a[220:221], v98 offset:33280\n"
    "ds_read_b64_tr_b16 a[222:223], v98 offset:41472\n"
    "v_accvgpr_read_b32 v121, a191\n"
    "v_mul_f32 v118, v118, v126\n"
    "v_mul_f32 v119, v119, v126\n"
    "v_pk_mul_f32 v[120:121], v[120:121], v[126:127] op_sel_hi:[1,0]\n"
    "v_cvt_pk_bf16_f32 v118, v118, v119\n"
    "s_waitcnt lgkmcnt(15)\n"
    "v_mfma_f32_16x16x32_bf16 a[200:203], a[240:243], v[32:35], a[200:203]\n"
    "v_cvt_pk_bf16_f32 v119, v120, v121\n"
    "v_mfma_f32_16x16x32_bf16 a[204:207], a[240:243], v[40:43], a[204:207]\n"
    "ds_read_b64_tr_b16 a[240:241], v97 offset:0\n"
    "ds_read_b64_tr_b16 a[242:243], v97 offset:8192\n"
    "ds_write2st64_b64 v111, v[114:115], v[118:119] offset0:21 offset1:23\n"
    "ds_read_b128 a[172:175], v113 offset:10240\n"
    "ds_read_b128 a[188:191], v113 offset:10496\n"
    "s_waitcnt lgkmcnt(9)\n"
    "buffer_store_dwordx4 a[168:171], v110, s[28:31], s96 offen offset:256 sc1\n"
    "v_mfma_f32_16x16x32_bf16 v[76:79], a[244:247], v[40:43], v[76:79]\n"
    "s_waitcnt lgkmcnt(8)\n"
    "buffer_store_dwordx4 a[184:187], v110, s[28:31], s97 offen offset:256 sc1\n"
    "v_mfma_f32_16x16x32_bf16 v[72:75], a[244:247], v[32:35], v[72:75]\n"
    "ds_read_b64_tr_b16 a[244:245], v98 offset:0\n"
    "ds_read_b64_tr_b16 a[246:247], v98 offset:8192\n"
    "v_mfma_f32_16x16x32_bf16 a[232:235], a[248:251], v[32:35], a[232:235]\n"
    "v_accvgpr_read_b32 v114, a200\n"
    "v_accvgpr_read_b32 v115, a201\n"
    "v_accvgpr_read_b32 v116, a202\n"
    "v_accvgpr_read_b32 v117, a203\n"
    "v_mul_f32 v114, v114, v124\n"
    "v_mul_f32 v115, v115, v124\n"
    "v_pk_mul_f32 v[116:117], v[116:117], v[124:125] op_sel_hi:[1,0]\n"
    "v_cvt_pk_bf16_f32 v114, v114, v115\n"
    "v_mfma_f32_16x16x32_bf16 a[236:239], a[248:251], v[40:43], a[236:239]\n"
    "ds_read_b64_tr_b16 a[248:249], v97 offset:512\n"
    "ds_read_b64_tr_b16 a[250:251], v97 offset:8704\n"
    "s_waitcnt lgkmcnt(5)\n"
    "buffer_store_dwordx4 a[172:175], v110, s[28:31], s98 offen offset:256 sc1\n"
    "s_waitcnt lgkmcnt(4)\n"
    "buffer_store_dwordx4 a[188:191], v110, s[28:31], s99 offen offset:256 sc1\n"
    "v_cvt_pk_bf16_f32 v115, v116, v117\n"
    "v_mfma_f32_16x16x32_bf16 v[92:95], a[252:255], v[40:43], v[92:95]\n"
    "v_mul_f32 v76, v76, v126\n"
    "v_mul_f32 v77, v77, v126\n"
    "v_mul_f32 v78, v78, v126\n"
    "v_mul_f32 v79, v79, v126\n"
    "v_cvt_pk_bf16_f32 v76, v76, v77\n"
    "v_mfma_f32_16x16x32_bf16 v[88:91], a[252:255], v[32:35], v[88:91]\n"
    "ds_read_b64_tr_b16 a[252:253], v98 offset:512\n"
    "ds_read_b64_tr_b16 a[254:255], v98 offset:8704\n"
    "v_cvt_pk_bf16_f32 v77, v78, v79\n"
    "v_mul_f32 v72, v72, v124\n"
    "v_mul_f32 v73, v73, v124\n"
    "v_pk_mul_f32 v[74:75], v[74:75], v[124:125] op_sel_hi:[1,0]\n"
    "v_cvt_pk_bf16_f32 v72, v72, v73\n"
    "v_mfma_f32_16x16x32_bf16 v[136:139], a[208:211], v[16:19], v[136:139]\n"
    "v_accvgpr_read_b32 v118, a232\n"
    "v_accvgpr_read_b32 v119, a233\n"
    "v_accvgpr_read_b32 v120, a234\n"
    "v_accvgpr_read_b32 v121, a235\n"
    "v_cvt_pk_bf16_f32 v73, v74, v75\n"
    "v_pk_mul_f32 v[118:119], v[118:119], v[124:125] op_sel_hi:[1,0]\n"
    "v_pk_mul_f32 v[120:121], v[120:121], v[124:125] op_sel_hi:[1,0]\n"
    "v_cvt_pk_bf16_f32 v118, v118, v119\n"
    "v_cvt_pk_bf16_f32 v119, v120, v121\n"
    "v_mfma_f32_16x16x32_bf16 v[140:143], a[208:211], v[24:27], v[140:143]\n"
    "ds_read_b64_tr_b16 a[208:209], v97 offset:33792\n"
    "ds_read_b64_tr_b16 a[210:211], v97 offset:41984\n"
    "ds_write2st64_b64 v123, v[114:115], v[118:119] offset0:24 offset1:26\n"
    "v_accvgpr_read_b32 v114, a204\n"
    "v_accvgpr_read_b32 v115, a205\n"
    "v_accvgpr_read_b32 v116, a206\n"
    "v_accvgpr_read_b32 v117, a207\n"
    "v_accvgpr_read_b32 v118, a236\n"
    "v_pk_mul_f32 v[114:115], v[114:115], v[126:127] op_sel_hi:[1,0]\n"
    "v_pk_mul_f32 v[116:117], v[116:117], v[126:127] op_sel_hi:[1,0]\n"
    "v_cvt_pk_bf16_f32 v114, v114, v115\n"
    "v_cvt_pk_bf16_f32 v115, v116, v117\n"
    "v_mfma_f32_16x16x32_bf16 v[156:159], a[212:215], v[24:27], v[156:159]\n"
    "v_accvgpr_read_b32 v119, a237\n"
    "v_accvgpr_read_b32 v120, a238\n"
    "v_accvgpr_read_b32 v121, a239\n"
    "v_mul_f32 v118, v118, v126\n"
    "v_mul_f32 v119, v119, v126\n"
    "v_pk_mul_f32 v[120:121], v[120:121], v[126:127] op_sel_hi:[1,0]\n"
    "v_pk_mul_f32 v[92:93], v[92:93], v[126:127] op_sel_hi:[1,0]\n"
    "v_pk_mul_f32 v[94:95], v[94:95], v[126:127] op_sel_hi:[1,0]\n"
    "v_cvt_pk_bf16_f32 v118, v118, v119\n"
    "v_mfma_f32_16x16x32_bf16 v[152:155], a[212:215], v[16:19], v[152:155]\n"
    "ds_read_b64_tr_b16 a[212:213], v98 offset:33792\n"
    "ds_read_b64_tr_b16 a[214:215], v98 offset:41984\n"
    "v_cvt_pk_bf16_f32 v119, v120, v121\n"
    "v_cvt_pk_bf16_f32 v92, v92, v93\n"
    "v_cvt_pk_bf16_f32 v93, v94, v95\n"
    "v_pk_mul_f32 v[88:89], v[88:89], v[124:125] op_sel_hi:[1,0]\n"
    "v_pk_mul_f32 v[90:91], v[90:91], v[124:125] op_sel_hi:[1,0]\n"
    "v_cvt_pk_bf16_f32 v88, v88, v89\n"
    "v_cvt_pk_bf16_f32 v89, v90, v91\n"
    "v_mfma_f32_16x16x32_bf16 v[168:171], a[216:219], v[16:19], v[168:171]\n"
    "ds_write2st64_b64 v111, v[72:73], v[88:89] offset0:25 offset1:27\n"
    "ds_read_b128 a[232:235], v113 offset:12288\n"
    "ds_read_b128 v[88:91], v113 offset:12544\n"
    "v_mfma_f32_16x16x32_bf16 v[172:175], a[216:219], v[24:27], v[172:175]\n"
    "ds_read_b64_tr_b16 a[216:217], v97 offset:34304\n"
    "ds_read_b64_tr_b16 a[218:219], v97 offset:42496\n"
    "v_mfma_f32_16x16x32_bf16 v[188:191], a[220:223], v[24:27], v[188:191]\n"
    "ds_write2st64_b64 v123, v[114:115], v[118:119] offset0:28 offset1:30\n"
    "v_mfma_f32_16x16x32_bf16 v[184:187], a[220:223], v[16:19], v[184:187]\n"
    "ds_read_b64_tr_b16 a[220:221], v98 offset:34304\n"
    "ds_read_b64_tr_b16 a[222:223], v98 offset:42496\n"
    "s_waitcnt lgkmcnt(15)\n"
    "v_mfma_f32_16x16x32_bf16 v[136:139], a[240:243], v[32:35], v[136:139]\n"
    "s_waitcnt lgkmcnt(6)\n"
    "buffer_store_dwordx4 a[232:235], v110, s[28:31], s96 offen offset:384 sc1\n"
    "s_waitcnt lgkmcnt(5)\n"
    "buffer_store_dwordx4 v[88:91], v110, s[28:31], s97 offen offset:384 sc1\n"
    "v_mfma_f32_16x16x32_bf16 v[140:143], a[240:243], v[40:43], v[140:143]\n"
    "ds_read_b64_tr_b16 a[240:241], v97 offset:1024\n"
    "ds_read_b64_tr_b16 a[242:243], v97 offset:9216\n"
    "ds_write2st64_b64 v111, v[76:77], v[92:93] offset0:29 offset1:31\n"
    "ds_read_b128 a[236:239], v113 offset:14336\n"
    "ds_read_b128 v[92:95], v113 offset:14592\n"
    "v_mfma_f32_16x16x32_bf16 v[156:159], a[244:247], v[40:43], v[156:159]\n"
    "v_mfma_f32_16x16x32_bf16 v[152:155], a[244:247], v[32:35], v[152:155]\n"
    "ds_read_b64_tr_b16 a[244:245], v98 offset:1024\n"
    "ds_read_b64_tr_b16 a[246:247], v98 offset:9216\n"
    "v_mfma_f32_16x16x32_bf16 v[168:171], a[248:251], v[32:35], v[168:171]\n"
    "v_mul_f32 v136, v136, v124\n"
    "v_mul_f32 v137, v137, v124\n"
    "v_mul_f32 v138, v138, v124\n"
    "v_mul_f32 v139, v139, v124\n"
    "v_cvt_pk_bf16_f32 v136, v136, v137\n"
    "v_mfma_f32_16x16x32_bf16 v[172:175], a[248:251], v[40:43], v[172:175]\n"
    "ds_read_b64_tr_b16 a[248:249], v97 offset:1536\n"
    "ds_read_b64_tr_b16 a[250:251], v97 offset:9728\n"
    "s_waitcnt lgkmcnt(5)\n"
    "buffer_store_dwordx4 a[236:239], v110, s[28:31], s98 offen offset:384 sc1\n"
    "s_waitcnt lgkmcnt(4)\n"
    "buffer_store_dwordx4 v[92:95], v110, s[28:31], s99 offen offset:384 sc1\n"
    "v_cvt_pk_bf16_f32 v137, v138, v139\n"
    "v_pk_mul_f32 v[140:141], v[140:141], v[126:127] op_sel_hi:[1,0]\n"
    "v_pk_mul_f32 v[142:143], v[142:143], v[126:127] op_sel_hi:[1,0]\n"
    "v_cvt_pk_bf16_f32 v140, v140, v141\n"
    "v_cvt_pk_bf16_f32 v141, v142, v143\n"
    "v_mfma_f32_16x16x32_bf16 v[188:191], a[252:255], v[40:43], v[188:191]\n"
    "v_mul_f32 v156, v156, v126\n"
    "v_mul_f32 v157, v157, v126\n"
    "v_mul_f32 v158, v158, v126\n"
    "v_mul_f32 v159, v159, v126\n"
    "v_cvt_pk_bf16_f32 v156, v156, v157\n"
    "v_mfma_f32_16x16x32_bf16 v[184:187], a[252:255], v[32:35], v[184:187]\n"
    "ds_read_b64_tr_b16 a[252:253], v98 offset:1536\n"
    "ds_read_b64_tr_b16 a[254:255], v98 offset:9728\n"
    "v_cvt_pk_bf16_f32 v157, v158, v159\n"
    "v_mul_f32 v152, v152, v124\n"
    "v_mul_f32 v153, v153, v124\n"
    "v_pk_mul_f32 v[154:155], v[154:155], v[124:125] op_sel_hi:[1,0]\n"
    "v_cvt_pk_bf16_f32 v152, v152, v153\n"
    "v_mfma_f32_16x16x32_bf16 v[200:203], a[208:211], v[16:19], v[200:203]\n"
    "v_cvt_pk_bf16_f32 v153, v154, v155\n"
    "v_mul_f32 v168, v168, v124\n"
    "v_mul_f32 v169, v169, v124\n"
    "v_mul_f32 v170, v170, v124\n"
    "v_mul_f32 v171, v171, v124\n"
    "v_cvt_pk_bf16_f32 v168, v168, v169\n"
    "v_mfma_f32_16x16x32_bf16 v[204:207], a[208:211], v[24:27], v[204:207]\n"
    "v_cvt_pk_bf16_f32 v169, v170, v171\n"
    "v_mul_f32 v172, v172, v126\n"
    "v_mul_f32 v173, v173, v126\n"
    "v_mul_f32 v174, v174, v126\n"
    "v_mul_f32 v175, v175, v126\n"
    "v_cvt_pk_bf16_f32 v172, v172, v173\n"
    "v_mfma_f32_16x16x32_bf16 v[220:223], a[212:215], v[24:27], v[220:223]\n"
    "ds_write2st64_b64 v123, v[136:137], v[168:169] offset0:0 offset1:2\n"
    "v_cvt_pk_bf16_f32 v173, v174, v175\n"
    "v_pk_mul_f32 v[188:189], v[188:189], v[126:127] op_sel_hi:[1,0]\n"
    "v_pk_mul_f32 v[190:191], v[190:191], v[126:127] op_sel_hi:[1,0]\n"
    "v_cvt_pk_bf16_f32 v188, v188, v189\n"
    "v_cvt_pk_bf16_f32 v189, v190, v191\n"
    "v_mfma_f32_16x16x32_bf16 v[216:219], a[212:215], v[16:19], v[216:219]\n"
    "v_mul_f32 v184, v184, v124\n"
    "v_mul_f32 v185, v185, v124\n"
    "v_mul_f32 v186, v186, v124\n"
    "v_mul_f32 v187, v187, v124\n"
    "v_cvt_pk_bf16_f32 v184, v184, v185\n"
    "v_mfma_f32_16x16x32_bf16 v[232:235], a[216:219], v[16:19], v[232:235]\n"
    "v_cvt_pk_bf16_f32 v185, v186, v187\n"
    "v_mfma_f32_16x16x32_bf16 v[236:239], a[216:219], v[24:27], v[236:239]\n"
    "ds_write2st64_b64 v111, v[152:153], v[184:185] offset0:1 offset1:3\n"
    "ds_read_b128 v[168:171], v113 offset:0\n"
    "ds_read_b128 v[184:187], v113 offset:256\n"
    "v_mfma_f32_16x16x32_bf16 v[252:255], a[220:223], v[24:27], v[252:255]\n"
    "v_mfma_f32_16x16x32_bf16 v[248:251], a[220:223], v[16:19], v[248:251]\n"
    "ds_write2st64_b64 v123, v[140:141], v[172:173] offset0:4 offset1:6\n"
    "s_waitcnt lgkmcnt(9)\n"
    "v_mfma_f32_16x16x32_bf16 v[200:203], a[240:243], v[32:35], v[200:203]\n"
    "v_mfma_f32_16x16x32_bf16 v[204:207], a[240:243], v[40:43], v[204:207]\n"
    "s_waitcnt lgkmcnt(2)\n"
    "buffer_store_dwordx4 v[168:171], v110, s[28:31], s96 offen offset:0 sc1\n"
    "s_waitcnt lgkmcnt(1)\n"
    "buffer_store_dwordx4 v[184:187], v110, s[28:31], s97 offen offset:0 sc1\n"
    "v_mfma_f32_16x16x32_bf16 v[220:223], a[244:247], v[40:43], v[220:223]\n"
    "ds_write2st64_b64 v111, v[156:157], v[188:189] offset0:5 offset1:7\n"
    "ds_read_b128 v[172:175], v113 offset:2048\n"
    "ds_read_b128 v[188:191], v113 offset:2304\n"
    "v_mfma_f32_16x16x32_bf16 v[216:219], a[244:247], v[32:35], v[216:219]\n"
    "v_mfma_f32_16x16x32_bf16 v[232:235], a[248:251], v[32:35], v[232:235]\n"
    "v_mul_f32 v200, v200, v124\n"
    "v_mul_f32 v201, v201, v124\n"
    "v_mul_f32 v202, v202, v124\n"
    "v_mul_f32 v203, v203, v124\n"
    "v_cvt_pk_bf16_f32 v200, v200, v201\n"
    "v_mfma_f32_16x16x32_bf16 v[236:239], a[248:251], v[40:43], v[236:239]\n"
    "v_cvt_pk_bf16_f32 v201, v202, v203\n"
    "v_mul_f32 v204, v204, v126\n"
    "v_mul_f32 v205, v205, v126\n"
    "v_mul_f32 v206, v206, v126\n"
    "v_mul_f32 v207, v207, v126\n"
    "v_cvt_pk_bf16_f32 v204, v204, v205\n"
    "v_mfma_f32_16x16x32_bf16 v[252:255], a[252:255], v[40:43], v[252:255]\n"
    "s_waitcnt lgkmcnt(1)\n"
    "buffer_store_dwordx4 v[172:175], v110, s[28:31], s98 offen offset:0 sc1\n"
    "s_waitcnt lgkmcnt(0)\n"
    "buffer_store_dwordx4 v[188:191], v110, s[28:31], s99 offen offset:0 sc1\n"
    "v_cvt_pk_bf16_f32 v205, v206, v207\n"
    "v_pk_mul_f32 v[220:221], v[220:221], v[126:127] op_sel_hi:[1,0]\n"
    "v_pk_mul_f32 v[222:223], v[222:223], v[126:127] op_sel_hi:[1,0]\n"
    "v_cvt_pk_bf16_f32 v220, v220, v221\n"
    "v_cvt_pk_bf16_f32 v221, v222, v223\n"
    "v_mfma_f32_16x16x32_bf16 v[248:251], a[252:255], v[32:35], v[248:251]\n"
    "v_mul_f32 v216, v216, v124\n"
    "v_mul_f32 v217, v217, v124\n"
    "v_mul_f32 v218, v218, v124\n"
    "v_mul_f32 v219, v219, v124\n"
    "v_cvt_pk_bf16_f32 v216, v216, v217\n"
    "v_pk_mul_f32 v[232:233], v[232:233], v[124:125] op_sel_hi:[1,0]\n"
    "v_pk_mul_f32 v[234:235], v[234:235], v[124:125] op_sel_hi:[1,0]\n"
    "v_cvt_pk_bf16_f32 v232, v232, v233\n"
    "v_cvt_pk_bf16_f32 v233, v234, v235\n"
    "v_cvt_pk_bf16_f32 v217, v218, v219\n"
    "ds_write2st64_b64 v123, v[200:201], v[232:233] offset0:8 offset1:10\n"
    "v_pk_mul_f32 v[236:237], v[236:237], v[126:127] op_sel_hi:[1,0]\n"
    "v_pk_mul_f32 v[238:239], v[238:239], v[126:127] op_sel_hi:[1,0]\n"
    "v_cvt_pk_bf16_f32 v236, v236, v237\n"
    "v_cvt_pk_bf16_f32 v237, v238, v239\n"
    "v_pk_mul_f32 v[252:253], v[252:253], v[126:127] op_sel_hi:[1,0]\n"
    "v_pk_mul_f32 v[254:255], v[254:255], v[126:127] op_sel_hi:[1,0]\n"
    "v_cvt_pk_bf16_f32 v252, v252, v253\n"
    "v_cvt_pk_bf16_f32 v253, v254, v255\n"
    "v_pk_mul_f32 v[248:249], v[248:249], v[124:125] op_sel_hi:[1,0]\n"
    "v_pk_mul_f32 v[250:251], v[250:251], v[124:125] op_sel_hi:[1,0]\n"
    "v_cvt_pk_bf16_f32 v248, v248, v249\n"
    "v_cvt_pk_bf16_f32 v249, v250, v251\n"
    "ds_write2st64_b64 v111, v[216:217], v[248:249] offset0:9 offset1:11\n"
    "ds_read_b128 v[232:235], v113 offset:4096\n"
    "ds_read_b128 v[248:251], v113 offset:4352\n"
    "ds_write2st64_b64 v123, v[204:205], v[236:237] offset0:12 offset1:14\n"
    "ds_write2st64_b64 v111, v[220:221], v[252:253] offset0:13 offset1:15\n"
    "ds_read_b128 v[236:239], v113 offset:6144\n"
    "ds_read_b128 v[252:255], v113 offset:6400\n"
    "s_waitcnt lgkmcnt(5)\n"
    "buffer_store_dwordx4 v[232:235], v110, s[28:31], s96 offen offset:128 sc1\n"
    "s_waitcnt lgkmcnt(4)\n"
    "buffer_store_dwordx4 v[248:251], v110, s[28:31], s97 offen offset:128 sc1\n"
    "s_waitcnt lgkmcnt(1)\n"
    "buffer_store_dwordx4 v[236:239], v110, s[28:31], s98 offen offset:128 sc1\n"
    "s_waitcnt lgkmcnt(0)\n"
    "buffer_store_dwordx4 v[252:255], v110, s[28:31], s99 offen offset:128 sc1\n"
    "s_endpgm\n"
    "L_ra2:\n"
    "s_mov_b32 s71, 0\n"
    "s_sub_u32 s73, s36, 65536\n"
    "s_mov_b32 s79, 0\n"
    "s_getpc_b64 s[74:75]\n"
    "s_branch L_rare1A\n"
    "s_branch L_ra2_ret\n"
    "L_ra3:\n"
    "s_mov_b32 s71, 0\n"
    "s_sub_u32 s73, s36, 65536\n"
    "s_mov_b32 s79, 0\n"
    "s_getpc_b64 s[74:75]\n"
    "s_branch L_rare0A\n"
    "s_branch L_ra3_ret\n"
    "L_ra4:\n"
    "s_mov_b32 s71, 0\n"
    "s_mov_b32 s73, 229376\n"
    "s_mov_b32 s79, 0\n"
    "s_getpc_b64 s[74:75]\n"
    "s_branch L_rare0A\n"
    "s_branch L_ra4_ret\n"
    "L_ra5:\n"
    "s_mov_b32 s71, 0\n"
    "s_mov_b32 s73, 245760\n"
    "s_mov_b32 s79, 0\n"
    "s_getpc_b64 s[74:75]\n"
    "s_branch L_rare1A\n"
    "s_branch L_ra5_ret\n"
    "L_ra6:\n"
    "s_mov_b32 s71, 0\n"
    "s_sub_u32 s73, s36, 65536\n"
    "s_mov_b32 s79, 262144\n"
    "s_getpc_b64 s[74:75]\n"
    "s_branch L_rare1A\n"
    "s_branch L_ra6_ret\n"
    "L_ra7:\n"
    "s_mov_b32 s71, 0\n"
    "s_sub_u32 s73, s36, 65536\n"
    "s_mov_b32 s79, 262144\n"
    "s_getpc_b64 s[74:75]\n"
    "s_branch L_rare0A\n"
    "s_branch L_ra7_ret\n"
    "L_ra8:\n"
    "s_mov_b32 s71, 0\n"
    "s_mov_b32 s73, 442368\n"
    "s_mov_b32 s79, 262144\n"
    "s_getpc_b64 s[74:75]\n"
    "s_branch L_rare1A\n"
    "s_branch L_ra8_ret\n"
    "L_ra9:\n"
    "s_mov_b32 s71, 0\n"
    "s_mov_b32 s73, 458752\n"
    "s_mov_b32 s79, 262144\n"
    "s_getpc_b64 s[74:75]\n"
    "s_branch L_rare0A\n"
    "s_branch L_ra9_ret\n"
    "L_ra10:\n"
    "s_mov_b32 s71, 1\n"
    "s_mov_b32 s73, 475136\n"
    "s_mov_b32 s79, 262144\n"
    "s_getpc_b64 s[74:75]\n"
    "s_branch L_rare1A\n"
    "s_branch L_ra10_ret\n"
    "L_ra11:\n"
    "s_mov_b32 s71, 1\n"
    "s_mov_b32 s73, 491520\n"
    "s_mov_b32 s79, 262144\n"
    "s_getpc_b64 s[74:75]\n"
    "s_branch L_rare0A\n"
    "s_branch L_ra11_ret\n"
    "L_ra12:\n"
    "s_mov_b32 s71, 1\n"
    "s_mov_b32 s73, 507904\n"
    "s_mov_b32 s79, 262144\n"
    "s_getpc_b64 s[74:75]\n"
    "s_branch L_rare1A\n"
    "s_branch L_ra12_ret\n"
    "L_ra13:\n"
    "s_mov_b32 s71, 0\n"
    "s_mov_b32 s73, 475136\n"
    "s_mov_b32 s79, 278528\n"
    "s_getpc_b64 s[74:75]\n"
    "s_branch L_rare1B\n"
    "s_branch L_ra13_ret\n"
    "L_ra14:\n"
    "s_mov_b32 s71, 2\n"
    "s_mov_b32 s73, 491520\n"
    "s_mov_b32 s79, 278528\n"
    "s_getpc_b64 s[74:75]\n"
    "s_branch L_rare0B\n"
    "s_branch L_ra14_ret\n"
    "L_ra15:\n"
    "s_mov_b32 s71, 2\n"
    "s_mov_b32 s73, 507904\n"
    "s_mov_b32 s79, 278528\n"
    "s_getpc_b64 s[74:75]\n"
    "s_branch L_rare2B\n"
    "s_branch L_ra15_ret\n"
    "L_rare0A:\n"
    "s_nop 15\n"
    "v_mbcnt_lo_u32_b32 v126, -1, 0\n"
    "v_mbcnt_hi_u32_b32 v126, -1, v126\n"
    "v_and_b32 v109, 15, v126\n"
    "v_lshlrev_b32 v109, 9, v109\n"
    "v_lshrrev_b32 v126, 4, v126\n"
    "v_lshl_add_u32 v109, v126, 4, v109\n"
    "s_add_u32 s72, s73, 8192\n"
    "s_and_b32 s83, s87, 0x1000\n"
    "s_and_b32 s84, s87, 0x6000\n"
    "s_xor_b32 s84, s84, s79\n"
    "v_xor_b32 v109, s83, v109\n"
    "buffer_load_dwordx4 v[8:11], v109, s[4:7], s84 offen offset:0\n"
    "v_xor_b32 v109, s83, v109\n"
    "buffer_load_dwordx4 v[124:127], v109, s[8:11], s73 offen offset:0\n"
    "s_waitcnt vmcnt(0)\n"
    "v_mfma_f32_16x16x32_bf16 v[0:3], v[124:127], v[8:11], 0\n"
    "s_nop 7\n"
    "buffer_load_dwordx4 v[124:127], v109, s[8:11], s72 offen offset:0\n"
    "s_waitcnt vmcnt(0)\n"
    "v_mfma_f32_16x16x32_bf16 v[4:7], v[124:127], v[8:11], 0\n"
    "s_nop 7\n"
    "v_xor_b32 v109, s83, v109\n"
    "buffer_load_dwordx4 v[8:11], v109, s[4:7], s84 offen offset:64\n"
    "v_xor_b32 v109, s83, v109\n"
    "buffer_load_dwordx4 v[124:127], v109, s[8:11], s73 offen offset:64\n"
    "s_waitcnt vmcnt(0)\n"
    "v_mfma_f32_16x16x32_bf16 v[0:3], v[124:127], v[8:11], v[0:3]\n"
    "s_nop 7\n"
    "buffer_load_dwordx4 v[124:127], v109, s[8:11], s72 offen offset:64\n"
    "s_waitcnt vmcnt(0)\n"
    "v_mfma_f32_16x16x32_bf16 v[4:7], v[124:127], v[8:11], v[4:7]\n"
    "s_nop 7\n"
    "v_xor_b32 v109, s83, v109\n"
    "buffer_load_dwordx4 v[8:11], v109, s[4:7], s84 offen offset:128\n"
    "v_xor_b32 v109, s83, v109\n"
    "buffer_load_dwordx4 v[124:127], v109, s[8:11], s73 offen offset:128\n"
    "s_waitcnt vmcnt(0)\n"
    "v_mfma_f32_16x16x32_bf16 v[0:3], v[124:127], v[8:11], v[0:3]\n"
    "s_nop 7\n"
    "buffer_load_dwordx4 v[124:127], v109, s[8:11], s72 offen offset:128\n"
    "s_waitcnt vmcnt(0)\n"
    "v_mfma_f32_16x16x32_bf16 v[4:7], v[124:127], v[8:11], v[4:7]\n"
    "s_nop 7\n"
    "v_xor_b32 v109, s83, v109\n"
    "buffer_load_dwordx4 v[8:11], v109, s[4:7], s84 offen offset:192\n"
    "v_xor_b32 v109, s83, v109\n"
    "buffer_load_dwordx4 v[124:127], v109, s[8:11], s73 offen offset:192\n"
    "s_waitcnt vmcnt(0)\n"
    "v_mfma_f32_16x16x32_bf16 v[0:3], v[124:127], v[8:11], v[0:3]\n"
    "s_nop 7\n"
    "buffer_load_dwordx4 v[124:127], v109, s[8:11], s72 offen offset:192\n"
    "s_waitcnt vmcnt(0)\n"
    "v_mfma_f32_16x16x32_bf16 v[4:7], v[124:127], v[8:11], v[4:7]\n"
    "s_nop 7\n"
    "v_xor_b32 v109, s83, v109\n"
    "buffer_load_dwordx4 v[8:11], v109, s[4:7], s84 offen offset:256\n"
    "v_xor_b32 v109, s83, v109\n"
    "buffer_load_dwordx4 v[124:127], v109, s[8:11], s73 offen offset:256\n"
    "s_waitcnt vmcnt(0)\n"
    "v_mfma_f32_16x16x32_bf16 v[0:3], v[124:127], v[8:11], v[0:3]\n"
    "s_nop 7\n"
    "buffer_load_dwordx4 v[124:127], v109, s[8:11], s72 offen offset:256\n"
    "s_waitcnt vmcnt(0)\n"
    "v_mfma_f32_16x16x32_bf16 v[4:7], v[124:127], v[8:11], v[4:7]\n"
    "s_nop 7\n"
    "v_xor_b32 v109, s83, v109\n"
    "buffer_load_dwordx4 v[8:11], v109, s[4:7], s84 offen offset:320\n"
    "v_xor_b32 v109, s83, v109\n"
    "buffer_load_dwordx4 v[124:127], v109, s[8:11], s73 offen offset:320\n"
    "s_waitcnt vmcnt(0)\n"
    "v_mfma_f32_16x16x32_bf16 v[0:3], v[124:127], v[8:11], v[0:3]\n"
    "s_nop 7\n"
    "buffer_load_dwordx4 v[124:127], v109, s[8:11], s72 offen offset:320\n"
    "s_waitcnt vmcnt(0)\n"
    "v_mfma_f32_16x16x32_bf16 v[4:7], v[124:127], v[8:11], v[4:7]\n"
    "s_nop 7\n"
    "v_xor_b32 v109, s83, v109\n"
    "buffer_load_dwordx4 v[8:11], v109, s[4:7], s84 offen offset:384\n"
    "v_xor_b32 v109, s83, v109\n"
    "buffer_load_dwordx4 v[124:127], v109, s[8:11], s73 offen offset:384\n"
    "s_waitcnt vmcnt(0)\n"
    "v_mfma_f32_16x16x32_bf16 v[0:3], v[124:127], v[8:11], v[0:3]\n"
    "s_nop 7\n"
    "buffer_load_dwordx4 v[124:127], v109, s[8:11], s72 offen offset:384\n"
    "s_waitcnt vmcnt(0)\n"
    "v_mfma_f32_16x16x32_bf16 v[4:7], v[124:127], v[8:11], v[4:7]\n"
    "s_nop 7\n"
    "v_xor_b32 v109, s83, v109\n"
    "buffer_load_dwordx4 v[8:11], v109, s[4:7], s84 offen offset:448\n"
    "v_xor_b32 v109, s83, v109\n"
    "buffer_load_dwordx4 v[124:127], v109, s[8:11], s73 offen offset:448\n"
    "s_waitcnt vmcnt(0)\n"
    "v_mfma_f32_16x16x32_bf16 v[0:3], v[124:127], v[8:11], v[0:3]\n"
    "s_nop 7\n"
    "buffer_load_dwordx4 v[124:127], v109, s[8:11], s72 offen offset:448\n"
    "s_waitcnt vmcnt(0)\n"
    "v_mfma_f32_16x16x32_bf16 v[4:7], v[124:127], v[8:11], v[4:7]\n"
    "s_nop 7\n"
    "s_add_u32 s79, s79, 8192\n"
    "s_nop 15\n"
    "v_max3_f32 v109, v0, v1, v2\n"
    "v_max3_f32 v109, v109, v3, v4\n"
    "v_max3_f32 v109, v109, v5, v6\n"
    "v_max_f32 v109, v109, v7\n"
    "v_mov_b32 v126, v109\n"
    "s_nop 1\n"
    "v_permlane32_swap_b32 v109, v126\n"
    "v_max_f32 v109, v109, v126\n"
    "v_mov_b32 v126, v109\n"
    "s_nop 1\n"
    "v_permlane16_swap_b32 v109, v126\n"
    "v_max_f32 v109, v109, v126\n"
    "v_fma_f32 v109, v109, s39, -v101\n"
    "v_add_f32 v109, s40, v109\n"
    "v_max_f32 v109, 0, v109\n"
    "v_ceil_f32 v109, v109\n"
    "v_add_f32 v101, v101, v109\n"
    "v_min_f32 v126, 0x43480000, v109\n"
    "v_cvt_i32_f32 v126, v126\n"
    "v_sub_u32 v126, 0, v126\n"
    "v_ldexp_f32 v127, 1.0, v126\n"
    "v_mul_f32 v105, v105, v127\n"
    "v_fma_f32 v0, v0, s39, -v101\n"
    "v_fma_f32 v1, v1, s39, -v101\n"
    "v_fma_f32 v2, v2, s39, -v101\n"
    "v_fma_f32 v3, v3, s39, -v101\n"
    "v_fma_f32 v4, v4, s39, -v101\n"
    "v_fma_f32 v5, v5, s39, -v101\n"
    "v_fma_f32 v6, v6, s39, -v101\n"
    "v_fma_f32 v7, v7, s39, -v101\n"
    "v_exp_f32 v0, v0\n"
    "v_exp_f32 v1, v1\n"
    "v_exp_f32 v2, v2\n"
    "v_exp_f32 v3, v3\n"
    "v_exp_f32 v4, v4\n"
    "v_exp_f32 v5, v5\n"
    "v_exp_f32 v6, v6\n"
    "v_exp_f32 v7, v7\n"
    "v_add_f32 v126, v0, v1\n"
    "v_add_f32 v126, v126, v2\n"
    "v_add_f32 v126, v126, v3\n"
    "v_add_f32 v126, v126, v4\n"
    "v_add_f32 v126, v126, v5\n"
    "v_add_f32 v126, v126, v6\n"
    "v_add_f32 v126, v126, v7\n"
    "v_add_f32 v105, v105, v126\n"
    "v_cvt_pk_bf16_f32 v0, v0, v1\n"
    "v_cvt_pk_bf16_f32 v1, v2, v3\n"
    "v_cvt_pk_bf16_f32 v2, v4, v5\n"
    "v_cvt_pk_bf16_f32 v3, v6, v7\n"
    "v_mul_f32 v128, v128, v127\n"
    "v_mul_f32 v129, v129, v127\n"
    "v_mul_f32 v130, v130, v127\n"
    "v_mul_f32 v131, v131, v127\n"
    "v_mul_f32 v144, v144, v127\n"
    "v_mul_f32 v145, v145, v127\n"
    "v_mul_f32 v146, v146, v127\n"
    "v_mul_f32 v147, v147, v127\n"
    "v_mul_f32 v160, v160, v127\n"
    "v_mul_f32 v161, v161, v127\n"
    "v_mul_f32 v162, v162, v127\n"
    "v_mul_f32 v163, v163, v127\n"
    "v_mul_f32 v176, v176, v127\n"
    "v_mul_f32 v177, v177, v127\n"
    "v_mul_f32 v178, v178, v127\n"
    "v_mul_f32 v179, v179, v127\n"
    "v_mul_f32 v192, v192, v127\n"
    "v_mul_f32 v193, v193, v127\n"
    "v_mul_f32 v194, v194, v127\n"
    "v_mul_f32 v195, v195, v127\n"
    "v_mul_f32 v208, v208, v127\n"
    "v_mul_f32 v209, v209, v127\n"
    "v_mul_f32 v210, v210, v127\n"
    "v_mul_f32 v211, v211, v127\n"
    "v_mul_f32 v224, v224, v127\n"
    "v_mul_f32 v225, v225, v127\n"
    "v_mul_f32 v226, v226, v127\n"
    "v_mul_f32 v227, v227, v127\n"
    "v_mul_f32 v240, v240, v127\n"
    "v_mul_f32 v241, v241, v127\n"
    "v_mul_f32 v242, v242, v127\n"
    "v_mul_f32 v243, v243, v127\n"
    "v_accvgpr_read_b32 v124, a128\n"
    "v_mul_f32 v124, v124, v127\n"
    "v_accvgpr_write_b32 a128, v124\n"
    "v_accvgpr_read_b32 v125, a129\n"
    "v_mul_f32 v125, v125, v127\n"
    "v_accvgpr_write_b32 a129, v125\n"
    "v_accvgpr_read_b32 v124, a130\n"
    "v_mul_f32 v124, v124, v127\n"
    "v_accvgpr_write_b32 a130, v124\n"
    "v_accvgpr_read_b32 v125, a131\n"
    "v_mul_f32 v125, v125, v127\n"
    "v_accvgpr_write_b32 a131, v125\n"
    "v_accvgpr_read_b32 v124, a144\n"
    "v_mul_f32 v124, v124, v127\n"
    "v_accvgpr_write_b32 a144, v124\n"
    "v_accvgpr_read_b32 v125, a145\n"
    "v_mul_f32 v125, v125, v127\n"
    "v_accvgpr_write_b32 a145, v125\n"
    "v_accvgpr_read_b32 v124, a146\n"
    "v_mul_f32 v124, v124, v127\n"
    "v_accvgpr_write_b32 a146, v124\n"
    "v_accvgpr_read_b32 v125, a147\n"
    "v_mul_f32 v125, v125, v127\n"
    "v_accvgpr_write_b32 a147, v125\n"
    "v_accvgpr_read_b32 v124, a160\n"
    "v_mul_f32 v124, v124, v127\n"
    "v_accvgpr_write_b32 a160, v124\n"
    "v_accvgpr_read_b32 v125, a161\n"
    "v_mul_f32 v125, v125, v127\n"
    "v_accvgpr_write_b32 a161, v125\n"
    "v_accvgpr_read_b32 v124, a162\n"
    "v_mul_f32 v124, v124, v127\n"
    "v_accvgpr_write_b32 a162, v124\n"
    "v_accvgpr_read_b32 v125, a163\n"
    "v_mul_f32 v125, v125, v127\n"
    "v_accvgpr_write_b32 a163, v125\n"
    "v_accvgpr_read_b32 v124, a176\n"
    "v_mul_f32 v124, v124, v127\n"
    "v_accvgpr_write_b32 a176, v124\n"
    "v_accvgpr_read_b32 v125, a177\n"
    "v_mul_f32 v125, v125, v127\n"
    "v_accvgpr_write_b32 a177, v125\n"
    "v_accvgpr_read_b32 v124, a178\n"
    "v_mul_f32 v124, v124, v127\n"
    "v_accvgpr_write_b32 a178, v124\n"
    "v_accvgpr_read_b32 v125, a179\n"
    "v_mul_f32 v125, v125, v127\n"
    "v_accvgpr_write_b32 a179, v125\n"
    "v_accvgpr_read_b32 v124, a192\n"
    "v_mul_f32 v124, v124, v127\n"
    "v_accvgpr_write_b32 a192, v124\n"
    "v_accvgpr_read_b32 v125, a193\n"
    "v_mul_f32 v125, v125, v127\n"
    "v_accvgpr_write_b32 a193, v125\n"
    "v_accvgpr_read_b32 v124, a194\n"
    "v_mul_f32 v124, v124, v127\n"
    "v_accvgpr_write_b32 a194, v124\n"
    "v_accvgpr_read_b32 v125, a195\n"
    "v_mul_f32 v125, v125, v127\n"
    "v_accvgpr_write_b32 a195, v125\n"
    "v_mul_f32 v64, v64, v127\n"
    "v_mul_f32 v65, v65, v127\n"
    "v_mul_f32 v66, v66, v127\n"
    "v_mul_f32 v67, v67, v127\n"
    "v_accvgpr_read_b32 v124, a224\n"
    "v_mul_f32 v124, v124, v127\n"
    "v_accvgpr_write_b32 a224, v124\n"
    "v_accvgpr_read_b32 v125, a225\n"
    "v_mul_f32 v125, v125, v127\n"
    "v_accvgpr_write_b32 a225, v125\n"
    "v_accvgpr_read_b32 v124, a226\n"
    "v_mul_f32 v124, v124, v127\n"
    "v_accvgpr_write_b32 a226, v124\n"
    "v_accvgpr_read_b32 v125, a227\n"
    "v_mul_f32 v125, v125, v127\n"
    "v_accvgpr_write_b32 a227, v125\n"
    "v_mul_f32 v80, v80, v127\n"
    "v_mul_f32 v81, v81, v127\n"
    "v_mul_f32 v82, v82, v127\n"
    "v_mul_f32 v83, v83, v127\n"
    "v_mbcnt_lo_u32_b32 v126, -1, 0\n"
    "v_mbcnt_hi_u32_b32 v126, -1, v126\n"
    "v_and_b32 v109, 15, v126\n"
    "v_lshlrev_b32 v109, 9, v109\n"
    "v_lshrrev_b32 v126, 4, v126\n"
    "v_lshl_add_u32 v109, v126, 4, v109\n"
    "s_add_u32 s72, s73, 8192\n"
    "s_and_b32 s83, s87, 0x1000\n"
    "s_and_b32 s84, s87, 0x6000\n"
    "s_xor_b32 s84, s84, s79\n"
    "v_xor_b32 v109, s83, v109\n"
    "buffer_load_dwordx4 v[4:7], v109, s[4:7], s84 offen offset:0\n"
    "v_xor_b32 v109, s83, v109\n"
    "buffer_load_dwordx4 v[124:127], v109, s[8:11], s73 offen offset:0\n"
    "s_waitcnt vmcnt(0)\n"
    "v_mfma_f32_16x16x32_bf16 v[8:11], v[124:127], v[4:7], 0\n"
    "s_nop 7\n"
    "buffer_load_dwordx4 v[124:127], v109, s[8:11], s72 offen offset:0\n"
    "s_waitcnt vmcnt(0)\n"
    "v_mfma_f32_16x16x32_bf16 v[12:15], v[124:127], v[4:7], 0\n"
    "s_nop 7\n"
    "v_xor_b32 v109, s83, v109\n"
    "buffer_load_dwordx4 v[4:7], v109, s[4:7], s84 offen offset:64\n"
    "v_xor_b32 v109, s83, v109\n"
    "buffer_load_dwordx4 v[124:127], v109, s[8:11], s73 offen offset:64\n"
    "s_waitcnt vmcnt(0)\n"
    "v_mfma_f32_16x16x32_bf16 v[8:11], v[124:127], v[4:7], v[8:11]\n"
    "s_nop 7\n"
    "buffer_load_dwordx4 v[124:127], v109, s[8:11], s72 offen offset:64\n"
    "s_waitcnt vmcnt(0)\n"
    "v_mfma_f32_16x16x32_bf16 v[12:15], v[124:127], v[4:7], v[12:15]\n"
    "s_nop 7\n"
    "v_xor_b32 v109, s83, v109\n"
    "buffer_load_dwordx4 v[4:7], v109, s[4:7], s84 offen offset:128\n"
    "v_xor_b32 v109, s83, v109\n"
    "buffer_load_dwordx4 v[124:127], v109, s[8:11], s73 offen offset:128\n"
    "s_waitcnt vmcnt(0)\n"
    "v_mfma_f32_16x16x32_bf16 v[8:11], v[124:127], v[4:7], v[8:11]\n"
    "s_nop 7\n"
    "buffer_load_dwordx4 v[124:127], v109, s[8:11], s72 offen offset:128\n"
    "s_waitcnt vmcnt(0)\n"
    "v_mfma_f32_16x16x32_bf16 v[12:15], v[124:127], v[4:7], v[12:15]\n"
    "s_nop 7\n"
    "v_xor_b32 v109, s83, v109\n"
    "buffer_load_dwordx4 v[4:7], v109, s[4:7], s84 offen offset:192\n"
    "v_xor_b32 v109, s83, v109\n"
    "buffer_load_dwordx4 v[124:127], v109, s[8:11], s73 offen offset:192\n"
    "s_waitcnt vmcnt(0)\n"
    "v_mfma_f32_16x16x32_bf16 v[8:11], v[124:127], v[4:7], v[8:11]\n"
    "s_nop 7\n"
    "buffer_load_dwordx4 v[124:127], v109, s[8:11], s72 offen offset:192\n"
    "s_waitcnt vmcnt(0)\n"
    "v_mfma_f32_16x16x32_bf16 v[12:15], v[124:127], v[4:7], v[12:15]\n"
    "s_nop 7\n"
    "v_xor_b32 v109, s83, v109\n"
    "buffer_load_dwordx4 v[4:7], v109, s[4:7], s84 offen offset:256\n"
    "v_xor_b32 v109, s83, v109\n"
    "buffer_load_dwordx4 v[124:127], v109, s[8:11], s73 offen offset:256\n"
    "s_waitcnt vmcnt(0)\n"
    "v_mfma_f32_16x16x32_bf16 v[8:11], v[124:127], v[4:7], v[8:11]\n"
    "s_nop 7\n"
    "buffer_load_dwordx4 v[124:127], v109, s[8:11], s72 offen offset:256\n"
    "s_waitcnt vmcnt(0)\n"
    "v_mfma_f32_16x16x32_bf16 v[12:15], v[124:127], v[4:7], v[12:15]\n"
    "s_nop 7\n"
    "v_xor_b32 v109, s83, v109\n"
    "buffer_load_dwordx4 v[4:7], v109, s[4:7], s84 offen offset:320\n"
    "v_xor_b32 v109, s83, v109\n"
    "buffer_load_dwordx4 v[124:127], v109, s[8:11], s73 offen offset:320\n"
    "s_waitcnt vmcnt(0)\n"
    "v_mfma_f32_16x16x32_bf16 v[8:11], v[124:127], v[4:7], v[8:11]\n"
    "s_nop 7\n"
    "buffer_load_dwordx4 v[124:127], v109, s[8:11], s72 offen offset:320\n"
    "s_waitcnt vmcnt(0)\n"
    "v_mfma_f32_16x16x32_bf16 v[12:15], v[124:127], v[4:7], v[12:15]\n"
    "s_nop 7\n"
    "v_xor_b32 v109, s83, v109\n"
    "buffer_load_dwordx4 v[4:7], v109, s[4:7], s84 offen offset:384\n"
    "v_xor_b32 v109, s83, v109\n"
    "buffer_load_dwordx4 v[124:127], v109, s[8:11], s73 offen offset:384\n"
    "s_waitcnt vmcnt(0)\n"
    "v_mfma_f32_16x16x32_bf16 v[8:11], v[124:127], v[4:7], v[8:11]\n"
    "s_nop 7\n"
    "buffer_load_dwordx4 v[124:127], v109, s[8:11], s72 offen offset:384\n"
    "s_waitcnt vmcnt(0)\n"
    "v_mfma_f32_16x16x32_bf16 v[12:15], v[124:127], v[4:7], v[12:15]\n"
    "s_nop 7\n"
    "v_xor_b32 v109, s83, v109\n"
    "buffer_load_dwordx4 v[4:7], v109, s[4:7], s84 offen offset:448\n"
    "v_xor_b32 v109, s83, v109\n"
    "buffer_load_dwordx4 v[124:127], v109, s[8:11], s73 offen offset:448\n"
    "s_waitcnt vmcnt(0)\n"
    "v_mfma_f32_16x16x32_bf16 v[8:11], v[124:127], v[4:7], v[8:11]\n"
    "s_nop 7\n"
    "buffer_load_dwordx4 v[124:127], v109, s[8:11], s72 offen offset:448\n"
    "s_waitcnt vmcnt(0)\n"
    "v_mfma_f32_16x16x32_bf16 v[12:15], v[124:127], v[4:7], v[12:15]\n"
    "s_nop 7\n"
    "s_add_u32 s79, s79, 8192\n"
    "s_nop 15\n"
    "v_max3_f32 v109, v8, v9, v10\n"
    "v_max3_f32 v109, v109, v11, v12\n"
    "v_max3_f32 v109, v109, v13, v14\n"
    "v_max_f32 v109, v109, v15\n"
    "v_mov_b32 v126, v109\n"
    "s_nop 1\n"
    "v_permlane32_swap_b32 v109, v126\n"
    "v_max_f32 v109, v109, v126\n"
    "v_mov_b32 v126, v109\n"
    "s_nop 1\n"
    "v_permlane16_swap_b32 v109, v126\n"
    "v_max_f32 v109, v109, v126\n"
    "v_fma_f32 v109, v109, s39, -v102\n"
    "v_add_f32 v109, s40, v109\n"
    "v_max_f32 v109, 0, v109\n"
    "v_ceil_f32 v109, v109\n"
    "v_add_f32 v102, v102, v109\n"
    "v_min_f32 v126, 0x43480000, v109\n"
    "v_cvt_i32_f32 v126, v126\n"
    "v_sub_u32 v126, 0, v126\n"
    "v_ldexp_f32 v127, 1.0, v126\n"
    "v_mul_f32 v106, v106, v127\n"
    "v_fma_f32 v8, v8, s39, -v102\n"
    "v_fma_f32 v9, v9, s39, -v102\n"
    "v_fma_f32 v10, v10, s39, -v102\n"
    "v_fma_f32 v11, v11, s39, -v102\n"
    "v_fma_f32 v12, v12, s39, -v102\n"
    "v_fma_f32 v13, v13, s39, -v102\n"
    "v_fma_f32 v14, v14, s39, -v102\n"
    "v_fma_f32 v15, v15, s39, -v102\n"
    "v_exp_f32 v8, v8\n"
    "v_exp_f32 v9, v9\n"
    "v_exp_f32 v10, v10\n"
    "v_exp_f32 v11, v11\n"
    "v_exp_f32 v12, v12\n"
    "v_exp_f32 v13, v13\n"
    "v_exp_f32 v14, v14\n"
    "v_exp_f32 v15, v15\n"
    "v_add_f32 v126, v8, v9\n"
    "v_add_f32 v126, v126, v10\n"
    "v_add_f32 v126, v126, v11\n"
    "v_add_f32 v126, v126, v12\n"
    "v_add_f32 v126, v126, v13\n"
    "v_add_f32 v126, v126, v14\n"
    "v_add_f32 v126, v126, v15\n"
    "v_add_f32 v106, v106, v126\n"
    "v_cvt_pk_bf16_f32 v8, v8, v9\n"
    "v_cvt_pk_bf16_f32 v9, v10, v11\n"
    "v_cvt_pk_bf16_f32 v10, v12, v13\n"
    "v_cvt_pk_bf16_f32 v11, v14, v15\n"
    "v_mul_f32 v132, v132, v127\n"
    "v_mul_f32 v133, v133, v127\n"
    "v_mul_f32 v134, v134, v127\n"
    "v_mul_f32 v135, v135, v127\n"
    "v_mul_f32 v148, v148, v127\n"
    "v_mul_f32 v149, v149, v127\n"
    "v_mul_f32 v150, v150, v127\n"
    "v_mul_f32 v151, v151, v127\n"
    "v_mul_f32 v164, v164, v127\n"
    "v_mul_f32 v165, v165, v127\n"
    "v_mul_f32 v166, v166, v127\n"
    "v_mul_f32 v167, v167, v127\n"
    "v_mul_f32 v180, v180, v127\n"
    "v_mul_f32 v181, v181, v127\n"
    "v_mul_f32 v182, v182, v127\n"
    "v_mul_f32 v183, v183, v127\n"
    "v_mul_f32 v196, v196, v127\n"
    "v_mul_f32 v197, v197, v127\n"
    "v_mul_f32 v198, v198, v127\n"
    "v_mul_f32 v199, v199, v127\n"
    "v_mul_f32 v212, v212, v127\n"
    "v_mul_f32 v213, v213, v127\n"
    "v_mul_f32 v214, v214, v127\n"
    "v_mul_f32 v215, v215, v127\n"
    "v_mul_f32 v228, v228, v127\n"
    "v_mul_f32 v229, v229, v127\n"
    "v_mul_f32 v230, v230, v127\n"
    "v_mul_f32 v231, v231, v127\n"
    "v_mul_f32 v244, v244, v127\n"
    "v_mul_f32 v245, v245, v127\n"
    "v_mul_f32 v246, v246, v127\n"
    "v_mul_f32 v247, v247, v127\n"
    "v_accvgpr_read_b32 v124, a132\n"
    "v_mul_f32 v124, v124, v127\n"
    "v_accvgpr_write_b32 a132, v124\n"
    "v_accvgpr_read_b32 v125, a133\n"
    "v_mul_f32 v125, v125, v127\n"
    "v_accvgpr_write_b32 a133, v125\n"
    "v_accvgpr_read_b32 v124, a134\n"
    "v_mul_f32 v124, v124, v127\n"
    "v_accvgpr_write_b32 a134, v124\n"
    "v_accvgpr_read_b32 v125, a135\n"
    "v_mul_f32 v125, v125, v127\n"
    "v_accvgpr_write_b32 a135, v125\n"
    "v_accvgpr_read_b32 v124, a148\n"
    "v_mul_f32 v124, v124, v127\n"
    "v_accvgpr_write_b32 a148, v124\n"
    "v_accvgpr_read_b32 v125, a149\n"
    "v_mul_f32 v125, v125, v127\n"
    "v_accvgpr_write_b32 a149, v125\n"
    "v_accvgpr_read_b32 v124, a150\n"
    "v_mul_f32 v124, v124, v127\n"
    "v_accvgpr_write_b32 a150, v124\n"
    "v_accvgpr_read_b32 v125, a151\n"
    "v_mul_f32 v125, v125, v127\n"
    "v_accvgpr_write_b32 a151, v125\n"
    "v_accvgpr_read_b32 v124, a164\n"
    "v_mul_f32 v124, v124, v127\n"
    "v_accvgpr_write_b32 a164, v124\n"
    "v_accvgpr_read_b32 v125, a165\n"
    "v_mul_f32 v125, v125, v127\n"
    "v_accvgpr_write_b32 a165, v125\n"
    "v_accvgpr_read_b32 v124, a166\n"
    "v_mul_f32 v124, v124, v127\n"
    "v_accvgpr_write_b32 a166, v124\n"
    "v_accvgpr_read_b32 v125, a167\n"
    "v_mul_f32 v125, v125, v127\n"
    "v_accvgpr_write_b32 a167, v125\n"
    "v_accvgpr_read_b32 v124, a180\n"
    "v_mul_f32 v124, v124, v127\n"
    "v_accvgpr_write_b32 a180, v124\n"
    "v_accvgpr_read_b32 v125, a181\n"
    "v_mul_f32 v125, v125, v127\n"
    "v_accvgpr_write_b32 a181, v125\n"
    "v_accvgpr_read_b32 v124, a182\n"
    "v_mul_f32 v124, v124, v127\n"
    "v_accvgpr_write_b32 a182, v124\n"
    "v_accvgpr_read_b32 v125, a183\n"
    "v_mul_f32 v125, v125, v127\n"
    "v_accvgpr_write_b32 a183, v125\n"
    "v_accvgpr_read_b32 v124, a196\n"
    "v_mul_f32 v124, v124, v127\n"
    "v_accvgpr_write_b32 a196, v124\n"
    "v_accvgpr_read_b32 v125, a197\n"
    "v_mul_f32 v125, v125, v127\n"
    "v_accvgpr_write_b32 a197, v125\n"
    "v_accvgpr_read_b32 v124, a198\n"
    "v_mul_f32 v124, v124, v127\n"
    "v_accvgpr_write_b32 a198, v124\n"
    "v_accvgpr_read_b32 v125, a199\n"
    "v_mul_f32 v125, v125, v127\n"
    "v_accvgpr_write_b32 a199, v125\n"
    "v_mul_f32 v68, v68, v127\n"
    "v_mul_f32 v69, v69, v127\n"
    "v_mul_f32 v70, v70, v127\n"
    "v_mul_f32 v71, v71, v127\n"
    "v_accvgpr_read_b32 v124, a228\n"
    "v_mul_f32 v124, v124, v127\n"
    "v_accvgpr_write_b32 a228, v124\n"
    "v_accvgpr_read_b32 v125, a229\n"
    "v_mul_f32 v125, v125, v127\n"
    "v_accvgpr_write_b32 a229, v125\n"
    "v_accvgpr_read_b32 v124, a230\n"
    "v_mul_f32 v124, v124, v127\n"
    "v_accvgpr_write_b32 a230, v124\n"
    "v_accvgpr_read_b32 v125, a231\n"
    "v_mul_f32 v125, v125, v127\n"
    "v_accvgpr_write_b32 a231, v125\n"
    "v_mul_f32 v84, v84, v127\n"
    "v_mul_f32 v85, v85, v127\n"
    "v_mul_f32 v86, v86, v127\n"
    "v_mul_f32 v87, v87, v127\n"
    "v_mov_b32 v4, 0\n"
    "v_mov_b32 v12, 0\n"
    "s_cmp_eq_u32 s71, 1\n"
    "s_cbranch_scc1 L_rare0R\n"
    "L_rare0B:\n"
    "s_nop 15\n"
    "v_mbcnt_lo_u32_b32 v126, -1, 0\n"
    "v_mbcnt_hi_u32_b32 v126, -1, v126\n"
    "v_and_b32 v109, 15, v126\n"
    "v_lshlrev_b32 v109, 9, v109\n"
    "v_lshrrev_b32 v126, 4, v126\n"
    "v_lshl_add_u32 v109, v126, 4, v109\n"
    "s_add_u32 s72, s73, 8192\n"
    "s_and_b32 s83, s87, 0x1000\n"
    "s_and_b32 s84, s87, 0x6000\n"
    "s_xor_b32 s84, s84, s79\n"
    "v_xor_b32 v109, s83, v109\n"
    "buffer_load_dwordx4 v[24:27], v109, s[4:7], s84 offen offset:0\n"
    "v_xor_b32 v109, s83, v109\n"
    "buffer_load_dwordx4 v[124:127], v109, s[8:11], s73 offen offset:0\n"
    "s_waitcnt vmcnt(0)\n"
    "v_mfma_f32_16x16x32_bf16 v[16:19], v[124:127], v[24:27], 0\n"
    "s_nop 7\n"
    "buffer_load_dwordx4 v[124:127], v109, s[8:11], s72 offen offset:0\n"
    "s_waitcnt vmcnt(0)\n"
    "v_mfma_f32_16x16x32_bf16 v[20:23], v[124:127], v[24:27], 0\n"
    "s_nop 7\n"
    "v_xor_b32 v109, s83, v109\n"
    "buffer_load_dwordx4 v[24:27], v109, s[4:7], s84 offen offset:64\n"
    "v_xor_b32 v109, s83, v109\n"
    "buffer_load_dwordx4 v[124:127], v109, s[8:11], s73 offen offset:64\n"
    "s_waitcnt vmcnt(0)\n"
    "v_mfma_f32_16x16x32_bf16 v[16:19], v[124:127], v[24:27], v[16:19]\n"
    "s_nop 7\n"
    "buffer_load_dwordx4 v[124:127], v109, s[8:11], s72 offen offset:64\n"
    "s_waitcnt vmcnt(0)\n"
    "v_mfma_f32_16x16x32_bf16 v[20:23], v[124:127], v[24:27], v[20:23]\n"
    "s_nop 7\n"
    "v_xor_b32 v109, s83, v109\n"
    "buffer_load_dwordx4 v[24:27], v109, s[4:7], s84 offen offset:128\n"
    "v_xor_b32 v109, s83, v109\n"
    "buffer_load_dwordx4 v[124:127], v109, s[8:11], s73 offen offset:128\n"
    "s_waitcnt vmcnt(0)\n"
    "v_mfma_f32_16x16x32_bf16 v[16:19], v[124:127], v[24:27], v[16:19]\n"
    "s_nop 7\n"
    "buffer_load_dwordx4 v[124:127], v109, s[8:11], s72 offen offset:128\n"
    "s_waitcnt vmcnt(0)\n"
    "v_mfma_f32_16x16x32_bf16 v[20:23], v[124:127], v[24:27], v[20:23]\n"
    "s_nop 7\n"
    "v_xor_b32 v109, s83, v109\n"
    "buffer_load_dwordx4 v[24:27], v109, s[4:7], s84 offen offset:192\n"
    "v_xor_b32 v109, s83, v109\n"
    "buffer_load_dwordx4 v[124:127], v109, s[8:11], s73 offen offset:192\n"
    "s_waitcnt vmcnt(0)\n"
    "v_mfma_f32_16x16x32_bf16 v[16:19], v[124:127], v[24:27], v[16:19]\n"
    "s_nop 7\n"
    "buffer_load_dwordx4 v[124:127], v109, s[8:11], s72 offen offset:192\n"
    "s_waitcnt vmcnt(0)\n"
    "v_mfma_f32_16x16x32_bf16 v[20:23], v[124:127], v[24:27], v[20:23]\n"
    "s_nop 7\n"
    "v_xor_b32 v109, s83, v109\n"
    "buffer_load_dwordx4 v[24:27], v109, s[4:7], s84 offen offset:256\n"
    "v_xor_b32 v109, s83, v109\n"
    "buffer_load_dwordx4 v[124:127], v109, s[8:11], s73 offen offset:256\n"
    "s_waitcnt vmcnt(0)\n"
    "v_mfma_f32_16x16x32_bf16 v[16:19], v[124:127], v[24:27], v[16:19]\n"
    "s_nop 7\n"
    "buffer_load_dwordx4 v[124:127], v109, s[8:11], s72 offen offset:256\n"
    "s_waitcnt vmcnt(0)\n"
    "v_mfma_f32_16x16x32_bf16 v[20:23], v[124:127], v[24:27], v[20:23]\n"
    "s_nop 7\n"
    "v_xor_b32 v109, s83, v109\n"
    "buffer_load_dwordx4 v[24:27], v109, s[4:7], s84 offen offset:320\n"
    "v_xor_b32 v109, s83, v109\n"
    "buffer_load_dwordx4 v[124:127], v109, s[8:11], s73 offen offset:320\n"
    "s_waitcnt vmcnt(0)\n"
    "v_mfma_f32_16x16x32_bf16 v[16:19], v[124:127], v[24:27], v[16:19]\n"
    "s_nop 7\n"
    "buffer_load_dwordx4 v[124:127], v109, s[8:11], s72 offen offset:320\n"
    "s_waitcnt vmcnt(0)\n"
    "v_mfma_f32_16x16x32_bf16 v[20:23], v[124:127], v[24:27], v[20:23]\n"
    "s_nop 7\n"
    "v_xor_b32 v109, s83, v109\n"
    "buffer_load_dwordx4 v[24:27], v109, s[4:7], s84 offen offset:384\n"
    "v_xor_b32 v109, s83, v109\n"
    "buffer_load_dwordx4 v[124:127], v109, s[8:11], s73 offen offset:384\n"
    "s_waitcnt vmcnt(0)\n"
    "v_mfma_f32_16x16x32_bf16 v[16:19], v[124:127], v[24:27], v[16:19]\n"
    "s_nop 7\n"
    "buffer_load_dwordx4 v[124:127], v109, s[8:11], s72 offen offset:384\n"
    "s_waitcnt vmcnt(0)\n"
    "v_mfma_f32_16x16x32_bf16 v[20:23], v[124:127], v[24:27], v[20:23]\n"
    "s_nop 7\n"
    "v_xor_b32 v109, s83, v109\n"
    "buffer_load_dwordx4 v[24:27], v109, s[4:7], s84 offen offset:448\n"
    "v_xor_b32 v109, s83, v109\n"
    "buffer_load_dwordx4 v[124:127], v109, s[8:11], s73 offen offset:448\n"
    "s_waitcnt vmcnt(0)\n"
    "v_mfma_f32_16x16x32_bf16 v[16:19], v[124:127], v[24:27], v[16:19]\n"
    "s_nop 7\n"
    "buffer_load_dwordx4 v[124:127], v109, s[8:11], s72 offen offset:448\n"
    "s_waitcnt vmcnt(0)\n"
    "v_mfma_f32_16x16x32_bf16 v[20:23], v[124:127], v[24:27], v[20:23]\n"
    "s_nop 7\n"
    "s_add_u32 s79, s79, 8192\n"
    "s_nop 15\n"
    "v_max3_f32 v109, v16, v17, v18\n"
    "v_max3_f32 v109, v109, v19, v20\n"
    "v_max3_f32 v109, v109, v21, v22\n"
    "v_max_f32 v109, v109, v23\n"
    "v_mov_b32 v126, v109\n"
    "s_nop 1\n"
    "v_permlane32_swap_b32 v109, v126\n"
    "v_max_f32 v109, v109, v126\n"
    "v_mov_b32 v126, v109\n"
    "s_nop 1\n"
    "v_permlane16_swap_b32 v109, v126\n"
    "v_max_f32 v109, v109, v126\n"
    "v_fma_f32 v109, v109, s39, -v103\n"
    "v_add_f32 v109, s40, v109\n"
    "v_max_f32 v109, 0, v109\n"
    "v_ceil_f32 v109, v109\n"
    "v_add_f32 v103, v103, v109\n"
    "v_min_f32 v126, 0x43480000, v109\n"
    "v_cvt_i32_f32 v126, v126\n"
    "v_sub_u32 v126, 0, v126\n"
    "v_ldexp_f32 v127, 1.0, v126\n"
    "v_mul_f32 v107, v107, v127\n"
    "s_cmp_eq_u32 s71, 2\n"
    "s_cbranch_scc0 L_rsk2\n"
    "v_lshlrev_b32 v124, 16, v48\n"
    "v_and_b32 v125, 0xffff0000, v48\n"
    "v_mul_f32 v124, v124, v127\n"
    "v_mul_f32 v125, v125, v127\n"
    "v_cvt_pk_bf16_f32 v48, v124, v125\n"
    "v_lshlrev_b32 v124, 16, v49\n"
    "v_and_b32 v125, 0xffff0000, v49\n"
    "v_mul_f32 v124, v124, v127\n"
    "v_mul_f32 v125, v125, v127\n"
    "v_cvt_pk_bf16_f32 v49, v124, v125\n"
    "v_lshlrev_b32 v124, 16, v50\n"
    "v_and_b32 v125, 0xffff0000, v50\n"
    "v_mul_f32 v124, v124, v127\n"
    "v_mul_f32 v125, v125, v127\n"
    "v_cvt_pk_bf16_f32 v50, v124, v125\n"
    "v_lshlrev_b32 v124, 16, v51\n"
    "v_and_b32 v125, 0xffff0000, v51\n"
    "v_mul_f32 v124, v124, v127\n"
    "v_mul_f32 v125, v125, v127\n"
    "v_cvt_pk_bf16_f32 v51, v124, v125\n"
    "L_rsk2:\n"
    "v_fma_f32 v16, v16, s39, -v103\n"
    "v_fma_f32 v17, v17, s39, -v103\n"
    "v_fma_f32 v18, v18, s39, -v103\n"
    "v_fma_f32 v19, v19, s39, -v103\n"
    "v_fma_f32 v20, v20, s39, -v103\n"
    "v_fma_f32 v21, v21, s39, -v103\n"
    "v_fma_f32 v22, v22, s39, -v103\n"
    "v_fma_f32 v23, v23, s39, -v103\n"
    "v_exp_f32 v16, v16\n"
    "v_exp_f32 v17, v17\n"
    "v_exp_f32 v18, v18\n"
    "v_exp_f32 v19, v19\n"
    "v_exp_f32 v20, v20\n"
    "v_exp_f32 v21, v21\n"
    "v_exp_f32 v22, v22\n"
    "v_exp_f32 v23, v23\n"
    "v_add_f32 v126, v16, v17\n"
    "v_add_f32 v126, v126, v18\n"
    "v_add_f32 v126, v126, v19\n"
    "v_add_f32 v126, v126, v20\n"
    "v_add_f32 v126, v126, v21\n"
    "v_add_f32 v126, v126, v22\n"
    "v_add_f32 v126, v126, v23\n"
    "v_add_f32 v107, v107, v126\n"
    "v_cvt_pk_bf16_f32 v16, v16, v17\n"
    "v_cvt_pk_bf16_f32 v17, v18, v19\n"
    "v_cvt_pk_bf16_f32 v18, v20, v21\n"
    "v_cvt_pk_bf16_f32 v19, v22, v23\n"
    "v_mul_f32 v136, v136, v127\n"
    "v_mul_f32 v137, v137, v127\n"
    "v_mul_f32 v138, v138, v127\n"
    "v_mul_f32 v139, v139, v127\n"
    "v_mul_f32 v152, v152, v127\n"
    "v_mul_f32 v153, v153, v127\n"
    "v_mul_f32 v154, v154, v127\n"
    "v_mul_f32 v155, v155, v127\n"
    "v_mul_f32 v168, v168, v127\n"
    "v_mul_f32 v169, v169, v127\n"
    "v_mul_f32 v170, v170, v127\n"
    "v_mul_f32 v171, v171, v127\n"
    "v_mul_f32 v184, v184, v127\n"
    "v_mul_f32 v185, v185, v127\n"
    "v_mul_f32 v186, v186, v127\n"
    "v_mul_f32 v187, v187, v127\n"
    "v_mul_f32 v200, v200, v127\n"
    "v_mul_f32 v201, v201, v127\n"
    "v_mul_f32 v202, v202, v127\n"
    "v_mul_f32 v203, v203, v127\n"
    "v_mul_f32 v216, v216, v127\n"
    "v_mul_f32 v217, v217, v127\n"
    "v_mul_f32 v218, v218, v127\n"
    "v_mul_f32 v219, v219, v127\n"
    "v_mul_f32 v232, v232, v127\n"
    "v_mul_f32 v233, v233, v127\n"
    "v_mul_f32 v234, v234, v127\n"
    "v_mul_f32 v235, v235, v127\n"
    "v_mul_f32 v248, v248, v127\n"
    "v_mul_f32 v249, v249, v127\n"
    "v_mul_f32 v250, v250, v127\n"
    "v_mul_f32 v251, v251, v127\n"
    "v_accvgpr_read_b32 v124, a136\n"
    "v_mul_f32 v124, v124, v127\n"
    "v_accvgpr_write_b32 a136, v124\n"
    "v_accvgpr_read_b32 v125, a137\n"
    "v_mul_f32 v125, v125, v127\n"
    "v_accvgpr_write_b32 a137, v125\n"
    "v_accvgpr_read_b32 v124, a138\n"
    "v_mul_f32 v124, v124, v127\n"
    "v_accvgpr_write_b32 a138, v124\n"
    "v_accvgpr_read_b32 v125, a139\n"
    "v_mul_f32 v125, v125, v127\n"
    "v_accvgpr_write_b32 a139, v125\n"
    "v_accvgpr_read_b32 v124, a152\n"
    "v_mul_f32 v124, v124, v127\n"
    "v_accvgpr_write_b32 a152, v124\n"
    "v_accvgpr_read_b32 v125, a153\n"
    "v_mul_f32 v125, v125, v127\n"
    "v_accvgpr_write_b32 a153, v125\n"
    "v_accvgpr_read_b32 v124, a154\n"
    "v_mul_f32 v124, v124, v127\n"
    "v_accvgpr_write_b32 a154, v124\n"
    "v_accvgpr_read_b32 v125, a155\n"
    "v_mul_f32 v125, v125, v127\n"
    "v_accvgpr_write_b32 a155, v125\n"
    "v_accvgpr_read_b32 v124, a168\n"
    "v_mul_f32 v124, v124, v127\n"
    "v_accvgpr_write_b32 a168, v124\n"
    "v_accvgpr_read_b32 v125, a169\n"
    "v_mul_f32 v125, v125, v127\n"
    "v_accvgpr_write_b32 a169, v125\n"
    "v_accvgpr_read_b32 v124, a170\n"
    "v_mul_f32 v124, v124, v127\n"
    "v_accvgpr_write_b32 a170, v124\n"
    "v_accvgpr_read_b32 v125, a171\n"
    "v_mul_f32 v125, v125, v127\n"
    "v_accvgpr_write_b32 a171, v125\n"
    "v_accvgpr_read_b32 v124, a184\n"
    "v_mul_f32 v124, v124, v127\n"
    "v_accvgpr_write_b32 a184, v124\n"
    "v_accvgpr_read_b32 v125, a185\n"
    "v_mul_f32 v125, v125, v127\n"
    "v_accvgpr_write_b32 a185, v125\n"
    "v_accvgpr_read_b32 v124, a186\n"
    "v_mul_f32 v124, v124, v127\n"
    "v_accvgpr_write_b32 a186, v124\n"
    "v_accvgpr_read_b32 v125, a187\n"
    "v_mul_f32 v125, v125, v127\n"
    "v_accvgpr_write_b32 a187, v125\n"
    "v_accvgpr_read_b32 v124, a200\n"
    "v_mul_f32 v124, v124, v127\n"
    "v_accvgpr_write_b32 a200, v124\n"
    "v_accvgpr_read_b32 v125, a201\n"
    "v_mul_f32 v125, v125, v127\n"
    "v_accvgpr_write_b32 a201, v125\n"
    "v_accvgpr_read_b32 v124, a202\n"
    "v_mul_f32 v124, v124, v127\n"
    "v_accvgpr_write_b32 a202, v124\n"
    "v_accvgpr_read_b32 v125, a203\n"
    "v_mul_f32 v125, v125, v127\n"
    "v_accvgpr_write_b32 a203, v125\n"
    "v_mul_f32 v72, v72, v127\n"
    "v_mul_f32 v73, v73, v127\n"
    "v_mul_f32 v74, v74, v127\n"
    "v_mul_f32 v75, v75, v127\n"
    "v_accvgpr_read_b32 v124, a232\n"
    "v_mul_f32 v124, v124, v127\n"
    "v_accvgpr_write_b32 a232, v124\n"
    "v_accvgpr_read_b32 v125, a233\n"
    "v_mul_f32 v125, v125, v127\n"
    "v_accvgpr_write_b32 a233, v125\n"
    "v_accvgpr_read_b32 v124, a234\n"
    "v_mul_f32 v124, v124, v127\n"
    "v_accvgpr_write_b32 a234, v124\n"
    "v_accvgpr_read_b32 v125, a235\n"
    "v_mul_f32 v125, v125, v127\n"
    "v_accvgpr_write_b32 a235, v125\n"
    "v_mul_f32 v88, v88, v127\n"
    "v_mul_f32 v89, v89, v127\n"
    "v_mul_f32 v90, v90, v127\n"
    "v_mul_f32 v91, v91, v127\n"
    "v_mbcnt_lo_u32_b32 v126, -1, 0\n"
    "v_mbcnt_hi_u32_b32 v126, -1, v126\n"
    "v_and_b32 v109, 15, v126\n"
    "v_lshlrev_b32 v109, 9, v109\n"
    "v_lshrrev_b32 v126, 4, v126\n"
    "v_lshl_add_u32 v109, v126, 4, v109\n"
    "s_add_u32 s72, s73, 8192\n"
    "s_and_b32 s83, s87, 0x1000\n"
    "s_and_b32 s84, s87, 0x6000\n"
    "s_xor_b32 s84, s84, s79\n"
    "v_xor_b32 v109, s83, v109\n"
    "buffer_load_dwordx4 v[20:23], v109, s[4:7], s84 offen offset:0\n"
    "v_xor_b32 v109, s83, v109\n"
    "buffer_load_dwordx4 v[124:127], v109, s[8:11], s73 offen offset:0\n"
    "s_waitcnt vmcnt(0)\n"
    "v_mfma_f32_16x16x32_bf16 v[24:27], v[124:127], v[20:23], 0\n"
    "s_nop 7\n"
    "buffer_load_dwordx4 v[124:127], v109, s[8:11], s72 offen offset:0\n"
    "s_waitcnt vmcnt(0)\n"
    "v_mfma_f32_16x16x32_bf16 v[28:31], v[124:127], v[20:23], 0\n"
    "s_nop 7\n"
    "v_xor_b32 v109, s83, v109\n"
    "buffer_load_dwordx4 v[20:23], v109, s[4:7], s84 offen offset:64\n"
    "v_xor_b32 v109, s83, v109\n"
    "buffer_load_dwordx4 v[124:127], v109, s[8:11], s73 offen offset:64\n"
    "s_waitcnt vmcnt(0)\n"
    "v_mfma_f32_16x16x32_bf16 v[24:27], v[124:127], v[20:23], v[24:27]\n"
    "s_nop 7\n"
    "buffer_load_dwordx4 v[124:127], v109, s[8:11], s72 offen offset:64\n"
    "s_waitcnt vmcnt(0)\n"
    "v_mfma_f32_16x16x32_bf16 v[28:31], v[124:127], v[20:23], v[28:31]\n"
    "s_nop 7\n"
    "v_xor_b32 v109, s83, v109\n"
    "buffer_load_dwordx4 v[20:23], v109, s[4:7], s84 offen offset:128\n"
    "v_xor_b32 v109, s83, v109\n"
    "buffer_load_dwordx4 v[124:127], v109, s[8:11], s73 offen offset:128\n"
    "s_waitcnt vmcnt(0)\n"
    "v_mfma_f32_16x16x32_bf16 v[24:27], v[124:127], v[20:23], v[24:27]\n"
    "s_nop 7\n"
    "buffer_load_dwordx4 v[124:127], v109, s[8:11], s72 offen offset:128\n"
    "s_waitcnt vmcnt(0)\n"
    "v_mfma_f32_16x16x32_bf16 v[28:31], v[124:127], v[20:23], v[28:31]\n"
    "s_nop 7\n"
    "v_xor_b32 v109, s83, v109\n"
    "buffer_load_dwordx4 v[20:23], v109, s[4:7], s84 offen offset:192\n"
    "v_xor_b32 v109, s83, v109\n"
    "buffer_load_dwordx4 v[124:127], v109, s[8:11], s73 offen offset:192\n"
    "s_waitcnt vmcnt(0)\n"
    "v_mfma_f32_16x16x32_bf16 v[24:27], v[124:127], v[20:23], v[24:27]\n"
    "s_nop 7\n"
    "buffer_load_dwordx4 v[124:127], v109, s[8:11], s72 offen offset:192\n"
    "s_waitcnt vmcnt(0)\n"
    "v_mfma_f32_16x16x32_bf16 v[28:31], v[124:127], v[20:23], v[28:31]\n"
    "s_nop 7\n"
    "v_xor_b32 v109, s83, v109\n"
    "buffer_load_dwordx4 v[20:23], v109, s[4:7], s84 offen offset:256\n"
    "v_xor_b32 v109, s83, v109\n"
    "buffer_load_dwordx4 v[124:127], v109, s[8:11], s73 offen offset:256\n"
    "s_waitcnt vmcnt(0)\n"
    "v_mfma_f32_16x16x32_bf16 v[24:27], v[124:127], v[20:23], v[24:27]\n"
    "s_nop 7\n"
    "buffer_load_dwordx4 v[124:127], v109, s[8:11], s72 offen offset:256\n"
    "s_waitcnt vmcnt(0)\n"
    "v_mfma_f32_16x16x32_bf16 v[28:31], v[124:127], v[20:23], v[28:31]\n"
    "s_nop 7\n"
    "v_xor_b32 v109, s83, v109\n"
    "buffer_load_dwordx4 v[20:23], v109, s[4:7], s84 offen offset:320\n"
    "v_xor_b32 v109, s83, v109\n"
    "buffer_load_dwordx4 v[124:127], v109, s[8:11], s73 offen offset:320\n"
    "s_waitcnt vmcnt(0)\n"
    "v_mfma_f32_16x16x32_bf16 v[24:27], v[124:127], v[20:23], v[24:27]\n"
    "s_nop 7\n"
    "buffer_load_dwordx4 v[124:127], v109, s[8:11], s72 offen offset:320\n"
    "s_waitcnt vmcnt(0)\n"
    "v_mfma_f32_16x16x32_bf16 v[28:31], v[124:127], v[20:23], v[28:31]\n"
    "s_nop 7\n"
    "v_xor_b32 v109, s83, v109\n"
    "buffer_load_dwordx4 v[20:23], v109, s[4:7], s84 offen offset:384\n"
    "v_xor_b32 v109, s83, v109\n"
    "buffer_load_dwordx4 v[124:127], v109, s[8:11], s73 offen offset:384\n"
    "s_waitcnt vmcnt(0)\n"
    "v_mfma_f32_16x16x32_bf16 v[24:27], v[124:127], v[20:23], v[24:27]\n"
    "s_nop 7\n"
    "buffer_load_dwordx4 v[124:127], v109, s[8:11], s72 offen offset:384\n"
    "s_waitcnt vmcnt(0)\n"
    "v_mfma_f32_16x16x32_bf16 v[28:31], v[124:127], v[20:23], v[28:31]\n"
    "s_nop 7\n"
    "v_xor_b32 v109, s83, v109\n"
    "buffer_load_dwordx4 v[20:23], v109, s[4:7], s84 offen offset:448\n"
    "v_xor_b32 v109, s83, v109\n"
    "buffer_load_dwordx4 v[124:127], v109, s[8:11], s73 offen offset:448\n"
    "s_waitcnt vmcnt(0)\n"
    "v_mfma_f32_16x16x32_bf16 v[24:27], v[124:127], v[20:23], v[24:27]\n"
    "s_nop 7\n"
    "buffer_load_dwordx4 v[124:127], v109, s[8:11], s72 offen offset:448\n"
    "s_waitcnt vmcnt(0)\n"
    "v_mfma_f32_16x16x32_bf16 v[28:31], v[124:127], v[20:23], v[28:31]\n"
    "s_nop 7\n"
    "s_add_u32 s79, s79, 8192\n"
    "s_nop 15\n"
    "v_max3_f32 v109, v24, v25, v26\n"
    "v_max3_f32 v109, v109, v27, v28\n"
    "v_max3_f32 v109, v109, v29, v30\n"
    "v_max_f32 v109, v109, v31\n"
    "v_mov_b32 v126, v109\n"
    "s_nop 1\n"
    "v_permlane32_swap_b32 v109, v126\n"
    "v_max_f32 v109, v109, v126\n"
    "v_mov_b32 v126, v109\n"
    "s_nop 1\n"
    "v_permlane16_swap_b32 v109, v126\n"
    "v_max_f32 v109, v109, v126\n"
    "v_fma_f32 v109, v109, s39, -v104\n"
    "v_add_f32 v109, s40, v109\n"
    "v_max_f32 v109, 0, v109\n"
    "v_ceil_f32 v109, v109\n"
    "v_add_f32 v104, v104, v109\n"
    "v_min_f32 v126, 0x43480000, v109\n"
    "v_cvt_i32_f32 v126, v126\n"
    "v_sub_u32 v126, 0, v126\n"
    "v_ldexp_f32 v127, 1.0, v126\n"
    "v_mul_f32 v108, v108, v127\n"
    "s_cmp_eq_u32 s71, 2\n"
    "s_cbranch_scc0 L_rsk3\n"
    "v_lshlrev_b32 v124, 16, v56\n"
    "v_and_b32 v125, 0xffff0000, v56\n"
    "v_mul_f32 v124, v124, v127\n"
    "v_mul_f32 v125, v125, v127\n"
    "v_cvt_pk_bf16_f32 v56, v124, v125\n"
    "v_lshlrev_b32 v124, 16, v57\n"
    "v_and_b32 v125, 0xffff0000, v57\n"
    "v_mul_f32 v124, v124, v127\n"
    "v_mul_f32 v125, v125, v127\n"
    "v_cvt_pk_bf16_f32 v57, v124, v125\n"
    "v_lshlrev_b32 v124, 16, v58\n"
    "v_and_b32 v125, 0xffff0000, v58\n"
    "v_mul_f32 v124, v124, v127\n"
    "v_mul_f32 v125, v125, v127\n"
    "v_cvt_pk_bf16_f32 v58, v124, v125\n"
    "v_lshlrev_b32 v124, 16, v59\n"
    "v_and_b32 v125, 0xffff0000, v59\n"
    "v_mul_f32 v124, v124, v127\n"
    "v_mul_f32 v125, v125, v127\n"
    "v_cvt_pk_bf16_f32 v59, v124, v125\n"
    "L_rsk3:\n"
    "v_fma_f32 v24, v24, s39, -v104\n"
    "v_fma_f32 v25, v25, s39, -v104\n"
    "v_fma_f32 v26, v26, s39, -v104\n"
    "v_fma_f32 v27, v27, s39, -v104\n"
    "v_fma_f32 v28, v28, s39, -v104\n"
    "v_fma_f32 v29, v29, s39, -v104\n"
    "v_fma_f32 v30, v30, s39, -v104\n"
    "v_fma_f32 v31, v31, s39, -v104\n"
    "v_exp_f32 v24, v24\n"
    "v_exp_f32 v25, v25\n"
    "v_exp_f32 v26, v26\n"
    "v_exp_f32 v27, v27\n"
    "v_exp_f32 v28, v28\n"
    "v_exp_f32 v29, v29\n"
    "v_exp_f32 v30, v30\n"
    "v_exp_f32 v31, v31\n"
    "v_add_f32 v126, v24, v25\n"
    "v_add_f32 v126, v126, v26\n"
    "v_add_f32 v126, v126, v27\n"
    "v_add_f32 v126, v126, v28\n"
    "v_add_f32 v126, v126, v29\n"
    "v_add_f32 v126, v126, v30\n"
    "v_add_f32 v126, v126, v31\n"
    "v_add_f32 v108, v108, v126\n"
    "v_cvt_pk_bf16_f32 v24, v24, v25\n"
    "v_cvt_pk_bf16_f32 v25, v26, v27\n"
    "v_cvt_pk_bf16_f32 v26, v28, v29\n"
    "v_cvt_pk_bf16_f32 v27, v30, v31\n"
    "v_mul_f32 v140, v140, v127\n"
    "v_mul_f32 v141, v141, v127\n"
    "v_mul_f32 v142, v142, v127\n"
    "v_mul_f32 v143, v143, v127\n"
    "v_mul_f32 v156, v156, v127\n"
    "v_mul_f32 v157, v157, v127\n"
    "v_mul_f32 v158, v158, v127\n"
    "v_mul_f32 v159, v159, v127\n"
    "v_mul_f32 v172, v172, v127\n"
    "v_mul_f32 v173, v173, v127\n"
    "v_mul_f32 v174, v174, v127\n"
    "v_mul_f32 v175, v175, v127\n"
    "v_mul_f32 v188, v188, v127\n"
    "v_mul_f32 v189, v189, v127\n"
    "v_mul_f32 v190, v190, v127\n"
    "v_mul_f32 v191, v191, v127\n"
    "v_mul_f32 v204, v204, v127\n"
    "v_mul_f32 v205, v205, v127\n"
    "v_mul_f32 v206, v206, v127\n"
    "v_mul_f32 v207, v207, v127\n"
    "v_mul_f32 v220, v220, v127\n"
    "v_mul_f32 v221, v221, v127\n"
    "v_mul_f32 v222, v222, v127\n"
    "v_mul_f32 v223, v223, v127\n"
    "v_mul_f32 v236, v236, v127\n"
    "v_mul_f32 v237, v237, v127\n"
    "v_mul_f32 v238, v238, v127\n"
    "v_mul_f32 v239, v239, v127\n"
    "v_mul_f32 v252, v252, v127\n"
    "v_mul_f32 v253, v253, v127\n"
    "v_mul_f32 v254, v254, v127\n"
    "v_mul_f32 v255, v255, v127\n"
    "v_accvgpr_read_b32 v124, a140\n"
    "v_mul_f32 v124, v124, v127\n"
    "v_accvgpr_write_b32 a140, v124\n"
    "v_accvgpr_read_b32 v125, a141\n"
    "v_mul_f32 v125, v125, v127\n"
    "v_accvgpr_write_b32 a141, v125\n"
    "v_accvgpr_read_b32 v124, a142\n"
    "v_mul_f32 v124, v124, v127\n"
    "v_accvgpr_write_b32 a142, v124\n"
    "v_accvgpr_read_b32 v125, a143\n"
    "v_mul_f32 v125, v125, v127\n"
    "v_accvgpr_write_b32 a143, v125\n"
    "v_accvgpr_read_b32 v124, a156\n"
    "v_mul_f32 v124, v124, v127\n"
    "v_accvgpr_write_b32 a156, v124\n"
    "v_accvgpr_read_b32 v125, a157\n"
    "v_mul_f32 v125, v125, v127\n"
    "v_accvgpr_write_b32 a157, v125\n"
    "v_accvgpr_read_b32 v124, a158\n"
    "v_mul_f32 v124, v124, v127\n"
    "v_accvgpr_write_b32 a158, v124\n"
    "v_accvgpr_read_b32 v125, a159\n"
    "v_mul_f32 v125, v125, v127\n"
    "v_accvgpr_write_b32 a159, v125\n"
    "v_accvgpr_read_b32 v124, a172\n"
    "v_mul_f32 v124, v124, v127\n"
    "v_accvgpr_write_b32 a172, v124\n"
    "v_accvgpr_read_b32 v125, a173\n"
    "v_mul_f32 v125, v125, v127\n"
    "v_accvgpr_write_b32 a173, v125\n"
    "v_accvgpr_read_b32 v124, a174\n"
    "v_mul_f32 v124, v124, v127\n"
    "v_accvgpr_write_b32 a174, v124\n"
    "v_accvgpr_read_b32 v125, a175\n"
    "v_mul_f32 v125, v125, v127\n"
    "v_accvgpr_write_b32 a175, v125\n"
    "v_accvgpr_read_b32 v124, a188\n"
    "v_mul_f32 v124, v124, v127\n"
    "v_accvgpr_write_b32 a188, v124\n"
    "v_accvgpr_read_b32 v125, a189\n"
    "v_mul_f32 v125, v125, v127\n"
    "v_accvgpr_write_b32 a189, v125\n"
    "v_accvgpr_read_b32 v124, a190\n"
    "v_mul_f32 v124, v124, v127\n"
    "v_accvgpr_write_b32 a190, v124\n"
    "v_accvgpr_read_b32 v125, a191\n"
    "v_mul_f32 v125, v125, v127\n"
    "v_accvgpr_write_b32 a191, v125\n"
    "v_accvgpr_read_b32 v124, a204\n"
    "v_mul_f32 v124, v124, v127\n"
    "v_accvgpr_write_b32 a204, v124\n"
    "v_accvgpr_read_b32 v125, a205\n"
    "v_mul_f32 v125, v125, v127\n"
    "v_accvgpr_write_b32 a205, v125\n"
    "v_accvgpr_read_b32 v124, a206\n"
    "v_mul_f32 v124, v124, v127\n"
    "v_accvgpr_write_b32 a206, v124\n"
    "v_accvgpr_read_b32 v125, a207\n"
    "v_mul_f32 v125, v125, v127\n"
    "v_accvgpr_write_b32 a207, v125\n"
    "v_mul_f32 v76, v76, v127\n"
    "v_mul_f32 v77, v77, v127\n"
    "v_mul_f32 v78, v78, v127\n"
    "v_mul_f32 v79, v79, v127\n"
    "v_accvgpr_read_b32 v124, a236\n"
    "v_mul_f32 v124, v124, v127\n"
    "v_accvgpr_write_b32 a236, v124\n"
    "v_accvgpr_read_b32 v125, a237\n"
    "v_mul_f32 v125, v125, v127\n"
    "v_accvgpr_write_b32 a237, v125\n"
    "v_accvgpr_read_b32 v124, a238\n"
    "v_mul_f32 v124, v124, v127\n"
    "v_accvgpr_write_b32 a238, v124\n"
    "v_accvgpr_read_b32 v125, a239\n"
    "v_mul_f32 v125, v125, v127\n"
    "v_accvgpr_write_b32 a239, v125\n"
    "v_mul_f32 v92, v92, v127\n"
    "v_mul_f32 v93, v93, v127\n"
    "v_mul_f32 v94, v94, v127\n"
    "v_mul_f32 v95, v95, v127\n"
    "v_mov_b32 v20, 0\n"
    "v_mov_b32 v28, 0\n"
    "L_rare0R:\n"
    "s_add_u32 s74, s74, 4\n"
    "s_addc_u32 s75, s75, 0\n"
    "s_nop 3\n"
    "s_setpc_b64 s[74:75]\n"
    "L_rare1A:\n"
    "s_nop 15\n"
    "v_mbcnt_lo_u32_b32 v126, -1, 0\n"
    "v_mbcnt_hi_u32_b32 v126, -1, v126\n"
    "v_and_b32 v109, 15, v126\n"
    "v_lshlrev_b32 v109, 9, v109\n"
    "v_lshrrev_b32 v126, 4, v126\n"
    "v_lshl_add_u32 v109, v126, 4, v109\n"
    "s_add_u32 s72, s73, 8192\n"
    "s_and_b32 s83, s87, 0x1000\n"
    "s_and_b32 s84, s87, 0x6000\n"
    "s_xor_b32 s84, s84, s79\n"
    "v_xor_b32 v109, s83, v109\n"
    "buffer_load_dwordx4 v[40:43], v109, s[4:7], s84 offen offset:0\n"
    "v_xor_b32 v109, s83, v109\n"
    "buffer_load_dwordx4 v[124:127], v109, s[8:11], s73 offen offset:0\n"
    "s_waitcnt vmcnt(0)\n"
    "v_mfma_f32_16x16x32_bf16 v[32:35], v[124:127], v[40:43], 0\n"
    "s_nop 7\n"
    "buffer_load_dwordx4 v[124:127], v109, s[8:11], s72 offen offset:0\n"
    "s_waitcnt vmcnt(0)\n"
    "v_mfma_f32_16x16x32_bf16 v[36:39], v[124:127], v[40:43], 0\n"
    "s_nop 7\n"
    "v_xor_b32 v109, s83, v109\n"
    "buffer_load_dwordx4 v[40:43], v109, s[4:7], s84 offen offset:64\n"
    "v_xor_b32 v109, s83, v109\n"
    "buffer_load_dwordx4 v[124:127], v109, s[8:11], s73 offen offset:64\n"
    "s_waitcnt vmcnt(0)\n"
    "v_mfma_f32_16x16x32_bf16 v[32:35], v[124:127], v[40:43], v[32:35]\n"
    "s_nop 7\n"
    "buffer_load_dwordx4 v[124:127], v109, s[8:11], s72 offen offset:64\n"
    "s_waitcnt vmcnt(0)\n"
    "v_mfma_f32_16x16x32_bf16 v[36:39], v[124:127], v[40:43], v[36:39]\n"
    "s_nop 7\n"
    "v_xor_b32 v109, s83, v109\n"
    "buffer_load_dwordx4 v[40:43], v109, s[4:7], s84 offen offset:128\n"
    "v_xor_b32 v109, s83, v109\n"
    "buffer_load_dwordx4 v[124:127], v109, s[8:11], s73 offen offset:128\n"
    "s_waitcnt vmcnt(0)\n"
    "v_mfma_f32_16x16x32_bf16 v[32:35], v[124:127], v[40:43], v[32:35]\n"
    "s_nop 7\n"
    "buffer_load_dwordx4 v[124:127], v109, s[8:11], s72 offen offset:128\n"
    "s_waitcnt vmcnt(0)\n"
    "v_mfma_f32_16x16x32_bf16 v[36:39], v[124:127], v[40:43], v[36:39]\n"
    "s_nop 7\n"
    "v_xor_b32 v109, s83, v109\n"
    "buffer_load_dwordx4 v[40:43], v109, s[4:7], s84 offen offset:192\n"
    "v_xor_b32 v109, s83, v109\n"
    "buffer_load_dwordx4 v[124:127], v109, s[8:11], s73 offen offset:192\n"
    "s_waitcnt vmcnt(0)\n"
    "v_mfma_f32_16x16x32_bf16 v[32:35], v[124:127], v[40:43], v[32:35]\n"
    "s_nop 7\n"
    "buffer_load_dwordx4 v[124:127], v109, s[8:11], s72 offen offset:192\n"
    "s_waitcnt vmcnt(0)\n"
    "v_mfma_f32_16x16x32_bf16 v[36:39], v[124:127], v[40:43], v[36:39]\n"
    "s_nop 7\n"
    "v_xor_b32 v109, s83, v109\n"
    "buffer_load_dwordx4 v[40:43], v109, s[4:7], s84 offen offset:256\n"
    "v_xor_b32 v109, s83, v109\n"
    "buffer_load_dwordx4 v[124:127], v109, s[8:11], s73 offen offset:256\n"
    "s_waitcnt vmcnt(0)\n"
    "v_mfma_f32_16x16x32_bf16 v[32:35], v[124:127], v[40:43], v[32:35]\n"
    "s_nop 7\n"
    "buffer_load_dwordx4 v[124:127], v109, s[8:11], s72 offen offset:256\n"
    "s_waitcnt vmcnt(0)\n"
    "v_mfma_f32_16x16x32_bf16 v[36:39], v[124:127], v[40:43], v[36:39]\n"
    "s_nop 7\n"
    "v_xor_b32 v109, s83, v109\n"
    "buffer_load_dwordx4 v[40:43], v109, s[4:7], s84 offen offset:320\n"
    "v_xor_b32 v109, s83, v109\n"
    "buffer_load_dwordx4 v[124:127], v109, s[8:11], s73 offen offset:320\n"
    "s_waitcnt vmcnt(0)\n"
    "v_mfma_f32_16x16x32_bf16 v[32:35], v[124:127], v[40:43], v[32:35]\n"
    "s_nop 7\n"
    "buffer_load_dwordx4 v[124:127], v109, s[8:11], s72 offen offset:320\n"
    "s_waitcnt vmcnt(0)\n"
    "v_mfma_f32_16x16x32_bf16 v[36:39], v[124:127], v[40:43], v[36:39]\n"
    "s_nop 7\n"
    "v_xor_b32 v109, s83, v109\n"
    "buffer_load_dwordx4 v[40:43], v109, s[4:7], s84 offen offset:384\n"
    "v_xor_b32 v109, s83, v109\n"
    "buffer_load_dwordx4 v[124:127], v109, s[8:11], s73 offen offset:384\n"
    "s_waitcnt vmcnt(0)\n"
    "v_mfma_f32_16x16x32_bf16 v[32:35], v[124:127], v[40:43], v[32:35]\n"
    "s_nop 7\n"
    "buffer_load_dwordx4 v[124:127], v109, s[8:11], s72 offen offset:384\n"
    "s_waitcnt vmcnt(0)\n"
    "v_mfma_f32_16x16x32_bf16 v[36:39], v[124:127], v[40:43], v[36:39]\n"
    "s_nop 7\n"
    "v_xor_b32 v109, s83, v109\n"
    "buffer_load_dwordx4 v[40:43], v109, s[4:7], s84 offen offset:448\n"
    "v_xor_b32 v109, s83, v109\n"
    "buffer_load_dwordx4 v[124:127], v109, s[8:11], s73 offen offset:448\n"
    "s_waitcnt vmcnt(0)\n"
    "v_mfma_f32_16x16x32_bf16 v[32:35], v[124:127], v[40:43], v[32:35]\n"
    "s_nop 7\n"
    "buffer_load_dwordx4 v[124:127], v109, s[8:11], s72 offen offset:448\n"
    "s_waitcnt vmcnt(0)\n"
    "v_mfma_f32_16x16x32_bf16 v[36:39], v[124:127], v[40:43], v[36:39]\n"
    "s_nop 7\n"
    "s_add_u32 s79, s79, 8192\n"
    "s_nop 15\n"
    "v_max3_f32 v109, v32, v33, v34\n"
    "v_max3_f32 v109, v109, v35, v36\n"
    "v_max3_f32 v109, v109, v37, v38\n"
    "v_max_f32 v109, v109, v39\n"
    "v_mov_b32 v126, v109\n"
    "s_nop 1\n"
    "v_permlane32_swap_b32 v109, v126\n"
    "v_max_f32 v109, v109, v126\n"
    "v_mov_b32 v126, v109\n"
    "s_nop 1\n"
    "v_permlane16_swap_b32 v109, v126\n"
    "v_max_f32 v109, v109, v126\n"
    "v_fma_f32 v109, v109, s39, -v101\n"
    "v_add_f32 v109, s40, v109\n"
    "v_max_f32 v109, 0, v109\n"
    "v_ceil_f32 v109, v109\n"
    "v_add_f32 v101, v101, v109\n"
    "v_min_f32 v126, 0x43480000, v109\n"
    "v_cvt_i32_f32 v126, v126\n"
    "v_sub_u32 v126, 0, v126\n"
    "v_ldexp_f32 v127, 1.0, v126\n"
    "v_mul_f32 v105, v105, v127\n"
    "v_fma_f32 v32, v32, s39, -v101\n"
    "v_fma_f32 v33, v33, s39, -v101\n"
    "v_fma_f32 v34, v34, s39, -v101\n"
    "v_fma_f32 v35, v35, s39, -v101\n"
    "v_fma_f32 v36, v36, s39, -v101\n"
    "v_fma_f32 v37, v37, s39, -v101\n"
    "v_fma_f32 v38, v38, s39, -v101\n"
    "v_fma_f32 v39, v39, s39, -v101\n"
    "v_exp_f32 v32, v32\n"
    "v_exp_f32 v33, v33\n"
    "v_exp_f32 v34, v34\n"
    "v_exp_f32 v35, v35\n"
    "v_exp_f32 v36, v36\n"
    "v_exp_f32 v37, v37\n"
    "v_exp_f32 v38, v38\n"
    "v_exp_f32 v39, v39\n"
    "v_add_f32 v126, v32, v33\n"
    "v_add_f32 v126, v126, v34\n"
    "v_add_f32 v126, v126, v35\n"
    "v_add_f32 v126, v126, v36\n"
    "v_add_f32 v126, v126, v37\n"
    "v_add_f32 v126, v126, v38\n"
    "v_add_f32 v126, v126, v39\n"
    "v_add_f32 v105, v105, v126\n"
    "v_cvt_pk_bf16_f32 v32, v32, v33\n"
    "v_cvt_pk_bf16_f32 v33, v34, v35\n"
    "v_cvt_pk_bf16_f32 v34, v36, v37\n"
    "v_cvt_pk_bf16_f32 v35, v38, v39\n"
    "v_mul_f32 v128, v128, v127\n"
    "v_mul_f32 v129, v129, v127\n"
    "v_mul_f32 v130, v130, v127\n"
    "v_mul_f32 v131, v131, v127\n"
    "v_mul_f32 v144, v144, v127\n"
    "v_mul_f32 v145, v145, v127\n"
    "v_mul_f32 v146, v146, v127\n"
    "v_mul_f32 v147, v147, v127\n"
    "v_mul_f32 v160, v160, v127\n"
    "v_mul_f32 v161, v161, v127\n"
    "v_mul_f32 v162, v162, v127\n"
    "v_mul_f32 v163, v163, v127\n"
    "v_mul_f32 v176, v176, v127\n"
    "v_mul_f32 v177, v177, v127\n"
    "v_mul_f32 v178, v178, v127\n"
    "v_mul_f32 v179, v179, v127\n"
    "v_mul_f32 v192, v192, v127\n"
    "v_mul_f32 v193, v193, v127\n"
    "v_mul_f32 v194, v194, v127\n"
    "v_mul_f32 v195, v195, v127\n"
    "v_mul_f32 v208, v208, v127\n"
    "v_mul_f32 v209, v209, v127\n"
    "v_mul_f32 v210, v210, v127\n"
    "v_mul_f32 v211, v211, v127\n"
    "v_mul_f32 v224, v224, v127\n"
    "v_mul_f32 v225, v225, v127\n"
    "v_mul_f32 v226, v226, v127\n"
    "v_mul_f32 v227, v227, v127\n"
    "v_mul_f32 v240, v240, v127\n"
    "v_mul_f32 v241, v241, v127\n"
    "v_mul_f32 v242, v242, v127\n"
    "v_mul_f32 v243, v243, v127\n"
    "v_accvgpr_read_b32 v124, a128\n"
    "v_mul_f32 v124, v124, v127\n"
    "v_accvgpr_write_b32 a128, v124\n"
    "v_accvgpr_read_b32 v125, a129\n"
    "v_mul_f32 v125, v125, v127\n"
    "v_accvgpr_write_b32 a129, v125\n"
    "v_accvgpr_read_b32 v124, a130\n"
    "v_mul_f32 v124, v124, v127\n"
    "v_accvgpr_write_b32 a130, v124\n"
    "v_accvgpr_read_b32 v125, a131\n"
    "v_mul_f32 v125, v125, v127\n"
    "v_accvgpr_write_b32 a131, v125\n"
    "v_accvgpr_read_b32 v124, a144\n"
    "v_mul_f32 v124, v124, v127\n"
    "v_accvgpr_write_b32 a144, v124\n"
    "v_accvgpr_read_b32 v125, a145\n"
    "v_mul_f32 v125, v125, v127\n"
    "v_accvgpr_write_b32 a145, v125\n"
    "v_accvgpr_read_b32 v124, a146\n"
    "v_mul_f32 v124, v124, v127\n"
    "v_accvgpr_write_b32 a146, v124\n"
    "v_accvgpr_read_b32 v125, a147\n"
    "v_mul_f32 v125, v125, v127\n"
    "v_accvgpr_write_b32 a147, v125\n"
    "v_accvgpr_read_b32 v124, a160\n"
    "v_mul_f32 v124, v124, v127\n"
    "v_accvgpr_write_b32 a160, v124\n"
    "v_accvgpr_read_b32 v125, a161\n"
    "v_mul_f32 v125, v125, v127\n"
    "v_accvgpr_write_b32 a161, v125\n"
    "v_accvgpr_read_b32 v124, a162\n"
    "v_mul_f32 v124, v124, v127\n"
    "v_accvgpr_write_b32 a162, v124\n"
    "v_accvgpr_read_b32 v125, a163\n"
    "v_mul_f32 v125, v125, v127\n"
    "v_accvgpr_write_b32 a163, v125\n"
    "v_accvgpr_read_b32 v124, a176\n"
    "v_mul_f32 v124, v124, v127\n"
    "v_accvgpr_write_b32 a176, v124\n"
    "v_accvgpr_read_b32 v125, a177\n"
    "v_mul_f32 v125, v125, v127\n"
    "v_accvgpr_write_b32 a177, v125\n"
    "v_accvgpr_read_b32 v124, a178\n"
    "v_mul_f32 v124, v124, v127\n"
    "v_accvgpr_write_b32 a178, v124\n"
    "v_accvgpr_read_b32 v125, a179\n"
    "v_mul_f32 v125, v125, v127\n"
    "v_accvgpr_write_b32 a179, v125\n"
    "v_accvgpr_read_b32 v124, a192\n"
    "v_mul_f32 v124, v124, v127\n"
    "v_accvgpr_write_b32 a192, v124\n"
    "v_accvgpr_read_b32 v125, a193\n"
    "v_mul_f32 v125, v125, v127\n"
    "v_accvgpr_write_b32 a193, v125\n"
    "v_accvgpr_read_b32 v124, a194\n"
    "v_mul_f32 v124, v124, v127\n"
    "v_accvgpr_write_b32 a194, v124\n"
    "v_accvgpr_read_b32 v125, a195\n"
    "v_mul_f32 v125, v125, v127\n"
    "v_accvgpr_write_b32 a195, v125\n"
    "v_mul_f32 v64, v64, v127\n"
    "v_mul_f32 v65, v65, v127\n"
    "v_mul_f32 v66, v66, v127\n"
    "v_mul_f32 v67, v67, v127\n"
    "v_accvgpr_read_b32 v124, a224\n"
    "v_mul_f32 v124, v124, v127\n"
    "v_accvgpr_write_b32 a224, v124\n"
    "v_accvgpr_read_b32 v125, a225\n"
    "v_mul_f32 v125, v125, v127\n"
    "v_accvgpr_write_b32 a225, v125\n"
    "v_accvgpr_read_b32 v124, a226\n"
    "v_mul_f32 v124, v124, v127\n"
    "v_accvgpr_write_b32 a226, v124\n"
    "v_accvgpr_read_b32 v125, a227\n"
    "v_mul_f32 v125, v125, v127\n"
    "v_accvgpr_write_b32 a227, v125\n"
    "v_mul_f32 v80, v80, v127\n"
    "v_mul_f32 v81, v81, v127\n"
    "v_mul_f32 v82, v82, v127\n"
    "v_mul_f32 v83, v83, v127\n"
    "v_mbcnt_lo_u32_b32 v126, -1, 0\n"
    "v_mbcnt_hi_u32_b32 v126, -1, v126\n"
    "v_and_b32 v109, 15, v126\n"
    "v_lshlrev_b32 v109, 9, v109\n"
    "v_lshrrev_b32 v126, 4, v126\n"
    "v_lshl_add_u32 v109, v126, 4, v109\n"
    "s_add_u32 s72, s73, 8192\n"
    "s_and_b32 s83, s87, 0x1000\n"
    "s_and_b32 s84, s87, 0x6000\n"
    "s_xor_b32 s84, s84, s79\n"
    "v_xor_b32 v109, s83, v109\n"
    "buffer_load_dwordx4 v[36:39], v109, s[4:7], s84 offen offset:0\n"
    "v_xor_b32 v109, s83, v109\n"
    "buffer_load_dwordx4 v[124:127], v109, s[8:11], s73 offen offset:0\n"
    "s_waitcnt vmcnt(0)\n"
    "v_mfma_f32_16x16x32_bf16 v[40:43], v[124:127], v[36:39], 0\n"
    "s_nop 7\n"
    "buffer_load_dwordx4 v[124:127], v109, s[8:11], s72 offen offset:0\n"
    "s_waitcnt vmcnt(0)\n"
    "v_mfma_f32_16x16x32_bf16 v[44:47], v[124:127], v[36:39], 0\n"
    "s_nop 7\n"
    "v_xor_b32 v109, s83, v109\n"
    "buffer_load_dwordx4 v[36:39], v109, s[4:7], s84 offen offset:64\n"
    "v_xor_b32 v109, s83, v109\n"
    "buffer_load_dwordx4 v[124:127], v109, s[8:11], s73 offen offset:64\n"
    "s_waitcnt vmcnt(0)\n"
    "v_mfma_f32_16x16x32_bf16 v[40:43], v[124:127], v[36:39], v[40:43]\n"
    "s_nop 7\n"
    "buffer_load_dwordx4 v[124:127], v109, s[8:11], s72 offen offset:64\n"
    "s_waitcnt vmcnt(0)\n"
    "v_mfma_f32_16x16x32_bf16 v[44:47], v[124:127], v[36:39], v[44:47]\n"
    "s_nop 7\n"
    "v_xor_b32 v109, s83, v109\n"
    "buffer_load_dwordx4 v[36:39], v109, s[4:7], s84 offen offset:128\n"
    "v_xor_b32 v109, s83, v109\n"
    "buffer_load_dwordx4 v[124:127], v109, s[8:11], s73 offen offset:128\n"
    "s_waitcnt vmcnt(0)\n"
    "v_mfma_f32_16x16x32_bf16 v[40:43], v[124:127], v[36:39], v[40:43]\n"
    "s_nop 7\n"
    "buffer_load_dwordx4 v[124:127], v109, s[8:11], s72 offen offset:128\n"
    "s_waitcnt vmcnt(0)\n"
    "v_mfma_f32_16x16x32_bf16 v[44:47], v[124:127], v[36:39], v[44:47]\n"
    "s_nop 7\n"
    "v_xor_b32 v109, s83, v109\n"
    "buffer_load_dwordx4 v[36:39], v109, s[4:7], s84 offen offset:192\n"
    "v_xor_b32 v109, s83, v109\n"
    "buffer_load_dwordx4 v[124:127], v109, s[8:11], s73 offen offset:192\n"
    "s_waitcnt vmcnt(0)\n"
    "v_mfma_f32_16x16x32_bf16 v[40:43], v[124:127], v[36:39], v[40:43]\n"
    "s_nop 7\n"
    "buffer_load_dwordx4 v[124:127], v109, s[8:11], s72 offen offset:192\n"
    "s_waitcnt vmcnt(0)\n"
    "v_mfma_f32_16x16x32_bf16 v[44:47], v[124:127], v[36:39], v[44:47]\n"
    "s_nop 7\n"
    "v_xor_b32 v109, s83, v109\n"
    "buffer_load_dwordx4 v[36:39], v109, s[4:7], s84 offen offset:256\n"
    "v_xor_b32 v109, s83, v109\n"
    "buffer_load_dwordx4 v[124:127], v109, s[8:11], s73 offen offset:256\n"
    "s_waitcnt vmcnt(0)\n"
    "v_mfma_f32_16x16x32_bf16 v[40:43], v[124:127], v[36:39], v[40:43]\n"
    "s_nop 7\n"
    "buffer_load_dwordx4 v[124:127], v109, s[8:11], s72 offen offset:256\n"
    "s_waitcnt vmcnt(0)\n"
    "v_mfma_f32_16x16x32_bf16 v[44:47], v[124:127], v[36:39], v[44:47]\n"
    "s_nop 7\n"
    "v_xor_b32 v109, s83, v109\n"
    "buffer_load_dwordx4 v[36:39], v109, s[4:7], s84 offen offset:320\n"
    "v_xor_b32 v109, s83, v109\n"
    "buffer_load_dwordx4 v[124:127], v109, s[8:11], s73 offen offset:320\n"
    "s_waitcnt vmcnt(0)\n"
    "v_mfma_f32_16x16x32_bf16 v[40:43], v[124:127], v[36:39], v[40:43]\n"
    "s_nop 7\n"
    "buffer_load_dwordx4 v[124:127], v109, s[8:11], s72 offen offset:320\n"
    "s_waitcnt vmcnt(0)\n"
    "v_mfma_f32_16x16x32_bf16 v[44:47], v[124:127], v[36:39], v[44:47]\n"
    "s_nop 7\n"
    "v_xor_b32 v109, s83, v109\n"
    "buffer_load_dwordx4 v[36:39], v109, s[4:7], s84 offen offset:384\n"
    "v_xor_b32 v109, s83, v109\n"
    "buffer_load_dwordx4 v[124:127], v109, s[8:11], s73 offen offset:384\n"
    "s_waitcnt vmcnt(0)\n"
    "v_mfma_f32_16x16x32_bf16 v[40:43], v[124:127], v[36:39], v[40:43]\n"
    "s_nop 7\n"
    "buffer_load_dwordx4 v[124:127], v109, s[8:11], s72 offen offset:384\n"
    "s_waitcnt vmcnt(0)\n"
    "v_mfma_f32_16x16x32_bf16 v[44:47], v[124:127], v[36:39], v[44:47]\n"
    "s_nop 7\n"
    "v_xor_b32 v109, s83, v109\n"
    "buffer_load_dwordx4 v[36:39], v109, s[4:7], s84 offen offset:448\n"
    "v_xor_b32 v109, s83, v109\n"
    "buffer_load_dwordx4 v[124:127], v109, s[8:11], s73 offen offset:448\n"
    "s_waitcnt vmcnt(0)\n"
    "v_mfma_f32_16x16x32_bf16 v[40:43], v[124:127], v[36:39], v[40:43]\n"
    "s_nop 7\n"
    "buffer_load_dwordx4 v[124:127], v109, s[8:11], s72 offen offset:448\n"
    "s_waitcnt vmcnt(0)\n"
    "v_mfma_f32_16x16x32_bf16 v[44:47], v[124:127], v[36:39], v[44:47]\n"
    "s_nop 7\n"
    "s_add_u32 s79, s79, 8192\n"
    "s_nop 15\n"
    "v_max3_f32 v109, v40, v41, v42\n"
    "v_max3_f32 v109, v109, v43, v44\n"
    "v_max3_f32 v109, v109, v45, v46\n"
    "v_max_f32 v109, v109, v47\n"
    "v_mov_b32 v126, v109\n"
    "s_nop 1\n"
    "v_permlane32_swap_b32 v109, v126\n"
    "v_max_f32 v109, v109, v126\n"
    "v_mov_b32 v126, v109\n"
    "s_nop 1\n"
    "v_permlane16_swap_b32 v109, v126\n"
    "v_max_f32 v109, v109, v126\n"
    "v_fma_f32 v109, v109, s39, -v102\n"
    "v_add_f32 v109, s40, v109\n"
    "v_max_f32 v109, 0, v109\n"
    "v_ceil_f32 v109, v109\n"
    "v_add_f32 v102, v102, v109\n"
    "v_min_f32 v126, 0x43480000, v109\n"
    "v_cvt_i32_f32 v126, v126\n"
    "v_sub_u32 v126, 0, v126\n"
    "v_ldexp_f32 v127, 1.0, v126\n"
    "v_mul_f32 v106, v106, v127\n"
    "v_fma_f32 v40, v40, s39, -v102\n"
    "v_fma_f32 v41, v41, s39, -v102\n"
    "v_fma_f32 v42, v42, s39, -v102\n"
    "v_fma_f32 v43, v43, s39, -v102\n"
    "v_fma_f32 v44, v44, s39, -v102\n"
    "v_fma_f32 v45, v45, s39, -v102\n"
    "v_fma_f32 v46, v46, s39, -v102\n"
    "v_fma_f32 v47, v47, s39, -v102\n"
    "v_exp_f32 v40, v40\n"
    "v_exp_f32 v41, v41\n"
    "v_exp_f32 v42, v42\n"
    "v_exp_f32 v43, v43\n"
    "v_exp_f32 v44, v44\n"
    "v_exp_f32 v45, v45\n"
    "v_exp_f32 v46, v46\n"
    "v_exp_f32 v47, v47\n"
    "v_add_f32 v126, v40, v41\n"
    "v_add_f32 v126, v126, v42\n"
    "v_add_f32 v126, v126, v43\n"
    "v_add_f32 v126, v126, v44\n"
    "v_add_f32 v126, v126, v45\n"
    "v_add_f32 v126, v126, v46\n"
    "v_add_f32 v126, v126, v47\n"
    "v_add_f32 v106, v106, v126\n"
    "v_cvt_pk_bf16_f32 v40, v40, v41\n"
    "v_cvt_pk_bf16_f32 v41, v42, v43\n"
    "v_cvt_pk_bf16_f32 v42, v44, v45\n"
    "v_cvt_pk_bf16_f32 v43, v46, v47\n"
    "v_mul_f32 v132, v132, v127\n"
    "v_mul_f32 v133, v133, v127\n"
    "v_mul_f32 v134, v134, v127\n"
    "v_mul_f32 v135, v135, v127\n"
    "v_mul_f32 v148, v148, v127\n"
    "v_mul_f32 v149, v149, v127\n"
    "v_mul_f32 v150, v150, v127\n"
    "v_mul_f32 v151, v151, v127\n"
    "v_mul_f32 v164, v164, v127\n"
    "v_mul_f32 v165, v165, v127\n"
    "v_mul_f32 v166, v166, v127\n"
    "v_mul_f32 v167, v167, v127\n"
    "v_mul_f32 v180, v180, v127\n"
    "v_mul_f32 v181, v181, v127\n"
    "v_mul_f32 v182, v182, v127\n"
    "v_mul_f32 v183, v183, v127\n"
    "v_mul_f32 v196, v196, v127\n"
    "v_mul_f32 v197, v197, v127\n"
    "v_mul_f32 v198, v198, v127\n"
    "v_mul_f32 v199, v199, v127\n"
    "v_mul_f32 v212, v212, v127\n"
    "v_mul_f32 v213, v213, v127\n"
    "v_mul_f32 v214, v214, v127\n"
    "v_mul_f32 v215, v215, v127\n"
    "v_mul_f32 v228, v228, v127\n"
    "v_mul_f32 v229, v229, v127\n"
    "v_mul_f32 v230, v230, v127\n"
    "v_mul_f32 v231, v231, v127\n"
    "v_mul_f32 v244, v244, v127\n"
    "v_mul_f32 v245, v245, v127\n"
    "v_mul_f32 v246, v246, v127\n"
    "v_mul_f32 v247, v247, v127\n"
    "v_accvgpr_read_b32 v124, a132\n"
    "v_mul_f32 v124, v124, v127\n"
    "v_accvgpr_write_b32 a132, v124\n"
    "v_accvgpr_read_b32 v125, a133\n"
    "v_mul_f32 v125, v125, v127\n"
    "v_accvgpr_write_b32 a133, v125\n"
    "v_accvgpr_read_b32 v124, a134\n"
    "v_mul_f32 v124, v124, v127\n"
    "v_accvgpr_write_b32 a134, v124\n"
    "v_accvgpr_read_b32 v125, a135\n"
    "v_mul_f32 v125, v125, v127\n"
    "v_accvgpr_write_b32 a135, v125\n"
    "v_accvgpr_read_b32 v124, a148\n"
    "v_mul_f32 v124, v124, v127\n"
    "v_accvgpr_write_b32 a148, v124\n"
    "v_accvgpr_read_b32 v125, a149\n"
    "v_mul_f32 v125, v125, v127\n"
    "v_accvgpr_write_b32 a149, v125\n"
    "v_accvgpr_read_b32 v124, a150\n"
    "v_mul_f32 v124, v124, v127\n"
    "v_accvgpr_write_b32 a150, v124\n"
    "v_accvgpr_read_b32 v125, a151\n"
    "v_mul_f32 v125, v125, v127\n"
    "v_accvgpr_write_b32 a151, v125\n"
    "v_accvgpr_read_b32 v124, a164\n"
    "v_mul_f32 v124, v124, v127\n"
    "v_accvgpr_write_b32 a164, v124\n"
    "v_accvgpr_read_b32 v125, a165\n"
    "v_mul_f32 v125, v125, v127\n"
    "v_accvgpr_write_b32 a165, v125\n"
    "v_accvgpr_read_b32 v124, a166\n"
    "v_mul_f32 v124, v124, v127\n"
    "v_accvgpr_write_b32 a166, v124\n"
    "v_accvgpr_read_b32 v125, a167\n"
    "v_mul_f32 v125, v125, v127\n"
    "v_accvgpr_write_b32 a167, v125\n"
    "v_accvgpr_read_b32 v124, a180\n"
    "v_mul_f32 v124, v124, v127\n"
    "v_accvgpr_write_b32 a180, v124\n"
    "v_accvgpr_read_b32 v125, a181\n"
    "v_mul_f32 v125, v125, v127\n"
    "v_accvgpr_write_b32 a181, v125\n"
    "v_accvgpr_read_b32 v124, a182\n"
    "v_mul_f32 v124, v124, v127\n"
    "v_accvgpr_write_b32 a182, v124\n"
    "v_accvgpr_read_b32 v125, a183\n"
    "v_mul_f32 v125, v125, v127\n"
    "v_accvgpr_write_b32 a183, v125\n"
    "v_accvgpr_read_b32 v124, a196\n"
    "v_mul_f32 v124, v124, v127\n"
    "v_accvgpr_write_b32 a196, v124\n"
    "v_accvgpr_read_b32 v125, a197\n"
    "v_mul_f32 v125, v125, v127\n"
    "v_accvgpr_write_b32 a197, v125\n"
    "v_accvgpr_read_b32 v124, a198\n"
    "v_mul_f32 v124, v124, v127\n"
    "v_accvgpr_write_b32 a198, v124\n"
    "v_accvgpr_read_b32 v125, a199\n"
    "v_mul_f32 v125, v125, v127\n"
    "v_accvgpr_write_b32 a199, v125\n"
    "v_mul_f32 v68, v68, v127\n"
    "v_mul_f32 v69, v69, v127\n"
    "v_mul_f32 v70, v70, v127\n"
    "v_mul_f32 v71, v71, v127\n"
    "v_accvgpr_read_b32 v124, a228\n"
    "v_mul_f32 v124, v124, v127\n"
    "v_accvgpr_write_b32 a228, v124\n"
    "v_accvgpr_read_b32 v125, a229\n"
    "v_mul_f32 v125, v125, v127\n"
    "v_accvgpr_write_b32 a229, v125\n"
    "v_accvgpr_read_b32 v124, a230\n"
    "v_mul_f32 v124, v124, v127\n"
    "v_accvgpr_write_b32 a230, v124\n"
    "v_accvgpr_read_b32 v125, a231\n"
    "v_mul_f32 v125, v125, v127\n"
    "v_accvgpr_write_b32 a231, v125\n"
    "v_mul_f32 v84, v84, v127\n"
    "v_mul_f32 v85, v85, v127\n"
    "v_mul_f32 v86, v86, v127\n"
    "v_mul_f32 v87, v87, v127\n"
    "v_mov_b32 v36, 0\n"
    "v_mov_b32 v44, 0\n"
    "s_cmp_eq_u32 s71, 1\n"
    "s_cbranch_scc1 L_rare1R\n"
    "L_rare1B:\n"
    "s_nop 15\n"
    "v_mbcnt_lo_u32_b32 v126, -1, 0\n"
    "v_mbcnt_hi_u32_b32 v126, -1, v126\n"
    "v_and_b32 v109, 15, v126\n"
    "v_lshlrev_b32 v109, 9, v109\n"
    "v_lshrrev_b32 v126, 4, v126\n"
    "v_lshl_add_u32 v109, v126, 4, v109\n"
    "s_add_u32 s72, s73, 8192\n"
    "s_and_b32 s83, s87, 0x1000\n"
    "s_and_b32 s84, s87, 0x6000\n"
    "s_xor_b32 s84, s84, s79\n"
    "v_xor_b32 v109, s83, v109\n"
    "buffer_load_dwordx4 v[56:59], v109, s[4:7], s84 offen offset:0\n"
    "v_xor_b32 v109, s83, v109\n"
    "buffer_load_dwordx4 v[124:127], v109, s[8:11], s73 offen offset:0\n"
    "s_waitcnt vmcnt(0)\n"
    "v_mfma_f32_16x16x32_bf16 v[48:51], v[124:127], v[56:59], 0\n"
    "s_nop 7\n"
    "buffer_load_dwordx4 v[124:127], v109, s[8:11], s72 offen offset:0\n"
    "s_waitcnt vmcnt(0)\n"
    "v_mfma_f32_16x16x32_bf16 v[52:55], v[124:127], v[56:59], 0\n"
    "s_nop 7\n"
    "v_xor_b32 v109, s83, v109\n"
    "buffer_load_dwordx4 v[56:59], v109, s[4:7], s84 offen offset:64\n"
    "v_xor_b32 v109, s83, v109\n"
    "buffer_load_dwordx4 v[124:127], v109, s[8:11], s73 offen offset:64\n"
    "s_waitcnt vmcnt(0)\n"
    "v_mfma_f32_16x16x32_bf16 v[48:51], v[124:127], v[56:59], v[48:51]\n"
    "s_nop 7\n"
    "buffer_load_dwordx4 v[124:127], v109, s[8:11], s72 offen offset:64\n"
    "s_waitcnt vmcnt(0)\n"
    "v_mfma_f32_16x16x32_bf16 v[52:55], v[124:127], v[56:59], v[52:55]\n"
    "s_nop 7\n"
    "v_xor_b32 v109, s83, v109\n"
    "buffer_load_dwordx4 v[56:59], v109, s[4:7], s84 offen offset:128\n"
    "v_xor_b32 v109, s83, v109\n"
    "buffer_load_dwordx4 v[124:127], v109, s[8:11], s73 offen offset:128\n"
    "s_waitcnt vmcnt(0)\n"
    "v_mfma_f32_16x16x32_bf16 v[48:51], v[124:127], v[56:59], v[48:51]\n"
    "s_nop 7\n"
    "buffer_load_dwordx4 v[124:127], v109, s[8:11], s72 offen offset:128\n"
    "s_waitcnt vmcnt(0)\n"
    "v_mfma_f32_16x16x32_bf16 v[52:55], v[124:127], v[56:59], v[52:55]\n"
    "s_nop 7\n"
    "v_xor_b32 v109, s83, v109\n"
    "buffer_load_dwordx4 v[56:59], v109, s[4:7], s84 offen offset:192\n"
    "v_xor_b32 v109, s83, v109\n"
    "buffer_load_dwordx4 v[124:127], v109, s[8:11], s73 offen offset:192\n"
    "s_waitcnt vmcnt(0)\n"
    "v_mfma_f32_16x16x32_bf16 v[48:51], v[124:127], v[56:59], v[48:51]\n"
    "s_nop 7\n"
    "buffer_load_dwordx4 v[124:127], v109, s[8:11], s72 offen offset:192\n"
    "s_waitcnt vmcnt(0)\n"
    "v_mfma_f32_16x16x32_bf16 v[52:55], v[124:127], v[56:59], v[52:55]\n"
    "s_nop 7\n"
    "v_xor_b32 v109, s83, v109\n"
    "buffer_load_dwordx4 v[56:59], v109, s[4:7], s84 offen offset:256\n"
    "v_xor_b32 v109, s83, v109\n"
    "buffer_load_dwordx4 v[124:127], v109, s[8:11], s73 offen offset:256\n"
    "s_waitcnt vmcnt(0)\n"
    "v_mfma_f32_16x16x32_bf16 v[48:51], v[124:127], v[56:59], v[48:51]\n"
    "s_nop 7\n"
    "buffer_load_dwordx4 v[124:127], v109, s[8:11], s72 offen offset:256\n"
    "s_waitcnt vmcnt(0)\n"
    "v_mfma_f32_16x16x32_bf16 v[52:55], v[124:127], v[56:59], v[52:55]\n"
    "s_nop 7\n"
    "v_xor_b32 v109, s83, v109\n"
    "buffer_load_dwordx4 v[56:59], v109, s[4:7], s84 offen offset:320\n"
    "v_xor_b32 v109, s83, v109\n"
    "buffer_load_dwordx4 v[124:127], v109, s[8:11], s73 offen offset:320\n"
    "s_waitcnt vmcnt(0)\n"
    "v_mfma_f32_16x16x32_bf16 v[48:51], v[124:127], v[56:59], v[48:51]\n"
    "s_nop 7\n"
    "buffer_load_dwordx4 v[124:127], v109, s[8:11], s72 offen offset:320\n"
    "s_waitcnt vmcnt(0)\n"
    "v_mfma_f32_16x16x32_bf16 v[52:55], v[124:127], v[56:59], v[52:55]\n"
    "s_nop 7\n"
    "v_xor_b32 v109, s83, v109\n"
    "buffer_load_dwordx4 v[56:59], v109, s[4:7], s84 offen offset:384\n"
    "v_xor_b32 v109, s83, v109\n"
    "buffer_load_dwordx4 v[124:127], v109, s[8:11], s73 offen offset:384\n"
    "s_waitcnt vmcnt(0)\n"
    "v_mfma_f32_16x16x32_bf16 v[48:51], v[124:127], v[56:59], v[48:51]\n"
    "s_nop 7\n"
    "buffer_load_dwordx4 v[124:127], v109, s[8:11], s72 offen offset:384\n"
    "s_waitcnt vmcnt(0)\n"
    "v_mfma_f32_16x16x32_bf16 v[52:55], v[124:127], v[56:59], v[52:55]\n"
    "s_nop 7\n"
    "v_xor_b32 v109, s83, v109\n"
    "buffer_load_dwordx4 v[56:59], v109, s[4:7], s84 offen offset:448\n"
    "v_xor_b32 v109, s83, v109\n"
    "buffer_load_dwordx4 v[124:127], v109, s[8:11], s73 offen offset:448\n"
    "s_waitcnt vmcnt(0)\n"
    "v_mfma_f32_16x16x32_bf16 v[48:51], v[124:127], v[56:59], v[48:51]\n"
    "s_nop 7\n"
    "buffer_load_dwordx4 v[124:127], v109, s[8:11], s72 offen offset:448\n"
    "s_waitcnt vmcnt(0)\n"
    "v_mfma_f32_16x16x32_bf16 v[52:55], v[124:127], v[56:59], v[52:55]\n"
    "s_nop 7\n"
    "s_add_u32 s79, s79, 8192\n"
    "s_nop 15\n"
    "v_max3_f32 v109, v48, v49, v50\n"
    "v_max3_f32 v109, v109, v51, v52\n"
    "v_max3_f32 v109, v109, v53, v54\n"
    "v_max_f32 v109, v109, v55\n"
    "v_mov_b32 v126, v109\n"
    "s_nop 1\n"
    "v_permlane32_swap_b32 v109, v126\n"
    "v_max_f32 v109, v109, v126\n"
    "v_mov_b32 v126, v109\n"
    "s_nop 1\n"
    "v_permlane16_swap_b32 v109, v126\n"
    "v_max_f32 v109, v109, v126\n"
    "v_fma_f32 v109, v109, s39, -v103\n"
    "v_add_f32 v109, s40, v109\n"
    "v_max_f32 v109, 0, v109\n"
    "v_ceil_f32 v109, v109\n"
    "v_add_f32 v103, v103, v109\n"
    "v_min_f32 v126, 0x43480000, v109\n"
    "v_cvt_i32_f32 v126, v126\n"
    "v_sub_u32 v126, 0, v126\n"
    "v_ldexp_f32 v127, 1.0, v126\n"
    "v_mul_f32 v107, v107, v127\n"
    "v_fma_f32 v48, v48, s39, -v103\n"
    "v_fma_f32 v49, v49, s39, -v103\n"
    "v_fma_f32 v50, v50, s39, -v103\n"
    "v_fma_f32 v51, v51, s39, -v103\n"
    "v_fma_f32 v52, v52, s39, -v103\n"
    "v_fma_f32 v53, v53, s39, -v103\n"
    "v_fma_f32 v54, v54, s39, -v103\n"
    "v_fma_f32 v55, v55, s39, -v103\n"
    "v_exp_f32 v48, v48\n"
    "v_exp_f32 v49, v49\n"
    "v_exp_f32 v50, v50\n"
    "v_exp_f32 v51, v51\n"
    "v_exp_f32 v52, v52\n"
    "v_exp_f32 v53, v53\n"
    "v_exp_f32 v54, v54\n"
    "v_exp_f32 v55, v55\n"
    "v_add_f32 v126, v48, v49\n"
    "v_add_f32 v126, v126, v50\n"
    "v_add_f32 v126, v126, v51\n"
    "v_add_f32 v126, v126, v52\n"
    "v_add_f32 v126, v126, v53\n"
    "v_add_f32 v126, v126, v54\n"
    "v_add_f32 v126, v126, v55\n"
    "v_add_f32 v107, v107, v126\n"
    "v_cvt_pk_bf16_f32 v48, v48, v49\n"
    "v_cvt_pk_bf16_f32 v49, v50, v51\n"
    "v_cvt_pk_bf16_f32 v50, v52, v53\n"
    "v_cvt_pk_bf16_f32 v51, v54, v55\n"
    "v_mul_f32 v136, v136, v127\n"
    "v_mul_f32 v137, v137, v127\n"
    "v_mul_f32 v138, v138, v127\n"
    "v_mul_f32 v139, v139, v127\n"
    "v_mul_f32 v152, v152, v127\n"
    "v_mul_f32 v153, v153, v127\n"
    "v_mul_f32 v154, v154, v127\n"
    "v_mul_f32 v155, v155, v127\n"
    "v_mul_f32 v168, v168, v127\n"
    "v_mul_f32 v169, v169, v127\n"
    "v_mul_f32 v170, v170, v127\n"
    "v_mul_f32 v171, v171, v127\n"
    "v_mul_f32 v184, v184, v127\n"
    "v_mul_f32 v185, v185, v127\n"
    "v_mul_f32 v186, v186, v127\n"
    "v_mul_f32 v187, v187, v127\n"
    "v_mul_f32 v200, v200, v127\n"
    "v_mul_f32 v201, v201, v127\n"
    "v_mul_f32 v202, v202, v127\n"
    "v_mul_f32 v203, v203, v127\n"
    "v_mul_f32 v216, v216, v127\n"
    "v_mul_f32 v217, v217, v127\n"
    "v_mul_f32 v218, v218, v127\n"
    "v_mul_f32 v219, v219, v127\n"
    "v_mul_f32 v232, v232, v127\n"
    "v_mul_f32 v233, v233, v127\n"
    "v_mul_f32 v234, v234, v127\n"
    "v_mul_f32 v235, v235, v127\n"
    "v_mul_f32 v248, v248, v127\n"
    "v_mul_f32 v249, v249, v127\n"
    "v_mul_f32 v250, v250, v127\n"
    "v_mul_f32 v251, v251, v127\n"
    "v_accvgpr_read_b32 v124, a136\n"
    "v_mul_f32 v124, v124, v127\n"
    "v_accvgpr_write_b32 a136, v124\n"
    "v_accvgpr_read_b32 v125, a137\n"
    "v_mul_f32 v125, v125, v127\n"
    "v_accvgpr_write_b32 a137, v125\n"
    "v_accvgpr_read_b32 v124, a138\n"
    "v_mul_f32 v124, v124, v127\n"
    "v_accvgpr_write_b32 a138, v124\n"
    "v_accvgpr_read_b32 v125, a139\n"
    "v_mul_f32 v125, v125, v127\n"
    "v_accvgpr_write_b32 a139, v125\n"
    "v_accvgpr_read_b32 v124, a152\n"
    "v_mul_f32 v124, v124, v127\n"
    "v_accvgpr_write_b32 a152, v124\n"
    "v_accvgpr_read_b32 v125, a153\n"
    "v_mul_f32 v125, v125, v127\n"
    "v_accvgpr_write_b32 a153, v125\n"
    "v_accvgpr_read_b32 v124, a154\n"
    "v_mul_f32 v124, v124, v127\n"
    "v_accvgpr_write_b32 a154, v124\n"
    "v_accvgpr_read_b32 v125, a155\n"
    "v_mul_f32 v125, v125, v127\n"
    "v_accvgpr_write_b32 a155, v125\n"
    "v_accvgpr_read_b32 v124, a168\n"
    "v_mul_f32 v124, v124, v127\n"
    "v_accvgpr_write_b32 a168, v124\n"
    "v_accvgpr_read_b32 v125, a169\n"
    "v_mul_f32 v125, v125, v127\n"
    "v_accvgpr_write_b32 a169, v125\n"
    "v_accvgpr_read_b32 v124, a170\n"
    "v_mul_f32 v124, v124, v127\n"
    "v_accvgpr_write_b32 a170, v124\n"
    "v_accvgpr_read_b32 v125, a171\n"
    "v_mul_f32 v125, v125, v127\n"
    "v_accvgpr_write_b32 a171, v125\n"
    "v_accvgpr_read_b32 v124, a184\n"
    "v_mul_f32 v124, v124, v127\n"
    "v_accvgpr_write_b32 a184, v124\n"
    "v_accvgpr_read_b32 v125, a185\n"
    "v_mul_f32 v125, v125, v127\n"
    "v_accvgpr_write_b32 a185, v125\n"
    "v_accvgpr_read_b32 v124, a186\n"
    "v_mul_f32 v124, v124, v127\n"
    "v_accvgpr_write_b32 a186, v124\n"
    "v_accvgpr_read_b32 v125, a187\n"
    "v_mul_f32 v125, v125, v127\n"
    "v_accvgpr_write_b32 a187, v125\n"
    "v_accvgpr_read_b32 v124, a200\n"
    "v_mul_f32 v124, v124, v127\n"
    "v_accvgpr_write_b32 a200, v124\n"
    "v_accvgpr_read_b32 v125, a201\n"
    "v_mul_f32 v125, v125, v127\n"
    "v_accvgpr_write_b32 a201, v125\n"
    "v_accvgpr_read_b32 v124, a202\n"
    "v_mul_f32 v124, v124, v127\n"
    "v_accvgpr_write_b32 a202, v124\n"
    "v_accvgpr_read_b32 v125, a203\n"
    "v_mul_f32 v125, v125, v127\n"
    "v_accvgpr_write_b32 a203, v125\n"
    "v_mul_f32 v72, v72, v127\n"
    "v_mul_f32 v73, v73, v127\n"
    "v_mul_f32 v74, v74, v127\n"
    "v_mul_f32 v75, v75, v127\n"
    "v_accvgpr_read_b32 v124, a232\n"
    "v_mul_f32 v124, v124, v127\n"
    "v_accvgpr_write_b32 a232, v124\n"
    "v_accvgpr_read_b32 v125, a233\n"
    "v_mul_f32 v125, v125, v127\n"
    "v_accvgpr_write_b32 a233, v125\n"
    "v_accvgpr_read_b32 v124, a234\n"
    "v_mul_f32 v124, v124, v127\n"
    "v_accvgpr_write_b32 a234, v124\n"
    "v_accvgpr_read_b32 v125, a235\n"
    "v_mul_f32 v125, v125, v127\n"
    "v_accvgpr_write_b32 a235, v125\n"
    "v_mul_f32 v88, v88, v127\n"
    "v_mul_f32 v89, v89, v127\n"
    "v_mul_f32 v90, v90, v127\n"
    "v_mul_f32 v91, v91, v127\n"
    "v_mbcnt_lo_u32_b32 v126, -1, 0\n"
    "v_mbcnt_hi_u32_b32 v126, -1, v126\n"
    "v_and_b32 v109, 15, v126\n"
    "v_lshlrev_b32 v109, 9, v109\n"
    "v_lshrrev_b32 v126, 4, v126\n"
    "v_lshl_add_u32 v109, v126, 4, v109\n"
    "s_add_u32 s72, s73, 8192\n"
    "s_and_b32 s83, s87, 0x1000\n"
    "s_and_b32 s84, s87, 0x6000\n"
    "s_xor_b32 s84, s84, s79\n"
    "v_xor_b32 v109, s83, v109\n"
    "buffer_load_dwordx4 v[52:55], v109, s[4:7], s84 offen offset:0\n"
    "v_xor_b32 v109, s83, v109\n"
    "buffer_load_dwordx4 v[124:127], v109, s[8:11], s73 offen offset:0\n"
    "s_waitcnt vmcnt(0)\n"
    "v_mfma_f32_16x16x32_bf16 v[56:59], v[124:127], v[52:55], 0\n"
    "s_nop 7\n"
    "buffer_load_dwordx4 v[124:127], v109, s[8:11], s72 offen offset:0\n"
    "s_waitcnt vmcnt(0)\n"
    "v_mfma_f32_16x16x32_bf16 v[60:63], v[124:127], v[52:55], 0\n"
    "s_nop 7\n"
    "v_xor_b32 v109, s83, v109\n"
    "buffer_load_dwordx4 v[52:55], v109, s[4:7], s84 offen offset:64\n"
    "v_xor_b32 v109, s83, v109\n"
    "buffer_load_dwordx4 v[124:127], v109, s[8:11], s73 offen offset:64\n"
    "s_waitcnt vmcnt(0)\n"
    "v_mfma_f32_16x16x32_bf16 v[56:59], v[124:127], v[52:55], v[56:59]\n"
    "s_nop 7\n"
    "buffer_load_dwordx4 v[124:127], v109, s[8:11], s72 offen offset:64\n"
    "s_waitcnt vmcnt(0)\n"
    "v_mfma_f32_16x16x32_bf16 v[60:63], v[124:127], v[52:55], v[60:63]\n"
    "s_nop 7\n"
    "v_xor_b32 v109, s83, v109\n"
    "buffer_load_dwordx4 v[52:55], v109, s[4:7], s84 offen offset:128\n"
    "v_xor_b32 v109, s83, v109\n"
    "buffer_load_dwordx4 v[124:127], v109, s[8:11], s73 offen offset:128\n"
    "s_waitcnt vmcnt(0)\n"
    "v_mfma_f32_16x16x32_bf16 v[56:59], v[124:127], v[52:55], v[56:59]\n"
    "s_nop 7\n"
    "buffer_load_dwordx4 v[124:127], v109, s[8:11], s72 offen offset:128\n"
    "s_waitcnt vmcnt(0)\n"
    "v_mfma_f32_16x16x32_bf16 v[60:63], v[124:127], v[52:55], v[60:63]\n"
    "s_nop 7\n"
    "v_xor_b32 v109, s83, v109\n"
    "buffer_load_dwordx4 v[52:55], v109, s[4:7], s84 offen offset:192\n"
    "v_xor_b32 v109, s83, v109\n"
    "buffer_load_dwordx4 v[124:127], v109, s[8:11], s73 offen offset:192\n"
    "s_waitcnt vmcnt(0)\n"
    "v_mfma_f32_16x16x32_bf16 v[56:59], v[124:127], v[52:55], v[56:59]\n"
    "s_nop 7\n"
    "buffer_load_dwordx4 v[124:127], v109, s[8:11], s72 offen offset:192\n"
    "s_waitcnt vmcnt(0)\n"
    "v_mfma_f32_16x16x32_bf16 v[60:63], v[124:127], v[52:55], v[60:63]\n"
    "s_nop 7\n"
    "v_xor_b32 v109, s83, v109\n"
    "buffer_load_dwordx4 v[52:55], v109, s[4:7], s84 offen offset:256\n"
    "v_xor_b32 v109, s83, v109\n"
    "buffer_load_dwordx4 v[124:127], v109, s[8:11], s73 offen offset:256\n"
    "s_waitcnt vmcnt(0)\n"
    "v_mfma_f32_16x16x32_bf16 v[56:59], v[124:127], v[52:55], v[56:59]\n"
    "s_nop 7\n"
    "buffer_load_dwordx4 v[124:127], v109, s[8:11], s72 offen offset:256\n"
    "s_waitcnt vmcnt(0)\n"
    "v_mfma_f32_16x16x32_bf16 v[60:63], v[124:127], v[52:55], v[60:63]\n"
    "s_nop 7\n"
    "v_xor_b32 v109, s83, v109\n"
    "buffer_load_dwordx4 v[52:55], v109, s[4:7], s84 offen offset:320\n"
    "v_xor_b32 v109, s83, v109\n"
    "buffer_load_dwordx4 v[124:127], v109, s[8:11], s73 offen offset:320\n"
    "s_waitcnt vmcnt(0)\n"
    "v_mfma_f32_16x16x32_bf16 v[56:59], v[124:127], v[52:55], v[56:59]\n"
    "s_nop 7\n"
    "buffer_load_dwordx4 v[124:127], v109, s[8:11], s72 offen offset:320\n"
    "s_waitcnt vmcnt(0)\n"
    "v_mfma_f32_16x16x32_bf16 v[60:63], v[124:127], v[52:55], v[60:63]\n"
    "s_nop 7\n"
    "v_xor_b32 v109, s83, v109\n"
    "buffer_load_dwordx4 v[52:55], v109, s[4:7], s84 offen offset:384\n"
    "v_xor_b32 v109, s83, v109\n"
    "buffer_load_dwordx4 v[124:127], v109, s[8:11], s73 offen offset:384\n"
    "s_waitcnt vmcnt(0)\n"
    "v_mfma_f32_16x16x32_bf16 v[56:59], v[124:127], v[52:55], v[56:59]\n"
    "s_nop 7\n"
    "buffer_load_dwordx4 v[124:127], v109, s[8:11], s72 offen offset:384\n"
    "s_waitcnt vmcnt(0)\n"
    "v_mfma_f32_16x16x32_bf16 v[60:63], v[124:127], v[52:55], v[60:63]\n"
    "s_nop 7\n"
    "v_xor_b32 v109, s83, v109\n"
    "buffer_load_dwordx4 v[52:55], v109, s[4:7], s84 offen offset:448\n"
    "v_xor_b32 v109, s83, v109\n"
    "buffer_load_dwordx4 v[124:127], v109, s[8:11], s73 offen offset:448\n"
    "s_waitcnt vmcnt(0)\n"
    "v_mfma_f32_16x16x32_bf16 v[56:59], v[124:127], v[52:55], v[56:59]\n"
    "s_nop 7\n"
    "buffer_load_dwordx4 v[124:127], v109, s[8:11], s72 offen offset:448\n"
    "s_waitcnt vmcnt(0)\n"
    "v_mfma_f32_16x16x32_bf16 v[60:63], v[124:127], v[52:55], v[60:63]\n"
    "s_nop 7\n"
    "s_add_u32 s79, s79, 8192\n"
    "s_nop 15\n"
    "v_max3_f32 v109, v56, v57, v58\n"
    "v_max3_f32 v109, v109, v59, v60\n"
    "v_max3_f32 v109, v109, v61, v62\n"
    "v_max_f32 v109, v109, v63\n"
    "v_mov_b32 v126, v109\n"
    "s_nop 1\n"
    "v_permlane32_swap_b32 v109, v126\n"
    "v_max_f32 v109, v109, v126\n"
    "v_mov_b32 v126, v109\n"
    "s_nop 1\n"
    "v_permlane16_swap_b32 v109, v126\n"
    "v_max_f32 v109, v109, v126\n"
    "v_fma_f32 v109, v109, s39, -v104\n"
    "v_add_f32 v109, s40, v109\n"
    "v_max_f32 v109, 0, v109\n"
    "v_ceil_f32 v109, v109\n"
    "v_add_f32 v104, v104, v109\n"
    "v_min_f32 v126, 0x43480000, v109\n"
    "v_cvt_i32_f32 v126, v126\n"
    "v_sub_u32 v126, 0, v126\n"
    "v_ldexp_f32 v127, 1.0, v126\n"
    "v_mul_f32 v108, v108, v127\n"
    "v_fma_f32 v56, v56, s39, -v104\n"
    "v_fma_f32 v57, v57, s39, -v104\n"
    "v_fma_f32 v58, v58, s39, -v104\n"
    "v_fma_f32 v59, v59, s39, -v104\n"
    "v_fma_f32 v60, v60, s39, -v104\n"
    "v_fma_f32 v61, v61, s39, -v104\n"
    "v_fma_f32 v62, v62, s39, -v104\n"
    "v_fma_f32 v63, v63, s39, -v104\n"
    "v_exp_f32 v56, v56\n"
    "v_exp_f32 v57, v57\n"
    "v_exp_f32 v58, v58\n"
    "v_exp_f32 v59, v59\n"
    "v_exp_f32 v60, v60\n"
    "v_exp_f32 v61, v61\n"
    "v_exp_f32 v62, v62\n"
    "v_exp_f32 v63, v63\n"
    "v_add_f32 v126, v56, v57\n"
    "v_add_f32 v126, v126, v58\n"
    "v_add_f32 v126, v126, v59\n"
    "v_add_f32 v126, v126, v60\n"
    "v_add_f32 v126, v126, v61\n"
    "v_add_f32 v126, v126, v62\n"
    "v_add_f32 v126, v126, v63\n"
    "v_add_f32 v108, v108, v126\n"
    "v_cvt_pk_bf16_f32 v56, v56, v57\n"
    "v_cvt_pk_bf16_f32 v57, v58, v59\n"
    "v_cvt_pk_bf16_f32 v58, v60, v61\n"
    "v_cvt_pk_bf16_f32 v59, v62, v63\n"
    "v_mul_f32 v140, v140, v127\n"
    "v_mul_f32 v141, v141, v127\n"
    "v_mul_f32 v142, v142, v127\n"
    "v_mul_f32 v143, v143, v127\n"
    "v_mul_f32 v156, v156, v127\n"
    "v_mul_f32 v157, v157, v127\n"
    "v_mul_f32 v158, v158, v127\n"
    "v_mul_f32 v159, v159, v127\n"
    "v_mul_f32 v172, v172, v127\n"
    "v_mul_f32 v173, v173, v127\n"
    "v_mul_f32 v174, v174, v127\n"
    "v_mul_f32 v175, v175, v127\n"
    "v_mul_f32 v188, v188, v127\n"
    "v_mul_f32 v189, v189, v127\n"
    "v_mul_f32 v190, v190, v127\n"
    "v_mul_f32 v191, v191, v127\n"
    "v_mul_f32 v204, v204, v127\n"
    "v_mul_f32 v205, v205, v127\n"
    "v_mul_f32 v206, v206, v127\n"
    "v_mul_f32 v207, v207, v127\n"
    "v_mul_f32 v220, v220, v127\n"
    "v_mul_f32 v221, v221, v127\n"
    "v_mul_f32 v222, v222, v127\n"
    "v_mul_f32 v223, v223, v127\n"
    "v_mul_f32 v236, v236, v127\n"
    "v_mul_f32 v237, v237, v127\n"
    "v_mul_f32 v238, v238, v127\n"
    "v_mul_f32 v239, v239, v127\n"
    "v_mul_f32 v252, v252, v127\n"
    "v_mul_f32 v253, v253, v127\n"
    "v_mul_f32 v254, v254, v127\n"
    "v_mul_f32 v255, v255, v127\n"
    "v_accvgpr_read_b32 v124, a140\n"
    "v_mul_f32 v124, v124, v127\n"
    "v_accvgpr_write_b32 a140, v124\n"
    "v_accvgpr_read_b32 v125, a141\n"
    "v_mul_f32 v125, v125, v127\n"
    "v_accvgpr_write_b32 a141, v125\n"
    "v_accvgpr_read_b32 v124, a142\n"
    "v_mul_f32 v124, v124, v127\n"
    "v_accvgpr_write_b32 a142, v124\n"
    "v_accvgpr_read_b32 v125, a143\n"
    "v_mul_f32 v125, v125, v127\n"
    "v_accvgpr_write_b32 a143, v125\n"
    "v_accvgpr_read_b32 v124, a156\n"
    "v_mul_f32 v124, v124, v127\n"
    "v_accvgpr_write_b32 a156, v124\n"
    "v_accvgpr_read_b32 v125, a157\n"
    "v_mul_f32 v125, v125, v127\n"
    "v_accvgpr_write_b32 a157, v125\n"
    "v_accvgpr_read_b32 v124, a158\n"
    "v_mul_f32 v124, v124, v127\n"
    "v_accvgpr_write_b32 a158, v124\n"
    "v_accvgpr_read_b32 v125, a159\n"
    "v_mul_f32 v125, v125, v127\n"
    "v_accvgpr_write_b32 a159, v125\n"
    "v_accvgpr_read_b32 v124, a172\n"
    "v_mul_f32 v124, v124, v127\n"
    "v_accvgpr_write_b32 a172, v124\n"
    "v_accvgpr_read_b32 v125, a173\n"
    "v_mul_f32 v125, v125, v127\n"
    "v_accvgpr_write_b32 a173, v125\n"
    "v_accvgpr_read_b32 v124, a174\n"
    "v_mul_f32 v124, v124, v127\n"
    "v_accvgpr_write_b32 a174, v124\n"
    "v_accvgpr_read_b32 v125, a175\n"
    "v_mul_f32 v125, v125, v127\n"
    "v_accvgpr_write_b32 a175, v125\n"
    "v_accvgpr_read_b32 v124, a188\n"
    "v_mul_f32 v124, v124, v127\n"
    "v_accvgpr_write_b32 a188, v124\n"
    "v_accvgpr_read_b32 v125, a189\n"
    "v_mul_f32 v125, v125, v127\n"
    "v_accvgpr_write_b32 a189, v125\n"
    "v_accvgpr_read_b32 v124, a190\n"
    "v_mul_f32 v124, v124, v127\n"
    "v_accvgpr_write_b32 a190, v124\n"
    "v_accvgpr_read_b32 v125, a191\n"
    "v_mul_f32 v125, v125, v127\n"
    "v_accvgpr_write_b32 a191, v125\n"
    "v_accvgpr_read_b32 v124, a204\n"
    "v_mul_f32 v124, v124, v127\n"
    "v_accvgpr_write_b32 a204, v124\n"
    "v_accvgpr_read_b32 v125, a205\n"
    "v_mul_f32 v125, v125, v127\n"
    "v_accvgpr_write_b32 a205, v125\n"
    "v_accvgpr_read_b32 v124, a206\n"
    "v_mul_f32 v124, v124, v127\n"
    "v_accvgpr_write_b32 a206, v124\n"
    "v_accvgpr_read_b32 v125, a207\n"
    "v_mul_f32 v125, v125, v127\n"
    "v_accvgpr_write_b32 a207, v125\n"
    "v_mul_f32 v76, v76, v127\n"
    "v_mul_f32 v77, v77, v127\n"
    "v_mul_f32 v78, v78, v127\n"
    "v_mul_f32 v79, v79, v127\n"
    "v_accvgpr_read_b32 v124, a236\n"
    "v_mul_f32 v124, v124, v127\n"
    "v_accvgpr_write_b32 a236, v124\n"
    "v_accvgpr_read_b32 v125, a237\n"
    "v_mul_f32 v125, v125, v127\n"
    "v_accvgpr_write_b32 a237, v125\n"
    "v_accvgpr_read_b32 v124, a238\n"
    "v_mul_f32 v124, v124, v127\n"
    "v_accvgpr_write_b32 a238, v124\n"
    "v_accvgpr_read_b32 v125, a239\n"
    "v_mul_f32 v125, v125, v127\n"
    "v_accvgpr_write_b32 a239, v125\n"
    "v_mul_f32 v92, v92, v127\n"
    "v_mul_f32 v93, v93, v127\n"
    "v_mul_f32 v94, v94, v127\n"
    "v_mul_f32 v95, v95, v127\n"
    "v_mov_b32 v52, 0\n"
    "v_mov_b32 v60, 0\n"
    "L_rare1R:\n"
    "s_add_u32 s74, s74, 4\n"
    "s_addc_u32 s75, s75, 0\n"
    "s_nop 3\n"
    "s_setpc_b64 s[74:75]\n"
    "L_rare2B:\n"
    "s_nop 15\n"
    "v_mbcnt_lo_u32_b32 v126, -1, 0\n"
    "v_mbcnt_hi_u32_b32 v126, -1, v126\n"
    "v_and_b32 v109, 15, v126\n"
    "v_lshlrev_b32 v109, 9, v109\n"
    "v_lshrrev_b32 v126, 4, v126\n"
    "v_lshl_add_u32 v109, v126, 4, v109\n"
    "s_add_u32 s72, s73, 8192\n"
    "s_and_b32 s83, s87, 0x1000\n"
    "s_and_b32 s84, s87, 0x6000\n"
    "s_xor_b32 s84, s84, s79\n"
    "v_xor_b32 v109, s83, v109\n"
    "buffer_load_dwordx4 v[40:43], v109, s[4:7], s84 offen offset:0\n"
    "v_xor_b32 v109, s83, v109\n"
    "buffer_load_dwordx4 v[124:127], v109, s[8:11], s73 offen offset:0\n"
    "s_waitcnt vmcnt(0)\n"
    "v_mfma_f32_16x16x32_bf16 v[32:35], v[124:127], v[40:43], 0\n"
    "s_nop 7\n"
    "buffer_load_dwordx4 v[124:127], v109, s[8:11], s72 offen offset:0\n"
    "s_waitcnt vmcnt(0)\n"
    "v_mfma_f32_16x16x32_bf16 v[36:39], v[124:127], v[40:43], 0\n"
    "s_nop 7\n"
    "v_xor_b32 v109, s83, v109\n"
    "buffer_load_dwordx4 v[40:43], v109, s[4:7], s84 offen offset:64\n"
    "v_xor_b32 v109, s83, v109\n"
    "buffer_load_dwordx4 v[124:127], v109, s[8:11], s73 offen offset:64\n"
    "s_waitcnt vmcnt(0)\n"
    "v_mfma_f32_16x16x32_bf16 v[32:35], v[124:127], v[40:43], v[32:35]\n"
    "s_nop 7\n"
    "buffer_load_dwordx4 v[124:127], v109, s[8:11], s72 offen offset:64\n"
    "s_waitcnt vmcnt(0)\n"
    "v_mfma_f32_16x16x32_bf16 v[36:39], v[124:127], v[40:43], v[36:39]\n"
    "s_nop 7\n"
    "v_xor_b32 v109, s83, v109\n"
    "buffer_load_dwordx4 v[40:43], v109, s[4:7], s84 offen offset:128\n"
    "v_xor_b32 v109, s83, v109\n"
    "buffer_load_dwordx4 v[124:127], v109, s[8:11], s73 offen offset:128\n"
    "s_waitcnt vmcnt(0)\n"
    "v_mfma_f32_16x16x32_bf16 v[32:35], v[124:127], v[40:43], v[32:35]\n"
    "s_nop 7\n"
    "buffer_load_dwordx4 v[124:127], v109, s[8:11], s72 offen offset:128\n"
    "s_waitcnt vmcnt(0)\n"
    "v_mfma_f32_16x16x32_bf16 v[36:39], v[124:127], v[40:43], v[36:39]\n"
    "s_nop 7\n"
    "v_xor_b32 v109, s83, v109\n"
    "buffer_load_dwordx4 v[40:43], v109, s[4:7], s84 offen offset:192\n"
    "v_xor_b32 v109, s83, v109\n"
    "buffer_load_dwordx4 v[124:127], v109, s[8:11], s73 offen offset:192\n"
    "s_waitcnt vmcnt(0)\n"
    "v_mfma_f32_16x16x32_bf16 v[32:35], v[124:127], v[40:43], v[32:35]\n"
    "s_nop 7\n"
    "buffer_load_dwordx4 v[124:127], v109, s[8:11], s72 offen offset:192\n"
    "s_waitcnt vmcnt(0)\n"
    "v_mfma_f32_16x16x32_bf16 v[36:39], v[124:127], v[40:43], v[36:39]\n"
    "s_nop 7\n"
    "v_xor_b32 v109, s83, v109\n"
    "buffer_load_dwordx4 v[40:43], v109, s[4:7], s84 offen offset:256\n"
    "v_xor_b32 v109, s83, v109\n"
    "buffer_load_dwordx4 v[124:127], v109, s[8:11], s73 offen offset:256\n"
    "s_waitcnt vmcnt(0)\n"
    "v_mfma_f32_16x16x32_bf16 v[32:35], v[124:127], v[40:43], v[32:35]\n"
    "s_nop 7\n"
    "buffer_load_dwordx4 v[124:127], v109, s[8:11], s72 offen offset:256\n"
    "s_waitcnt vmcnt(0)\n"
    "v_mfma_f32_16x16x32_bf16 v[36:39], v[124:127], v[40:43], v[36:39]\n"
    "s_nop 7\n"
    "v_xor_b32 v109, s83, v109\n"
    "buffer_load_dwordx4 v[40:43], v109, s[4:7], s84 offen offset:320\n"
    "v_xor_b32 v109, s83, v109\n"
    "buffer_load_dwordx4 v[124:127], v109, s[8:11], s73 offen offset:320\n"
    "s_waitcnt vmcnt(0)\n"
    "v_mfma_f32_16x16x32_bf16 v[32:35], v[124:127], v[40:43], v[32:35]\n"
    "s_nop 7\n"
    "buffer_load_dwordx4 v[124:127], v109, s[8:11], s72 offen offset:320\n"
    "s_waitcnt vmcnt(0)\n"
    "v_mfma_f32_16x16x32_bf16 v[36:39], v[124:127], v[40:43], v[36:39]\n"
    "s_nop 7\n"
    "v_xor_b32 v109, s83, v109\n"
    "buffer_load_dwordx4 v[40:43], v109, s[4:7], s84 offen offset:384\n"
    "v_xor_b32 v109, s83, v109\n"
    "buffer_load_dwordx4 v[124:127], v109, s[8:11], s73 offen offset:384\n"
    "s_waitcnt vmcnt(0)\n"
    "v_mfma_f32_16x16x32_bf16 v[32:35], v[124:127], v[40:43], v[32:35]\n"
    "s_nop 7\n"
    "buffer_load_dwordx4 v[124:127], v109, s[8:11], s72 offen offset:384\n"
    "s_waitcnt vmcnt(0)\n"
    "v_mfma_f32_16x16x32_bf16 v[36:39], v[124:127], v[40:43], v[36:39]\n"
    "s_nop 7\n"
    "v_xor_b32 v109, s83, v109\n"
    "buffer_load_dwordx4 v[40:43], v109, s[4:7], s84 offen offset:448\n"
    "v_xor_b32 v109, s83, v109\n"
    "buffer_load_dwordx4 v[124:127], v109, s[8:11], s73 offen offset:448\n"
    "s_waitcnt vmcnt(0)\n"
    "v_mfma_f32_16x16x32_bf16 v[32:35], v[124:127], v[40:43], v[32:35]\n"
    "s_nop 7\n"
    "buffer_load_dwordx4 v[124:127], v109, s[8:11], s72 offen offset:448\n"
    "s_waitcnt vmcnt(0)\n"
    "v_mfma_f32_16x16x32_bf16 v[36:39], v[124:127], v[40:43], v[36:39]\n"
    "s_nop 7\n"
    "s_add_u32 s79, s79, 8192\n"
    "s_nop 15\n"
    "v_max3_f32 v109, v32, v33, v34\n"
    "v_max3_f32 v109, v109, v35, v36\n"
    "v_max3_f32 v109, v109, v37, v38\n"
    "v_max_f32 v109, v109, v39\n"
    "v_mov_b32 v126, v109\n"
    "s_nop 1\n"
    "v_permlane32_swap_b32 v109, v126\n"
    "v_max_f32 v109, v109, v126\n"
    "v_mov_b32 v126, v109\n"
    "s_nop 1\n"
    "v_permlane16_swap_b32 v109, v126\n"
    "v_max_f32 v109, v109, v126\n"
    "v_fma_f32 v109, v109, s39, -v103\n"
    "v_add_f32 v109, s40, v109\n"
    "v_max_f32 v109, 0, v109\n"
    "v_ceil_f32 v109, v109\n"
    "v_add_f32 v103, v103, v109\n"
    "v_min_f32 v126, 0x43480000, v109\n"
    "v_cvt_i32_f32 v126, v126\n"
    "v_sub_u32 v126, 0, v126\n"
    "v_ldexp_f32 v127, 1.0, v126\n"
    "v_mul_f32 v107, v107, v127\n"
    "v_lshlrev_b32 v124, 16, v16\n"
    "v_and_b32 v125, 0xffff0000, v16\n"
    "v_mul_f32 v124, v124, v127\n"
    "v_mul_f32 v125, v125, v127\n"
    "v_cvt_pk_bf16_f32 v16, v124, v125\n"
    "v_lshlrev_b32 v124, 16, v17\n"
    "v_and_b32 v125, 0xffff0000, v17\n"
    "v_mul_f32 v124, v124, v127\n"
    "v_mul_f32 v125, v125, v127\n"
    "v_cvt_pk_bf16_f32 v17, v124, v125\n"
    "v_lshlrev_b32 v124, 16, v18\n"
    "v_and_b32 v125, 0xffff0000, v18\n"
    "v_mul_f32 v124, v124, v127\n"
    "v_mul_f32 v125, v125, v127\n"
    "v_cvt_pk_bf16_f32 v18, v124, v125\n"
    "v_lshlrev_b32 v124, 16, v19\n"
    "v_and_b32 v125, 0xffff0000, v19\n"
    "v_mul_f32 v124, v124, v127\n"
    "v_mul_f32 v125, v125, v127\n"
    "v_cvt_pk_bf16_f32 v19, v124, v125\n"
    "v_fma_f32 v32, v32, s39, -v103\n"
    "v_fma_f32 v33, v33, s39, -v103\n"
    "v_fma_f32 v34, v34, s39, -v103\n"
    "v_fma_f32 v35, v35, s39, -v103\n"
    "v_fma_f32 v36, v36, s39, -v103\n"
    "v_fma_f32 v37, v37, s39, -v103\n"
    "v_fma_f32 v38, v38, s39, -v103\n"
    "v_fma_f32 v39, v39, s39, -v103\n"
    "v_exp_f32 v32, v32\n"
    "v_exp_f32 v33, v33\n"
    "v_exp_f32 v34, v34\n"
    "v_exp_f32 v35, v35\n"
    "v_exp_f32 v36, v36\n"
    "v_exp_f32 v37, v37\n"
    "v_exp_f32 v38, v38\n"
    "v_exp_f32 v39, v39\n"
    "v_add_f32 v126, v32, v33\n"
    "v_add_f32 v126, v126, v34\n"
    "v_add_f32 v126, v126, v35\n"
    "v_add_f32 v126, v126, v36\n"
    "v_add_f32 v126, v126, v37\n"
    "v_add_f32 v126, v126, v38\n"
    "v_add_f32 v126, v126, v39\n"
    "v_add_f32 v107, v107, v126\n"
    "v_cvt_pk_bf16_f32 v32, v32, v33\n"
    "v_cvt_pk_bf16_f32 v33, v34, v35\n"
    "v_cvt_pk_bf16_f32 v34, v36, v37\n"
    "v_cvt_pk_bf16_f32 v35, v38, v39\n"
    "v_mul_f32 v136, v136, v127\n"
    "v_mul_f32 v137, v137, v127\n"
    "v_mul_f32 v138, v138, v127\n"
    "v_mul_f32 v139, v139, v127\n"
    "v_mul_f32 v152, v152, v127\n"
    "v_mul_f32 v153, v153, v127\n"
    "v_mul_f32 v154, v154, v127\n"
    "v_mul_f32 v155, v155, v127\n"
    "v_mul_f32 v168, v168, v127\n"
    "v_mul_f32 v169, v169, v127\n"
    "v_mul_f32 v170, v170, v127\n"
    "v_mul_f32 v171, v171, v127\n"
    "v_mul_f32 v184, v184, v127\n"
    "v_mul_f32 v185, v185, v127\n"
    "v_mul_f32 v186, v186, v127\n"
    "v_mul_f32 v187, v187, v127\n"
    "v_mul_f32 v200, v200, v127\n"
    "v_mul_f32 v201, v201, v127\n"
    "v_mul_f32 v202, v202, v127\n"
    "v_mul_f32 v203, v203, v127\n"
    "v_mul_f32 v216, v216, v127\n"
    "v_mul_f32 v217, v217, v127\n"
    "v_mul_f32 v218, v218, v127\n"
    "v_mul_f32 v219, v219, v127\n"
    "v_mul_f32 v232, v232, v127\n"
    "v_mul_f32 v233, v233, v127\n"
    "v_mul_f32 v234, v234, v127\n"
    "v_mul_f32 v235, v235, v127\n"
    "v_mul_f32 v248, v248, v127\n"
    "v_mul_f32 v249, v249, v127\n"
    "v_mul_f32 v250, v250, v127\n"
    "v_mul_f32 v251, v251, v127\n"
    "v_accvgpr_read_b32 v124, a136\n"
    "v_mul_f32 v124, v124, v127\n"
    "v_accvgpr_write_b32 a136, v124\n"
    "v_accvgpr_read_b32 v125, a137\n"
    "v_mul_f32 v125, v125, v127\n"
    "v_accvgpr_write_b32 a137, v125\n"
    "v_accvgpr_read_b32 v124, a138\n"
    "v_mul_f32 v124, v124, v127\n"
    "v_accvgpr_write_b32 a138, v124\n"
    "v_accvgpr_read_b32 v125, a139\n"
    "v_mul_f32 v125, v125, v127\n"
    "v_accvgpr_write_b32 a139, v125\n"
    "v_accvgpr_read_b32 v124, a152\n"
    "v_mul_f32 v124, v124, v127\n"
    "v_accvgpr_write_b32 a152, v124\n"
    "v_accvgpr_read_b32 v125, a153\n"
    "v_mul_f32 v125, v125, v127\n"
    "v_accvgpr_write_b32 a153, v125\n"
    "v_accvgpr_read_b32 v124, a154\n"
    "v_mul_f32 v124, v124, v127\n"
    "v_accvgpr_write_b32 a154, v124\n"
    "v_accvgpr_read_b32 v125, a155\n"
    "v_mul_f32 v125, v125, v127\n"
    "v_accvgpr_write_b32 a155, v125\n"
    "v_accvgpr_read_b32 v124, a168\n"
    "v_mul_f32 v124, v124, v127\n"
    "v_accvgpr_write_b32 a168, v124\n"
    "v_accvgpr_read_b32 v125, a169\n"
    "v_mul_f32 v125, v125, v127\n"
    "v_accvgpr_write_b32 a169, v125\n"
    "v_accvgpr_read_b32 v124, a170\n"
    "v_mul_f32 v124, v124, v127\n"
    "v_accvgpr_write_b32 a170, v124\n"
    "v_accvgpr_read_b32 v125, a171\n"
    "v_mul_f32 v125, v125, v127\n"
    "v_accvgpr_write_b32 a171, v125\n"
    "v_accvgpr_read_b32 v124, a184\n"
    "v_mul_f32 v124, v124, v127\n"
    "v_accvgpr_write_b32 a184, v124\n"
    "v_accvgpr_read_b32 v125, a185\n"
    "v_mul_f32 v125, v125, v127\n"
    "v_accvgpr_write_b32 a185, v125\n"
    "v_accvgpr_read_b32 v124, a186\n"
    "v_mul_f32 v124, v124, v127\n"
    "v_accvgpr_write_b32 a186, v124\n"
    "v_accvgpr_read_b32 v125, a187\n"
    "v_mul_f32 v125, v125, v127\n"
    "v_accvgpr_write_b32 a187, v125\n"
    "v_accvgpr_read_b32 v124, a200\n"
    "v_mul_f32 v124, v124, v127\n"
    "v_accvgpr_write_b32 a200, v124\n"
    "v_accvgpr_read_b32 v125, a201\n"
    "v_mul_f32 v125, v125, v127\n"
    "v_accvgpr_write_b32 a201, v125\n"
    "v_accvgpr_read_b32 v124, a202\n"
    "v_mul_f32 v124, v124, v127\n"
    "v_accvgpr_write_b32 a202, v124\n"
    "v_accvgpr_read_b32 v125, a203\n"
    "v_mul_f32 v125, v125, v127\n"
    "v_accvgpr_write_b32 a203, v125\n"
    "v_mul_f32 v72, v72, v127\n"
    "v_mul_f32 v73, v73, v127\n"
    "v_mul_f32 v74, v74, v127\n"
    "v_mul_f32 v75, v75, v127\n"
    "v_accvgpr_read_b32 v124, a232\n"
    "v_mul_f32 v124, v124, v127\n"
    "v_accvgpr_write_b32 a232, v124\n"
    "v_accvgpr_read_b32 v125, a233\n"
    "v_mul_f32 v125, v125, v127\n"
    "v_accvgpr_write_b32 a233, v125\n"
    "v_accvgpr_read_b32 v124, a234\n"
    "v_mul_f32 v124, v124, v127\n"
    "v_accvgpr_write_b32 a234, v124\n"
    "v_accvgpr_read_b32 v125, a235\n"
    "v_mul_f32 v125, v125, v127\n"
    "v_accvgpr_write_b32 a235, v125\n"
    "v_mul_f32 v88, v88, v127\n"
    "v_mul_f32 v89, v89, v127\n"
    "v_mul_f32 v90, v90, v127\n"
    "v_mul_f32 v91, v91, v127\n"
    "v_mbcnt_lo_u32_b32 v126, -1, 0\n"
    "v_mbcnt_hi_u32_b32 v126, -1, v126\n"
    "v_and_b32 v109, 15, v126\n"
    "v_lshlrev_b32 v109, 9, v109\n"
    "v_lshrrev_b32 v126, 4, v126\n"
    "v_lshl_add_u32 v109, v126, 4, v109\n"
    "s_add_u32 s72, s73, 8192\n"
    "s_and_b32 s83, s87, 0x1000\n"
    "s_and_b32 s84, s87, 0x6000\n"
    "s_xor_b32 s84, s84, s79\n"
    "v_xor_b32 v109, s83, v109\n"
    "buffer_load_dwordx4 v[36:39], v109, s[4:7], s84 offen offset:0\n"
    "v_xor_b32 v109, s83, v109\n"
    "buffer_load_dwordx4 v[124:127], v109, s[8:11], s73 offen offset:0\n"
    "s_waitcnt vmcnt(0)\n"
    "v_mfma_f32_16x16x32_bf16 v[40:43], v[124:127], v[36:39], 0\n"
    "s_nop 7\n"
    "buffer_load_dwordx4 v[124:127], v109, s[8:11], s72 offen offset:0\n"
    "s_waitcnt vmcnt(0)\n"
    "v_mfma_f32_16x16x32_bf16 v[44:47], v[124:127], v[36:39], 0\n"
    "s_nop 7\n"
    "v_xor_b32 v109, s83, v109\n"
    "buffer_load_dwordx4 v[36:39], v109, s[4:7], s84 offen offset:64\n"
    "v_xor_b32 v109, s83, v109\n"
    "buffer_load_dwordx4 v[124:127], v109, s[8:11], s73 offen offset:64\n"
    "s_waitcnt vmcnt(0)\n"
    "v_mfma_f32_16x16x32_bf16 v[40:43], v[124:127], v[36:39], v[40:43]\n"
    "s_nop 7\n"
    "buffer_load_dwordx4 v[124:127], v109, s[8:11], s72 offen offset:64\n"
    "s_waitcnt vmcnt(0)\n"
    "v_mfma_f32_16x16x32_bf16 v[44:47], v[124:127], v[36:39], v[44:47]\n"
    "s_nop 7\n"
    "v_xor_b32 v109, s83, v109\n"
    "buffer_load_dwordx4 v[36:39], v109, s[4:7], s84 offen offset:128\n"
    "v_xor_b32 v109, s83, v109\n"
    "buffer_load_dwordx4 v[124:127], v109, s[8:11], s73 offen offset:128\n"
    "s_waitcnt vmcnt(0)\n"
    "v_mfma_f32_16x16x32_bf16 v[40:43], v[124:127], v[36:39], v[40:43]\n"
    "s_nop 7\n"
    "buffer_load_dwordx4 v[124:127], v109, s[8:11], s72 offen offset:128\n"
    "s_waitcnt vmcnt(0)\n"
    "v_mfma_f32_16x16x32_bf16 v[44:47], v[124:127], v[36:39], v[44:47]\n"
    "s_nop 7\n"
    "v_xor_b32 v109, s83, v109\n"
    "buffer_load_dwordx4 v[36:39], v109, s[4:7], s84 offen offset:192\n"
    "v_xor_b32 v109, s83, v109\n"
    "buffer_load_dwordx4 v[124:127], v109, s[8:11], s73 offen offset:192\n"
    "s_waitcnt vmcnt(0)\n"
    "v_mfma_f32_16x16x32_bf16 v[40:43], v[124:127], v[36:39], v[40:43]\n"
    "s_nop 7\n"
    "buffer_load_dwordx4 v[124:127], v109, s[8:11], s72 offen offset:192\n"
    "s_waitcnt vmcnt(0)\n"
    "v_mfma_f32_16x16x32_bf16 v[44:47], v[124:127], v[36:39], v[44:47]\n"
    "s_nop 7\n"
    "v_xor_b32 v109, s83, v109\n"
    "buffer_load_dwordx4 v[36:39], v109, s[4:7], s84 offen offset:256\n"
    "v_xor_b32 v109, s83, v109\n"
    "buffer_load_dwordx4 v[124:127], v109, s[8:11], s73 offen offset:256\n"
    "s_waitcnt vmcnt(0)\n"
    "v_mfma_f32_16x16x32_bf16 v[40:43], v[124:127], v[36:39], v[40:43]\n"
    "s_nop 7\n"
    "buffer_load_dwordx4 v[124:127], v109, s[8:11], s72 offen offset:256\n"
    "s_waitcnt vmcnt(0)\n"
    "v_mfma_f32_16x16x32_bf16 v[44:47], v[124:127], v[36:39], v[44:47]\n"
    "s_nop 7\n"
    "v_xor_b32 v109, s83, v109\n"
    "buffer_load_dwordx4 v[36:39], v109, s[4:7], s84 offen offset:320\n"
    "v_xor_b32 v109, s83, v109\n"
    "buffer_load_dwordx4 v[124:127], v109, s[8:11], s73 offen offset:320\n"
    "s_waitcnt vmcnt(0)\n"
    "v_mfma_f32_16x16x32_bf16 v[40:43], v[124:127], v[36:39], v[40:43]\n"
    "s_nop 7\n"
    "buffer_load_dwordx4 v[124:127], v109, s[8:11], s72 offen offset:320\n"
    "s_waitcnt vmcnt(0)\n"
    "v_mfma_f32_16x16x32_bf16 v[44:47], v[124:127], v[36:39], v[44:47]\n"
    "s_nop 7\n"
    "v_xor_b32 v109, s83, v109\n"
    "buffer_load_dwordx4 v[36:39], v109, s[4:7], s84 offen offset:384\n"
    "v_xor_b32 v109, s83, v109\n"
    "buffer_load_dwordx4 v[124:127], v109, s[8:11], s73 offen offset:384\n"
    "s_waitcnt vmcnt(0)\n"
    "v_mfma_f32_16x16x32_bf16 v[40:43], v[124:127], v[36:39], v[40:43]\n"
    "s_nop 7\n"
    "buffer_load_dwordx4 v[124:127], v109, s[8:11], s72 offen offset:384\n"
    "s_waitcnt vmcnt(0)\n"
    "v_mfma_f32_16x16x32_bf16 v[44:47], v[124:127], v[36:39], v[44:47]\n"
    "s_nop 7\n"
    "v_xor_b32 v109, s83, v109\n"
    "buffer_load_dwordx4 v[36:39], v109, s[4:7], s84 offen offset:448\n"
    "v_xor_b32 v109, s83, v109\n"
    "buffer_load_dwordx4 v[124:127], v109, s[8:11], s73 offen offset:448\n"
    "s_waitcnt vmcnt(0)\n"
    "v_mfma_f32_16x16x32_bf16 v[40:43], v[124:127], v[36:39], v[40:43]\n"
    "s_nop 7\n"
    "buffer_load_dwordx4 v[124:127], v109, s[8:11], s72 offen offset:448\n"
    "s_waitcnt vmcnt(0)\n"
    "v_mfma_f32_16x16x32_bf16 v[44:47], v[124:127], v[36:39], v[44:47]\n"
    "s_nop 7\n"
    "s_add_u32 s79, s79, 8192\n"
    "s_nop 15\n"
    "v_max3_f32 v109, v40, v41, v42\n"
    "v_max3_f32 v109, v109, v43, v44\n"
    "v_max3_f32 v109, v109, v45, v46\n"
    "v_max_f32 v109, v109, v47\n"
    "v_mov_b32 v126, v109\n"
    "s_nop 1\n"
    "v_permlane32_swap_b32 v109, v126\n"
    "v_max_f32 v109, v109, v126\n"
    "v_mov_b32 v126, v109\n"
    "s_nop 1\n"
    "v_permlane16_swap_b32 v109, v126\n"
    "v_max_f32 v109, v109, v126\n"
    "v_fma_f32 v109, v109, s39, -v104\n"
    "v_add_f32 v109, s40, v109\n"
    "v_max_f32 v109, 0, v109\n"
    "v_ceil_f32 v109, v109\n"
    "v_add_f32 v104, v104, v109\n"
    "v_min_f32 v126, 0x43480000, v109\n"
    "v_cvt_i32_f32 v126, v126\n"
    "v_sub_u32 v126, 0, v126\n"
    "v_ldexp_f32 v127, 1.0, v126\n"
    "v_mul_f32 v108, v108, v127\n"
    "v_lshlrev_b32 v124, 16, v24\n"
    "v_and_b32 v125, 0xffff0000, v24\n"
    "v_mul_f32 v124, v124, v127\n"
    "v_mul_f32 v125, v125, v127\n"
    "v_cvt_pk_bf16_f32 v24, v124, v125\n"
    "v_lshlrev_b32 v124, 16, v25\n"
    "v_and_b32 v125, 0xffff0000, v25\n"
    "v_mul_f32 v124, v124, v127\n"
    "v_mul_f32 v125, v125, v127\n"
    "v_cvt_pk_bf16_f32 v25, v124, v125\n"
    "v_lshlrev_b32 v124, 16, v26\n"
    "v_and_b32 v125, 0xffff0000, v26\n"
    "v_mul_f32 v124, v124, v127\n"
    "v_mul_f32 v125, v125, v127\n"
    "v_cvt_pk_bf16_f32 v26, v124, v125\n"
    "v_lshlrev_b32 v124, 16, v27\n"
    "v_and_b32 v125, 0xffff0000, v27\n"
    "v_mul_f32 v124, v124, v127\n"
    "v_mul_f32 v125, v125, v127\n"
    "v_cvt_pk_bf16_f32 v27, v124, v125\n"
    "v_fma_f32 v40, v40, s39, -v104\n"
    "v_fma_f32 v41, v41, s39, -v104\n"
    "v_fma_f32 v42, v42, s39, -v104\n"
    "v_fma_f32 v43, v43, s39, -v104\n"
    "v_fma_f32 v44, v44, s39, -v104\n"
    "v_fma_f32 v45, v45, s39, -v104\n"
    "v_fma_f32 v46, v46, s39, -v104\n"
    "v_fma_f32 v47, v47, s39, -v104\n"
    "v_exp_f32 v40, v40\n"
    "v_exp_f32 v41, v41\n"
    "v_exp_f32 v42, v42\n"
    "v_exp_f32 v43, v43\n"
    "v_exp_f32 v44, v44\n"
    "v_exp_f32 v45, v45\n"
    "v_exp_f32 v46, v46\n"
    "v_exp_f32 v47, v47\n"
    "v_add_f32 v126, v40, v41\n"
    "v_add_f32 v126, v126, v42\n"
    "v_add_f32 v126, v126, v43\n"
    "v_add_f32 v126, v126, v44\n"
    "v_add_f32 v126, v126, v45\n"
    "v_add_f32 v126, v126, v46\n"
    "v_add_f32 v126, v126, v47\n"
    "v_add_f32 v108, v108, v126\n"
    "v_cvt_pk_bf16_f32 v40, v40, v41\n"
    "v_cvt_pk_bf16_f32 v41, v42, v43\n"
    "v_cvt_pk_bf16_f32 v42, v44, v45\n"
    "v_cvt_pk_bf16_f32 v43, v46, v47\n"
    "v_mul_f32 v140, v140, v127\n"
    "v_mul_f32 v141, v141, v127\n"
    "v_mul_f32 v142, v142, v127\n"
    "v_mul_f32 v143, v143, v127\n"
    "v_mul_f32 v156, v156, v127\n"
    "v_mul_f32 v157, v157, v127\n"
    "v_mul_f32 v158, v158, v127\n"
    "v_mul_f32 v159, v159, v127\n"
    "v_mul_f32 v172, v172, v127\n"
    "v_mul_f32 v173, v173, v127\n"
    "v_mul_f32 v174, v174, v127\n"
    "v_mul_f32 v175, v175, v127\n"
    "v_mul_f32 v188, v188, v127\n"
    "v_mul_f32 v189, v189, v127\n"
    "v_mul_f32 v190, v190, v127\n"
    "v_mul_f32 v191, v191, v127\n"
    "v_mul_f32 v204, v204, v127\n"
    "v_mul_f32 v205, v205, v127\n"
    "v_mul_f32 v206, v206, v127\n"
    "v_mul_f32 v207, v207, v127\n"
    "v_mul_f32 v220, v220, v127\n"
    "v_mul_f32 v221, v221, v127\n"
    "v_mul_f32 v222, v222, v127\n"
    "v_mul_f32 v223, v223, v127\n"
    "v_mul_f32 v236, v236, v127\n"
    "v_mul_f32 v237, v237, v127\n"
    "v_mul_f32 v238, v238, v127\n"
    "v_mul_f32 v239, v239, v127\n"
    "v_mul_f32 v252, v252, v127\n"
    "v_mul_f32 v253, v253, v127\n"
    "v_mul_f32 v254, v254, v127\n"
    "v_mul_f32 v255, v255, v127\n"
    "v_accvgpr_read_b32 v124, a140\n"
    "v_mul_f32 v124, v124, v127\n"
    "v_accvgpr_write_b32 a140, v124\n"
    "v_accvgpr_read_b32 v125, a141\n"
    "v_mul_f32 v125, v125, v127\n"
    "v_accvgpr_write_b32 a141, v125\n"
    "v_accvgpr_read_b32 v124, a142\n"
    "v_mul_f32 v124, v124, v127\n"
    "v_accvgpr_write_b32 a142, v124\n"
    "v_accvgpr_read_b32 v125, a143\n"
    "v_mul_f32 v125, v125, v127\n"
    "v_accvgpr_write_b32 a143, v125\n"
    "v_accvgpr_read_b32 v124, a156\n"
    "v_mul_f32 v124, v124, v127\n"
    "v_accvgpr_write_b32 a156, v124\n"
    "v_accvgpr_read_b32 v125, a157\n"
    "v_mul_f32 v125, v125, v127\n"
    "v_accvgpr_write_b32 a157, v125\n"
    "v_accvgpr_read_b32 v124, a158\n"
    "v_mul_f32 v124, v124, v127\n"
    "v_accvgpr_write_b32 a158, v124\n"
    "v_accvgpr_read_b32 v125, a159\n"
    "v_mul_f32 v125, v125, v127\n"
    "v_accvgpr_write_b32 a159, v125\n"
    "v_accvgpr_read_b32 v124, a172\n"
    "v_mul_f32 v124, v124, v127\n"
    "v_accvgpr_write_b32 a172, v124\n"
    "v_accvgpr_read_b32 v125, a173\n"
    "v_mul_f32 v125, v125, v127\n"
    "v_accvgpr_write_b32 a173, v125\n"
    "v_accvgpr_read_b32 v124, a174\n"
    "v_mul_f32 v124, v124, v127\n"
    "v_accvgpr_write_b32 a174, v124\n"
    "v_accvgpr_read_b32 v125, a175\n"
    "v_mul_f32 v125, v125, v127\n"
    "v_accvgpr_write_b32 a175, v125\n"
    "v_accvgpr_read_b32 v124, a188\n"
    "v_mul_f32 v124, v124, v127\n"
    "v_accvgpr_write_b32 a188, v124\n"
    "v_accvgpr_read_b32 v125, a189\n"
    "v_mul_f32 v125, v125, v127\n"
    "v_accvgpr_write_b32 a189, v125\n"
    "v_accvgpr_read_b32 v124, a190\n"
    "v_mul_f32 v124, v124, v127\n"
    "v_accvgpr_write_b32 a190, v124\n"
    "v_accvgpr_read_b32 v125, a191\n"
    "v_mul_f32 v125, v125, v127\n"
    "v_accvgpr_write_b32 a191, v125\n"
    "v_accvgpr_read_b32 v124, a204\n"
    "v_mul_f32 v124, v124, v127\n"
    "v_accvgpr_write_b32 a204, v124\n"
    "v_accvgpr_read_b32 v125, a205\n"
    "v_mul_f32 v125, v125, v127\n"
    "v_accvgpr_write_b32 a205, v125\n"
    "v_accvgpr_read_b32 v124, a206\n"
    "v_mul_f32 v124, v124, v127\n"
    "v_accvgpr_write_b32 a206, v124\n"
    "v_accvgpr_read_b32 v125, a207\n"
    "v_mul_f32 v125, v125, v127\n"
    "v_accvgpr_write_b32 a207, v125\n"
    "v_mul_f32 v76, v76, v127\n"
    "v_mul_f32 v77, v77, v127\n"
    "v_mul_f32 v78, v78, v127\n"
    "v_mul_f32 v79, v79, v127\n"
    "v_accvgpr_read_b32 v124, a236\n"
    "v_mul_f32 v124, v124, v127\n"
    "v_accvgpr_write_b32 a236, v124\n"
    "v_accvgpr_read_b32 v125, a237\n"
    "v_mul_f32 v125, v125, v127\n"
    "v_accvgpr_write_b32 a237, v125\n"
    "v_accvgpr_read_b32 v124, a238\n"
    "v_mul_f32 v124, v124, v127\n"
    "v_accvgpr_write_b32 a238, v124\n"
    "v_accvgpr_read_b32 v125, a239\n"
    "v_mul_f32 v125, v125, v127\n"
    "v_accvgpr_write_b32 a239, v125\n"
    "v_mul_f32 v92, v92, v127\n"
    "v_mul_f32 v93, v93, v127\n"
    "v_mul_f32 v94, v94, v127\n"
    "v_mul_f32 v95, v95, v127\n"
    "v_mov_b32 v36, 0\n"
    "v_mov_b32 v44, 0\n"
    "L_rare2R:\n"
    "s_add_u32 s74, s74, 4\n"
    "s_addc_u32 s75, s75, 0\n"
    "s_nop 3\n"
    "s_setpc_b64 s[74:75]\n"
    "L_done:\n"
    "L_end:\n"
    : "+v"(tid)
    : "s"(ka), "s"(blockIdx.x), "s"((unsigned)(uintptr_t)lds)
    : "v0", "v1", "v2", "v3", "v4", "v5", "v6", "v7", "v8", "v9", "v10", "v11", "v12", "v13", "v14", "v15", "v16", "v17", "v18", "v19", "v20", "v21", "v22", "v23", "v24", "v25", "v26", "v27", "v28", "v29", "v30", "v31", "v32", "v33", "v34", "v35", "v36", "v37", "v38", "v39", "v40", "v41", "v42", "v43", "v44", "v45", "v46", "v47", "v48", "v49", "v50", "v51", "v52", "v53", "v54", "v55", "v56", "v57", "v58", "v59", "v60", "v61", "v62", "v63", "v64", "v65", "v66", "v67", "v68", "v69", "v70", "v71", "v72", "v73", "v74", "v75", "v76", "v77", "v78", "v79", "v80", "v81", "v82", "v83", "v84", "v85", "v86", "v87", "v88", "v89", "v90", "v91", "v92", "v93", "v94", "v95", "v96", "v97", "v98", "v99", "v100", "v101", "v102", "v103", "v104", "v105", "v106", "v107", "v108", "v109", "v110", "v111", "v112", "v113", "v114", "v115", "v116", "v117", "v118", "v119", "v120", "v121", "v122", "v123", "v124", "v125", "v126", "v128", "v129", "v130", "v131", "v132", "v133", "v134", "v135", "v136", "v137", "v138", "v139", "v140", "v141", "v142", "v143", "v144", "v145", "v146", "v147", "v148", "v149", "v150", "v151", "v152", "v153", "v154", "v155", "v156", "v157", "v158", "v159", "v160", "v161", "v162", "v163", "v164", "v165", "v166", "v167", "v168", "v169", "v170", "v171", "v172", "v173", "v174", "v175", "v176", "v177", "v178", "v179", "v180", "v181", "v182", "v183", "v184", "v185", "v186", "v187", "v188", "v189", "v190", "v191", "v192", "v193", "v194", "v195", "v196", "v197", "v198", "v199", "v200", "v201", "v202", "v203", "v204", "v205", "v206", "v207", "v208", "v209", "v210", "v211", "v212", "v213", "v214", "v215", "v216", "v217", "v218", "v219", "v220", "v221", "v222", "v223", "v224", "v225", "v226", "v227", "v228", "v229", "v230", "v231", "v232", "v233", "v234", "v235", "v236", "v237", "v238", "v239", "v240", "v241", "v242", "v243", "v244", "v245", "v246", "v247", "v248", "v249", "v250", "v251", "v252", "v253", "v254", "v255", "a0", "a1", "a2", "a3", "a4", "a5", "a6", "a7", "a8", "a9", "a10", "a11", "a12", "a13", "a14", "a15", "a16", "a17", "a18", "a19", "a20", "a21", "a22", "a23", "a24", "a25", "a26", "a27", "a28", "a29", "a30", "a31", "a32", "a33", "a34", "a35", "a36", "a37", "a38", "a39", "a40", "a41", "a42", "a43", "a44", "a45", "a46", "a47", "a48", "a49", "a50", "a51", "a52", "a53", "a54", "a55", "a56", "a57", "a58", "a59", "a60", "a61", "a62", "a63", "a64", "a65", "a66", "a67", "a68", "a69", "a70", "a71", "a72", "a73", "a74", "a75", "a76", "a77", "a78", "a79", "a80", "a81", "a82", "a83", "a84", "a85", "a86", "a87", "a88", "a89", "a90", "a91", "a92", "a93", "a94", "a95", "a96", "a97", "a98", "a99", "a100", "a101", "a102", "a103", "a104", "a105", "a106", "a107", "a108", "a109", "a110", "a111", "a112", "a113", "a114", "a115", "a116", "a117", "a118", "a119", "a120", "a121", "a122", "a123", "a124", "a125", "a126", "a127", "a128", "a129", "a130", "a131", "a132", "a133", "a134", "a135", "a136", "a137", "a138", "a139", "a140", "a141", "a142", "a143", "a144", "a145", "a146", "a147", "a148", "a149", "a150", "a151", "a152", "a153", "a154", "a155", "a156", "a157", "a158", "a159", "a160", "a161", "a162", "a163", "a164", "a165", "a166", "a167", "a168", "a169", "a170", "a171", "a172", "a173", "a174", "a175", "a176", "a177", "a178", "a179", "a180", "a181", "a182", "a183", "a184", "a185", "a186", "a187", "a188", "a189", "a190", "a191", "a192", "a193", "a194", "a195", "a196", "a197", "a198", "a199", "a200", "a201", "a202", "a203", "a204", "a205", "a206", "a207", "a208", "a209", "a210", "a211", "a212", "a213", "a214", "a215", "a216", "a217", "a218", "a219", "a220", "a221", "a222", "a223", "a224", "a225", "a226", "a227", "a228", "a229", "a230", "a231", "a232", "a233", "a234", "a235", "a236", "a237", "a238", "a239", "a240", "a241", "a242", "a243", "a244", "a245", "a246", "a247", "a248", "a249", "a250", "a251", "a252", "a253", "a254", "a255", "s4", "s5", "s6", "s7", "s8", "s9", "s10", "s11", "s12", "s13", "s16", "s17", "s18", "s19", "s20", "s21", "s22", "s23", "s24", "s25", "s26", "s27", "s28", "s29", "s30", "s31", "s34", "s35", "s36", "s37", "s38", "s39", "s40", "s41", "s42", "s43", "s44", "s45", "s46", "s47", "s48", "s49", "s50", "s51", "s52", "s53", "s54", "s55", "s56", "s57", "s58", "s59", "s60", "s61", "s62", "s63", "s64", "s65", "s66", "s67", "s68", "s69", "s70", "s71", "s72", "s73", "s74", "s75", "s76", "s77", "s78", "s79", "s80", "s81", "s82", "s83", "s84", "s85", "s86", "s87", "s88", "s89", "s90", "s91", "s92", "s93", "s94", "s95", "s96", "s97", "s98", "s99", "vcc", "scc", "memory");
}

extern "C" void kernel_launch(void* const* d_in, const int* in_sizes, int n_in,
                              void* d_out, int out_size, void* d_ws, size_t ws_size,
                              hipStream_t stream) {
  gqa_attn_kernel<<<256, 256, 0, stream>>>(d_in[0], d_in[1], d_in[2], d_out, d_ws);
}
```
